```python
import torch

def _softmax(x, axis=-1):
    return torch.softmax(x, dim=axis)

def _gelu(x):
    return torch.nn.functional.gelu(x, approximate='tanh')

def _silu(x):
    return torch.nn.functional.silu(x)

def _sigmoid(x):
    return torch.sigmoid(x)

def _layernorm(x, axis=-1, eps=1e-05):
    m = x.mean(dim=axis, keepdim=True)
    v = x.var(dim=axis, keepdim=True, unbiased=False)
    return (x - m) / torch.sqrt(v + eps)

def _scatter_add(tgt, idx, src, axis=0):
    return tgt.clone().index_add_(axis, idx, src)

def _segment_sum(src, seg, n):
    out = torch.zeros((n,) + src.shape[1:], dtype=src.dtype)
    return out.index_add_(0, seg, src)

def _pad(x, pw):
    flat = tuple((v for pair in reversed(pw) for v in pair))
    return torch.nn.functional.pad(x, flat)
kernel_name = 'standalone__autoregressive_conv_wavernn__speech'

def setup_inputs():

    _w = lambda a: (lambda t: t.to(torch.bfloat16) if t.is_floating_point() else t)(torch.as_tensor(a))
    B, T, D, K = (2, 1000, 256, 31)
    return {'x': _w((lambda *s: torch.randn(*s, dtype=torch.bfloat16) * 0.05)(B, T, D)), 'w': _w((lambda *s: torch.randn(*s, dtype=torch.bfloat16) * 0.05)(K, D, 2 * D))}

def reference(x, w):
    B, T, D = x.shape
    K = w.shape[0]
    dil = 2
    pad = (K - 1) * dil
    xp = _pad(x, ((0, 0), (pad, 0), (0, 0)))
    h = torch.einsum('btd,de->bte', xp[:, 0:T, :], w[0])
    for i in range(1, K):
        h = h + torch.einsum('btd,de->bte', xp[:, i * dil:i * dil + T, :], w[i])
    f, g = (h[..., :D].clone(), h[..., D:].clone())
    return torch.tanh(f) * _sigmoid(g)
```

```cpp
#include <hip/hip_runtime.h>
#include <hip/hip_bf16.h>
typedef __attribute__((ext_vector_type(8))) short bf16x8;
typedef __attribute__((ext_vector_type(4))) float f32x4;
typedef unsigned short u16;

constexpr int B_ = 2, T_ = 1000, D_ = 256, K_ = 31;

__device__ __forceinline__ float bf2f(u16 v) { return __uint_as_float(((unsigned)v) << 16); }

__global__ __launch_bounds__(64) void conv_naive(const u16* __restrict__ x, const u16* __restrict__ w, __hip_bfloat16* __restrict__ out) {
  const int lane = threadIdx.x;
  const int nt = blockIdx.x & 15;
  const int mt = blockIdx.x >> 4;
  const int b = mt / 63, t0 = (mt % 63) * 16;
  const int c = lane & 15, q = lane >> 4;
  f32x4 af = {0,0,0,0}, ag = {0,0,0,0};
  for (int i = 0; i < K_; ++i) {
    int t = t0 + c + 2 * i - 60;
    bool ok = (t >= 0 && t < T_);
    for (int d0 = 0; d0 < D_; d0 += 32) {
      bf16x8 a = {0,0,0,0,0,0,0,0};
      if (ok) a = *(const bf16x8*)(x + ((size_t)(b * T_ + t)) * D_ + d0 + q * 8);
      bf16x8 bf, bg;
      #pragma unroll
      for (int j = 0; j < 8; ++j) {
        const u16* wr = w + ((size_t)(i * D_ + d0 + q * 8 + j)) * 512 + nt * 16 + c;
        bf[j] = (short)wr[0]; bg[j] = (short)wr[256];
      }
      af = __builtin_amdgcn_mfma_f32_16x16x32_bf16(a, bf, af, 0, 0, 0);
      ag = __builtin_amdgcn_mfma_f32_16x16x32_bf16(a, bg, ag, 0, 0, 0);
    }
  }
  #pragma unroll
  for (int j = 0; j < 4; ++j) {
    int t = t0 + q * 4 + j;
    if (t < T_) {
      float f = af[j], g = ag[j];
      float th = tanhf(f), sg = 1.f / (1.f + __expf(-g));
      out[((size_t)(b * T_ + t)) * D_ + nt * 16 + c] = __float2bfloat16(th * sg);
    }
  }
}

extern "C" void kernel_launch(void* const* d_in, const int* in_sizes, int n_in,
                              void* d_out, int out_size, void* d_ws, size_t ws_size,
                              hipStream_t stream) {
  conv_naive<<<126 * 16, 64, 0, stream>>>((const u16*)d_in[0], (const u16*)d_in[1], (__hip_bfloat16*)d_out);
}
```

```cpp
#include <hip/hip_runtime.h>
__global__ __launch_bounds__(512, 2) void conv_kernel(const void* x, const void* w, void* out, void* ws) {
  __shared__ __attribute__((aligned(256))) char smem[163840];
  asm volatile(
    ".Lstart%=:\n"
    "s_load_dwordx8 s[8:15], %[karg], 0\n"
    "s_mov_b32 s36, %[bid]\n"
    "v_readfirstlane_b32 s37, %[tid]\n"
    "v_and_b32 v1, 63, %[tid]\n"
    "s_and_b32 s47, s36, 3\n"
    "s_bfe_u32 s49, s36, 0x10006\n"
    "s_bfe_u32 s50, s36, 0x10005\n"
    "s_bfe_u32 s48, s36, 0x10003\n"
    "s_lshr_b32 s37, s37, 6\n"
    "s_mulk_i32 s50, 250\n"
    "s_lshl_b32 s53, s37, 2\n"
    "s_add_i32 s43, s50, -30\n"
    "s_lshl_b32 s43, s43, 1\n"
    "s_add_i32 s43, s43, s49\n"
    "s_lshl_b32 s43, s43, 2\n"
    "s_add_i32 s43, s43, s47\n"
    "s_lshl_b32 s43, s43, 7\n"
    "v_lshrrev_b32 v9, 3, v1\n"
    "v_and_b32 v10, 7, v1\n"
    "v_lshlrev_b32 v2, 10, v9\n"
    "v_lshl_add_u32 v2, v10, 4, v2\n"
    "v_add_u32 v2, s43, v2\n"
    "s_lshl_b32 s43, s53, 13\n"
    "v_add_u32 v11, s43, v2\n"
    "s_lshr_b32 s43, s53, 1\n"
    "s_lshl_b32 s43, s43, 5\n"
    "s_lshl_b32 s46, s53, 10\n"
    "s_add_u32 m0, s46, s43\n"
    "s_lshr_b32 s44, s36, 7\n"
    "s_mul_i32 s44, s44, 512000\n"
    "s_mov_b32 s18, 512000\n"
    "s_mov_b32 s19, 0x20000\n"
    "s_mov_b32 s22, 8126464\n"
    "s_mov_b32 s23, 0x20000\n"
    "s_and_b32 s38, s37, 3\n"
    "s_bfe_u32 s40, s36, 0x10002\n"
    "s_bfe_u32 s43, s36, 0x10004\n"
    "s_lshl_b32 s40, s40, 1\n"
    "s_add_i32 s40, s40, s43\n"
    "s_lshr_b32 s42, s36, 5\n"
    "s_lshl_b32 s45, s47, 7\n"
    "s_add_i32 s45, s45, s48\n"
    "s_lshl_b32 s45, s45, 2\n"
    "s_add_i32 s45, s45, s40\n"
    "s_lshl_b32 s45, s45, 7\n"
    "s_lshl_b32 s43, s42, 2\n"
    "s_add_i32 s51, s43, s40\n"
    "s_add_i32 s43, s43, s38\n"
    "s_lshr_b32 s52, s43, 1\n"
    "s_lshl_b32 s52, s52, 18\n"
    "s_and_b32 s43, s43, 1\n"
    "s_lshl_b32 s43, s43, 14\n"
    "s_add_i32 s43, s43, s52\n"
    "s_add_i32 s43, s43, s45\n"
    "v_and_b32 v4, 47, v1\n"
    "v_lshlrev_b32 v4, 10, v4\n"
    "v_bfe_u32 v3, v1, 4, 1\n"
    "v_lshl_add_u32 v4, v3, 22, v4\n"
    "v_add_u32 v4, s43, v4\n"
    "s_lshl_b32 s43, s51, 2\n"
    "s_add_i32 s43, s43, s38\n"
    "s_lshl_b32 s43, s43, 7\n"
    "v_mov_b32 v18, s43\n"
    "s_mov_b32 s26, 8683520\n"
    "s_mov_b32 s27, 0x20000\n"
    "s_and_b32 s43, s38, 1\n"
    "s_lshl_b32 s43, s43, 2\n"
    "v_and_b32 v3, 2, v9\n"
    "v_or_b32 v3, s43, v3\n"
    "v_xor_b32 v3, v3, v10\n"
    "v_lshlrev_b32 v3, 4, v3\n"
    "v_lshl_add_u32 v3, v9, 10, v3\n"
    "s_lshl_b32 s43, s38, 13\n"
    "s_add_i32 s43, s43, s45\n"
    "v_add_u32 v3, s43, v3\n"
    "s_waitcnt lgkmcnt(0)\n"
    "s_add_u32 s16, s8, s44\n"
    "s_addc_u32 s17, s9, 0\n"
    "s_and_b32 s17, s17, 0xffff\n"
    "buffer_load_dwordx4 v11, s[16:19], 0 offen lds\n"
    "s_mov_b32 s24, s14\n"
    "s_and_b32 s25, s15, 0xffff\n"
    "s_mov_b32 s20, s10\n"
    "s_and_b32 s21, s11, 0xffff\n"
    "buffer_load_dword v18, v18, s[24:27], 0 offen sc1\n"
    "s_cmp_ge_u32 s37, 4\n"
    "s_cbranch_scc1 .Lprod%=\n"
    "s_add_i32 s53, s53, 2\n"
    "s_lshl_b32 s43, s53, 13\n"
    "v_add_u32 v11, s43, v2\n"
    "s_lshr_b32 s43, s53, 1\n"
    "s_lshl_b32 s43, s43, 5\n"
    "s_lshl_b32 s46, s53, 10\n"
    "s_add_u32 m0, s46, s43\n"
    "s_nop 0\n"
    "buffer_load_dwordx4 v11, s[16:19], 0 offen lds\n"
    "s_and_b32 s53, s36, 24\n"
    "s_lshl_b32 s43, s38, 1\n"
    "s_add_i32 s53, s53, s43\n"
    "s_add_i32 s53, s53, 1\n"
    "s_lshl_b32 s43, s53, 13\n"
    "v_add_u32 v11, s43, v2\n"
    "s_lshr_b32 s43, s53, 1\n"
    "s_lshl_b32 s43, s43, 5\n"
    "s_lshl_b32 s46, s53, 10\n"
    "s_add_u32 m0, s46, s43\n"
    "s_nop 0\n"
    "buffer_load_dwordx4 v11, s[16:19], 0 offen lds\n"
    "s_mov_b32 exec_lo, 0xffff\n"
    "s_mov_b32 exec_hi, 0x0\n"
    "buffer_load_dword v12, v4, s[20:23], 0 offen\n"
    "s_mov_b64 exec, -1\n"
    "s_getpc_b64 s[32:33]\n"
    ".Lpc%=:\n"
    "s_and_b32 s33, s33, 0xffff\n"
    "s_mov_b32 s34, .Lend%=-.Lpc%=\n"
    "s_mov_b32 s35, 0x20000\n"
    "s_lshr_b32 s43, s36, 3\n"
    "s_lshl_b32 s43, s43, 2\n"
    "s_add_i32 s43, s43, s38\n"
    "s_lshl_b32 s43, s43, 7\n"
    "v_lshlrev_b32 v10, 14, v1\n"
    "v_add_u32 v10, s43, v10\n"
    "buffer_load_dword v3, v10, s[32:35], 0 offen\n"
    "s_branch .Lcons%=\n"
    ".Lprod%=:\n"
    "s_add_i32 s53, s53, 2\n"
    "s_lshl_b32 s43, s53, 13\n"
    "v_add_u32 v11, s43, v2\n"
    "s_lshr_b32 s43, s53, 1\n"
    "s_lshl_b32 s43, s43, 5\n"
    "s_lshl_b32 s46, s53, 10\n"
    "s_add_u32 m0, s46, s43\n"
    "s_nop 0\n"
    "buffer_load_dwordx4 v11, s[16:19], 0 offen lds\n"
    "s_lshl_b32 s46, s38, 10\n"
    "s_add_i32 s46, s46, 56576\n"
    "s_add_u32 m0, s46, 0\n"
    "s_mov_b32 s43, 0\n"
    "buffer_load_dwordx4 v3, s[20:23], s43 offen lds\n"
    "s_add_u32 m0, s46, 4096\n"
    "s_mov_b32 s43, 262144\n"
    "buffer_load_dwordx4 v3, s[20:23], s43 offen lds\n"
    "s_sleep 1\n"
    "s_and_b32 s53, s36, 24\n"
    "s_mul_i32 s43, s38, 6\n"
    "s_add_i32 s53, s53, s43\n"
    "s_add_i32 s53, s53, 7\n"
    "s_add_i32 s53, s53, 2\n"
    "s_and_b32 s53, s53, 31\n"
    "s_lshl_b32 s43, s53, 13\n"
    "v_add_u32 v11, s43, v2\n"
    "s_lshr_b32 s43, s53, 1\n"
    "s_lshl_b32 s43, s43, 5\n"
    "s_lshl_b32 s46, s53, 10\n"
    "s_add_u32 m0, s46, s43\n"
    "s_nop 0\n"
    "buffer_load_dwordx4 v11, s[16:19], 0 offen lds\n"
    "s_add_i32 s53, s53, 2\n"
    "s_and_b32 s53, s53, 31\n"
    "s_lshl_b32 s43, s53, 13\n"
    "v_add_u32 v11, s43, v2\n"
    "s_lshr_b32 s43, s53, 1\n"
    "s_lshl_b32 s43, s43, 5\n"
    "s_lshl_b32 s46, s53, 10\n"
    "s_add_u32 m0, s46, s43\n"
    "s_nop 0\n"
    "buffer_load_dwordx4 v11, s[16:19], 0 offen lds\n"
    "s_add_i32 s53, s53, 2\n"
    "s_and_b32 s53, s53, 31\n"
    "s_lshl_b32 s43, s53, 13\n"
    "v_add_u32 v11, s43, v2\n"
    "s_lshr_b32 s43, s53, 1\n"
    "s_lshl_b32 s43, s43, 5\n"
    "s_lshl_b32 s46, s53, 10\n"
    "s_add_u32 m0, s46, s43\n"
    "s_nop 0\n"
    "buffer_load_dwordx4 v11, s[16:19], 0 offen lds\n"
    "s_add_i32 s53, s38, 32\n"
    "s_lshl_b32 s43, s53, 13\n"
    "v_add_u32 v11, s43, v2\n"
    "s_lshr_b32 s43, s53, 1\n"
    "s_lshl_b32 s43, s43, 5\n"
    "s_lshl_b32 s46, s53, 10\n"
    "s_add_u32 m0, s46, s43\n"
    "s_nop 0\n"
    "buffer_load_dwordx4 v11, s[16:19], 0 offen lds\n"
    "s_lshl_b32 s46, s38, 10\n"
    "s_add_i32 s46, s46, 56576\n"
    "s_lshl_b32 s39, s47, 1\n"
    "s_add_i32 s39, s39, s48\n"
    "s_lshl_b32 s41, s39, 12\n"
    "s_lshl_b32 s43, s51, 8\n"
    "s_add_i32 s43, s43, s38\n"
    "s_lshl_b32 s43, s43, 10\n"
    "s_add_i32 s43, s43, 294912\n"
    "v_lshlrev_b32 v16, 4, v1\n"
    "v_add_u32 v8, s43, v16\n"
    "s_mul_i32 s43, s38, 6720\n"
    "s_add_i32 s43, s43, 97536\n"
    "v_lshrrev_b32 v10, 4, v1\n"
    "v_lshl_add_u32 v16, v10, 4, v16\n"
    "v_add_u32 v16, s43, v16\n"
    "s_lshl_b32 s43, s38, 2\n"
    "v_add_u32 v9, s43, v10\n"
    "v_bfe_u32 v14, v1, 2, 2\n"
    "v_mul_u32_u24 v14, 6720, v14\n"
    "v_lshl_add_u32 v14, v9, 4, v14\n"
    "v_and_b32 v10, 3, v1\n"
    "v_mul_u32_u24 v10, 272, v10\n"
    "s_lshr_b32 s52, s37, 2\n"
    "s_mul_i32 s43, s52, 1088\n"
    "s_add_i32 s43, s43, 97536\n"
    "v_add3_u32 v14, v14, v10, s43\n"
    "s_mov_b32 s28, s12\n"
    "s_and_b32 s29, s13, 0xffff\n"
    "s_mov_b32 s30, 1024000\n"
    "s_mov_b32 s31, 0x20000\n"
    "s_lshr_b32 s44, s36, 7\n"
    "s_mul_i32 s44, s44, 512000\n"
    "s_lshl_b32 s43, s50, 1\n"
    "s_add_i32 s43, s43, s49\n"
    "s_lshl_b32 s43, s43, 2\n"
    "s_add_i32 s43, s43, s40\n"
    "s_lshl_b32 s43, s43, 7\n"
    "s_add_i32 s43, s43, s44\n"
    "s_lshl_b32 s44, s39, 1\n"
    "s_add_i32 s44, s44, s52\n"
    "v_lshlrev_b32 v9, 4, v9\n"
    "v_add_u32 v9, s44, v9\n"
    "v_and_b32 v10, 15, v1\n"
    "v_lshlrev_b32 v15, 10, v9\n"
    "v_lshl_add_u32 v15, v10, 3, v15\n"
    "v_add_u32 v15, s43, v15\n"
    "v_cmp_gt_u32 vcc, 250, v9\n"
    "v_mov_b32 v10, -1\n"
    "s_nop 0\n"
    "v_cndmask_b32 v15, v10, v15, vcc\n"
    "s_mov_b32 s43, 0x4038aa3b\n"
    "s_mov_b32 s44, 0xbfb8aa3b\n"
    "s_cmp_eq_u32 s48, 0\n"
    "s_cselect_b32 s56, s43, s44\n"
    "s_cselect_b32 s57, s44, s43\n"
    "s_cselect_b32 s58, -2.0, 1.0\n"
    "s_cselect_b32 s59, 1.0, -2.0\n"
    "s_cselect_b32 s43, 1.0, 0\n"
    "s_cselect_b32 s44, 0, 1.0\n"
    "v_mov_b32 v17, s43\n"
    "v_mov_b32 v81, s44\n"
    "s_lshl_b32 s43, s39, 15\n"
    "v_add_u32 v8, s43, v8\n"
    "s_lshl_b32 s43, s51, 2\n"
    "s_add_i32 s43, s43, s38\n"
    "s_lshl_b32 s43, s43, 7\n"
    "v_mov_b32 v89, s43\n"
    "s_lshl_b32 s43, s43, 3\n"
    "s_add_i32 s43, s43, 32768\n"
    "s_lshl_b32 s44, s39, 7\n"
    "s_add_i32 s43, s43, s44\n"
    "v_mov_b32 v90, s43\n"
    "v_and_b32 v12, 7, v1\n"
    "v_max_u32 v12, 1, v12\n"
    "v_lshlrev_b32 v12, 7, v12\n"
    "v_xor_b32 v12, v12, v90\n"
    "s_mov_b32 s8, 0xffff0000\n"
    "v_mov_b32 v20, -1\n"
    "v_mov_b32 v21, -1\n"
    "v_mov_b32 v22, -1\n"
    "v_mov_b32 v23, -1\n"
    "v_and_b32 v9, 15, v1\n"
    "v_lshrrev_b32 v10, 4, v1\n"
    "v_mul_u32_u24 v92, 2080, v9\n"
    "v_lshl_add_u32 v92, v10, 4, v92\n"
    "s_lshl_b32 s43, s38, 9\n"
    "v_add_u32 v92, s43, v92\n"
    "s_cmp_eq_u32 s38, 3\n"
    "s_cselect_b32 s44, 32, 0\n"
    "v_add_u32 v13, s44, v92\n"
    "v_mul_u32_u24 v93, 1056, v9\n"
    "v_lshl_add_u32 v93, v10, 4, v93\n"
    "s_lshl_b32 s43, s38, 8\n"
    "s_add_i32 s43, s43, 37440\n"
    "v_add_u32 v93, s43, v93\n"
    "v_lshrrev_b32 v9, 4, v1\n"
    "s_and_b32 s43, s38, 1\n"
    "s_lshl_b32 s43, s43, 2\n"
    "v_add_u32 v9, s43, v9\n"
    "v_and_b32 v10, 15, v1\n"
    "v_lshlrev_b32 v10, 2, v10\n"
    "s_lshr_b32 s43, s38, 1\n"
    "s_add_i32 s43, s43, 16\n"
    "s_mul_i32 s44, s43, 2080\n"
    "v_lshl_add_u32 v94, v9, 8, v10\n"
    "v_add_u32 v94, s44, v94\n"
    "s_mul_i32 s44, s43, 1056\n"
    "s_add_i32 s44, s44, 37440\n"
    "v_lshl_add_u32 v96, v9, 7, v10\n"
    "v_add_u32 v96, s44, v96\n"
    "v_cmp_eq_u32 vcc, 7, v9\n"
    "v_mov_b32 v9, 32\n"
    "v_xor_b32 v97, 8, v96\n"
    "v_cndmask_b32 v95, 0, v9, vcc\n"
    "v_add_u32 v95, v95, v94\n"
    "s_add_u32 m0, s46, 8192\n"
    "s_mov_b32 s43, 524288\n"
    "buffer_load_dwordx4 v3, s[20:23], s43 offen lds\n"
    "s_add_u32 m0, s46, 12288\n"
    "s_mov_b32 s43, 786432\n"
    "buffer_load_dwordx4 v3, s[20:23], s43 offen lds\n"
    "s_add_u32 m0, s46, 16384\n"
    "s_mov_b32 s43, 1048576\n"
    "buffer_load_dwordx4 v3, s[20:23], s43 offen lds\n"
    "s_add_u32 m0, s46, 20480\n"
    "s_mov_b32 s43, 1310720\n"
    "buffer_load_dwordx4 v3, s[20:23], s43 offen lds\n"
    "s_add_u32 m0, s46, 24576\n"
    "s_mov_b32 s43, 1572864\n"
    "buffer_load_dwordx4 v3, s[20:23], s43 offen lds\n"
    "s_waitcnt vmcnt(9)\n"
    "s_barrier\n"
    "s_add_u32 m0, s46, 28672\n"
    "s_mov_b32 s43, 1835008\n"
    "buffer_load_dwordx4 v3, s[20:23], s43 offen lds\n"
    "s_add_u32 m0, s46, 32768\n"
    "s_mov_b32 s43, 2097152\n"
    "buffer_load_dwordx4 v3, s[20:23], s43 offen lds\n"
    "s_add_u32 m0, s46, 36864\n"
    "s_mov_b32 s43, 2359296\n"
    "buffer_load_dwordx4 v3, s[20:23], s43 offen lds\n"
    "s_add_u32 m0, s46, 40960\n"
    "s_mov_b32 s43, 2621440\n"
    "buffer_load_dwordx4 v3, s[20:23], s43 offen lds\n"
    "s_add_u32 m0, s46, 45056\n"
    "s_mov_b32 s43, 2883584\n"
    "buffer_load_dwordx4 v3, s[20:23], s43 offen lds\n"
    "s_add_u32 m0, s46, 49152\n"
    "s_mov_b32 s43, 3145728\n"
    "buffer_load_dwordx4 v3, s[20:23], s43 offen lds\n"
    "s_add_u32 m0, s46, 53248\n"
    "s_mov_b32 s43, 3407872\n"
    "buffer_load_dwordx4 v3, s[20:23], s43 offen lds\n"
    "s_add_u32 m0, s46, 57344\n"
    "s_mov_b32 s43, 3670016\n"
    "buffer_load_dwordx4 v3, s[20:23], s43 offen lds\n"
    "s_add_u32 m0, s46, 61440\n"
    "s_mov_b32 s43, 3932160\n"
    "buffer_load_dwordx4 v3, s[20:23], s43 offen lds\n"
    "s_mov_b32 exec_lo, 0xffff0000\n"
    "s_mov_b32 exec_hi, 0xffff\n"
    "buffer_load_dword v24, v4, s[20:23], 0 offen\n"
    "s_mov_b64 exec, -1\n"
    "s_waitcnt vmcnt(10)\n"
    "s_barrier\n"
    "ds_read_b128 v[100:103], v92 offset:0\n"
    "ds_read_b128 v[104:107], v92 offset:128\n"
    "ds_read_b128 v[108:111], v92 offset:256\n"
    "ds_read_b128 v[112:115], v92 offset:384\n"
    "ds_read_b128 v[116:119], v13 offset:512\n"
    "ds_read_b32 v124, v94\n"
    "ds_read_b32 v125, v94 offset:128\n"
    "ds_read_b32 v126, v95 offset:256\n"
    "s_waitcnt lgkmcnt(0)\n"
    "v_lshlrev_b32 v140, 16, v100\n"
    "v_and_b32 v141, s8, v100\n"
    "v_lshlrev_b32 v142, 16, v104\n"
    "v_and_b32 v143, s8, v104\n"
    "v_lshlrev_b32 v144, 16, v108\n"
    "v_and_b32 v145, s8, v108\n"
    "v_lshlrev_b32 v146, 16, v112\n"
    "v_and_b32 v147, s8, v112\n"
    "v_lshlrev_b32 v148, 16, v116\n"
    "v_and_b32 v149, s8, v116\n"
    "v_sub_f32 v140, v142, v140\n"
    "v_sub_f32 v141, v143, v141\n"
    "v_sub_f32 v142, v142, v144\n"
    "v_sub_f32 v143, v143, v145\n"
    "v_sub_f32 v144, v146, v144\n"
    "v_sub_f32 v145, v147, v145\n"
    "v_sub_f32 v146, v146, v148\n"
    "v_sub_f32 v147, v147, v149\n"
    "v_cvt_pk_bf16_f32 v154, v140, v141\n"
    "v_cvt_pk_bf16_f32 v156, v142, v143\n"
    "v_cvt_pk_bf16_f32 v162, v144, v145\n"
    "v_cvt_pk_bf16_f32 v164, v146, v147\n"
    "v_lshlrev_b32 v140, 16, v101\n"
    "v_and_b32 v141, s8, v101\n"
    "v_lshlrev_b32 v142, 16, v105\n"
    "v_and_b32 v143, s8, v105\n"
    "v_lshlrev_b32 v144, 16, v109\n"
    "v_and_b32 v145, s8, v109\n"
    "v_lshlrev_b32 v146, 16, v113\n"
    "v_and_b32 v147, s8, v113\n"
    "v_lshlrev_b32 v148, 16, v117\n"
    "v_and_b32 v149, s8, v117\n"
    "v_sub_f32 v140, v142, v140\n"
    "v_sub_f32 v141, v143, v141\n"
    "v_sub_f32 v142, v142, v144\n"
    "v_sub_f32 v143, v143, v145\n"
    "v_sub_f32 v144, v146, v144\n"
    "v_sub_f32 v145, v147, v145\n"
    "v_sub_f32 v146, v146, v148\n"
    "v_sub_f32 v147, v147, v149\n"
    "v_cvt_pk_bf16_f32 v155, v140, v141\n"
    "v_cvt_pk_bf16_f32 v157, v142, v143\n"
    "v_cvt_pk_bf16_f32 v163, v144, v145\n"
    "v_cvt_pk_bf16_f32 v165, v146, v147\n"
    "v_lshlrev_b32 v140, 16, v102\n"
    "v_and_b32 v141, s8, v102\n"
    "v_lshlrev_b32 v142, 16, v106\n"
    "v_and_b32 v143, s8, v106\n"
    "v_lshlrev_b32 v144, 16, v110\n"
    "v_and_b32 v145, s8, v110\n"
    "v_lshlrev_b32 v146, 16, v114\n"
    "v_and_b32 v147, s8, v114\n"
    "v_lshlrev_b32 v148, 16, v118\n"
    "v_and_b32 v149, s8, v118\n"
    "v_sub_f32 v140, v142, v140\n"
    "v_sub_f32 v141, v143, v141\n"
    "v_sub_f32 v142, v142, v144\n"
    "v_sub_f32 v143, v143, v145\n"
    "v_sub_f32 v144, v146, v144\n"
    "v_sub_f32 v145, v147, v145\n"
    "v_sub_f32 v146, v146, v148\n"
    "v_sub_f32 v147, v147, v149\n"
    "v_cvt_pk_bf16_f32 v152, v140, v141\n"
    "v_cvt_pk_bf16_f32 v158, v142, v143\n"
    "v_cvt_pk_bf16_f32 v160, v144, v145\n"
    "v_cvt_pk_bf16_f32 v166, v146, v147\n"
    "v_lshlrev_b32 v140, 16, v103\n"
    "v_and_b32 v141, s8, v103\n"
    "v_lshlrev_b32 v142, 16, v107\n"
    "v_and_b32 v143, s8, v107\n"
    "v_lshlrev_b32 v144, 16, v111\n"
    "v_and_b32 v145, s8, v111\n"
    "v_lshlrev_b32 v146, 16, v115\n"
    "v_and_b32 v147, s8, v115\n"
    "v_lshlrev_b32 v148, 16, v119\n"
    "v_and_b32 v149, s8, v119\n"
    "v_sub_f32 v140, v142, v140\n"
    "v_sub_f32 v141, v143, v141\n"
    "v_sub_f32 v142, v142, v144\n"
    "v_sub_f32 v143, v143, v145\n"
    "v_sub_f32 v144, v146, v144\n"
    "v_sub_f32 v145, v147, v145\n"
    "v_sub_f32 v146, v146, v148\n"
    "v_sub_f32 v147, v147, v149\n"
    "v_cvt_pk_bf16_f32 v153, v140, v141\n"
    "v_cvt_pk_bf16_f32 v159, v142, v143\n"
    "v_cvt_pk_bf16_f32 v161, v144, v145\n"
    "v_cvt_pk_bf16_f32 v167, v146, v147\n"
    "s_waitcnt lgkmcnt(0)\n"
    "v_lshlrev_b32 v140, 16, v124\n"
    "v_and_b32 v141, s8, v124\n"
    "v_lshlrev_b32 v142, 16, v125\n"
    "v_and_b32 v143, s8, v125\n"
    "v_lshlrev_b32 v144, 16, v126\n"
    "v_and_b32 v145, s8, v126\n"
    "v_sub_f32 v140, v142, v140\n"
    "v_sub_f32 v141, v143, v141\n"
    "v_sub_f32 v144, v142, v144\n"
    "v_sub_f32 v145, v143, v145\n"
    "v_cvt_pk_bf16_f32 v124, v140, v141\n"
    "v_cvt_pk_bf16_f32 v126, v144, v145\n"
    "s_waitcnt vmcnt(1)\n"
    "ds_write_b128 v93, v[152:155] offset:0\n"
    "ds_write_b128 v93, v[156:159] offset:64\n"
    "ds_write_b128 v93, v[160:163] offset:128\n"
    "ds_write_b128 v93, v[164:167] offset:192\n"
    "ds_write_b32 v97, v124\n"
    "ds_write_b32 v96, v126 offset:64\n"
    "s_waitcnt lgkmcnt(0)\n"
    "s_waitcnt vmcnt(2)\n"
    "s_barrier\n"
    "s_mov_b64 exec, 0\n"
    "s_branch .Lpad0%=\n"
    ".Lpad0r%=:\n"
    "s_mov_b64 exec, -1\n"
    "s_add_u32 m0, s46, 65536\n"
    "s_mov_b32 s43, 4194304\n"
    "buffer_load_dwordx4 v3, s[20:23], s43 offen lds\n"
    "s_add_u32 m0, s46, 69632\n"
    "s_mov_b32 s43, 4456448\n"
    "buffer_load_dwordx4 v3, s[20:23], s43 offen lds\n"
    "s_add_u32 m0, s46, 73728\n"
    "s_mov_b32 s43, 4718592\n"
    "buffer_load_dwordx4 v3, s[20:23], s43 offen lds\n"
    "s_add_u32 m0, s46, 77824\n"
    "s_mov_b32 s43, 4980736\n"
    "buffer_load_dwordx4 v3, s[20:23], s43 offen lds\n"
    "s_add_u32 m0, s46, 81920\n"
    "s_mov_b32 s43, 5242880\n"
    "buffer_load_dwordx4 v3, s[20:23], s43 offen lds\n"
    "s_add_u32 m0, s46, 86016\n"
    "s_mov_b32 s43, 5505024\n"
    "buffer_load_dwordx4 v3, s[20:23], s43 offen lds\n"
    "s_add_u32 m0, s46, 90112\n"
    "s_mov_b32 s43, 5767168\n"
    "buffer_load_dwordx4 v3, s[20:23], s43 offen lds\n"
    "s_add_u32 m0, s46, 94208\n"
    "s_mov_b32 s43, 6029312\n"
    "buffer_load_dwordx4 v3, s[20:23], s43 offen lds\n"
    "s_mov_b32 exec_lo, 0x0\n"
    "s_mov_b32 exec_hi, 0xffff0000\n"
    "buffer_load_dword v24, v4, s[20:23], 0 offen\n"
    "s_mov_b64 exec, -1\n"
    "s_waitcnt vmcnt(6)\n"
    "s_barrier\n"
    "s_add_u32 m0, s46, 98304\n"
    "s_mov_b32 s43, 6291456\n"
    "buffer_load_dwordx4 v3, s[20:23], s43 offen lds\n"
    "s_add_u32 m0, s46, 102400\n"
    "s_mov_b32 s43, 6553600\n"
    "buffer_load_dwordx4 v3, s[20:23], s43 offen lds\n"
    "s_add_u32 m0, s46, 0\n"
    "s_mov_b32 s43, 6815744\n"
    "buffer_load_dwordx4 v3, s[20:23], s43 offen lds\n"
    "s_add_u32 m0, s46, 4096\n"
    "s_mov_b32 s43, 7077888\n"
    "buffer_load_dwordx4 v3, s[20:23], s43 offen lds\n"
    "s_add_u32 m0, s46, 8192\n"
    "s_mov_b32 s43, 7340032\n"
    "buffer_load_dwordx4 v3, s[20:23], s43 offen lds\n"
    "s_add_u32 m0, s46, 12288\n"
    "s_mov_b32 s43, 7602176\n"
    "buffer_load_dwordx4 v3, s[20:23], s43 offen lds\n"
    "s_movk_i32 s43, 16384\n"
    "buffer_store_dwordx4 v[20:23], v8, s[24:27], s43 offen sc1\n"
    "s_and_b32 s43, s39, 6\n"
    "s_cmp_lg_u32 s43, 2\n"
    "s_cbranch_scc1 .Lpad4s%=\n"
    "s_mov_b64 exec, 0\n"
    "s_branch .Lpad4%=\n"
    ".Lpad4r%=:\n"
    "s_mov_b64 exec, -1\n"
    ".Lpad4s%=:\n"
    "s_and_b32 s43, s39, 6\n"
    "s_cmp_lg_u32 s43, 4\n"
    "s_cbranch_scc1 .Lpad8s%=\n"
    "s_mov_b64 exec, 0\n"
    "s_branch .Lpad8%=\n"
    ".Lpad8r%=:\n"
    "s_mov_b64 exec, -1\n"
    ".Lpad8s%=:\n"
    "s_and_b32 s43, s39, 6\n"
    "s_cmp_lg_u32 s43, 6\n"
    "s_cbranch_scc1 .Lpad13s%=\n"
    "s_mov_b64 exec, 0\n"
    "s_branch .Lpad13%=\n"
    ".Lpad13r%=:\n"
    "s_mov_b64 exec, -1\n"
    ".Lpad13s%=:\n"
    "s_and_b32 s43, s39, 6\n"
    "s_cmp_lg_u32 s43, 4\n"
    "s_cbranch_scc1 .Lpad9s%=\n"
    "s_mov_b64 exec, 0\n"
    "s_branch .Lpad9%=\n"
    ".Lpad9r%=:\n"
    "s_mov_b64 exec, -1\n"
    ".Lpad9s%=:\n"
    "s_waitcnt vmcnt(9)\n"
    "s_barrier\n"
    "s_memrealtime s[10:11]\n"
    "s_memtime s[12:13]\n"
    "s_add_u32 m0, s46, 16384\n"
    "s_mov_b32 s43, 7864320\n"
    "buffer_load_dwordx4 v3, s[20:23], s43 offen lds\n"
    "s_add_u32 m0, s46, 20480\n"
    "s_mov_b32 s43, 32768\n"
    "buffer_load_dwordx4 v3, s[20:23], s43 offen lds\n"
    "s_add_u32 m0, s46, 24576\n"
    "s_mov_b32 s43, 294912\n"
    "buffer_load_dwordx4 v3, s[20:23], s43 offen lds\n"
    "s_add_u32 m0, s46, 28672\n"
    "s_mov_b32 s43, 557056\n"
    "buffer_load_dwordx4 v3, s[20:23], s43 offen lds\n"
    "s_add_u32 m0, s46, 32768\n"
    "s_mov_b32 s43, 819200\n"
    "buffer_load_dwordx4 v3, s[20:23], s43 offen lds\n"
    "s_add_u32 m0, s46, 36864\n"
    "s_mov_b32 s43, 1081344\n"
    "buffer_load_dwordx4 v3, s[20:23], s43 offen lds\n"
    "s_add_u32 m0, s46, 40960\n"
    "s_mov_b32 s43, 1343488\n"
    "buffer_load_dwordx4 v3, s[20:23], s43 offen lds\n"
    "s_add_u32 m0, s46, 45056\n"
    "s_mov_b32 s43, 1605632\n"
    "buffer_load_dwordx4 v3, s[20:23], s43 offen lds\n"
    "s_movk_i32 s43, 20480\n"
    "buffer_store_dwordx4 v[20:23], v8, s[24:27], s43 offen sc1\n"
    "s_movk_i32 s43, 8192\n"
    "buffer_store_dwordx4 v[20:23], v8, s[24:27], s43 offen sc1\n"
    "s_waitcnt vmcnt(14)\n"
    "s_barrier\n"
    "s_add_u32 m0, s46, 49152\n"
    "s_mov_b32 s43, 1867776\n"
    "buffer_load_dwordx4 v3, s[20:23], s43 offen lds\n"
    "s_add_u32 m0, s46, 53248\n"
    "s_mov_b32 s43, 2129920\n"
    "buffer_load_dwordx4 v3, s[20:23], s43 offen lds\n"
    "s_add_u32 m0, s46, 57344\n"
    "s_mov_b32 s43, 2392064\n"
    "buffer_load_dwordx4 v3, s[20:23], s43 offen lds\n"
    "s_add_u32 m0, s46, 61440\n"
    "s_mov_b32 s43, 2654208\n"
    "buffer_load_dwordx4 v3, s[20:23], s43 offen lds\n"
    "s_add_u32 m0, s46, 65536\n"
    "s_mov_b32 s43, 2916352\n"
    "buffer_load_dwordx4 v3, s[20:23], s43 offen lds\n"
    "s_add_u32 m0, s46, 69632\n"
    "s_mov_b32 s43, 3178496\n"
    "buffer_load_dwordx4 v3, s[20:23], s43 offen lds\n"
    "s_add_u32 m0, s46, 73728\n"
    "s_mov_b32 s43, 3440640\n"
    "buffer_load_dwordx4 v3, s[20:23], s43 offen lds\n"
    "s_add_u32 m0, s46, 77824\n"
    "s_mov_b32 s43, 3702784\n"
    "buffer_load_dwordx4 v3, s[20:23], s43 offen lds\n"
    "s_movk_i32 s43, 12288\n"
    "buffer_store_dwordx4 v[20:23], v8, s[24:27], s43 offen sc1\n"
    "s_movk_i32 s43, 4096\n"
    "buffer_store_dwordx4 v[20:23], v8, s[24:27], s43 offen sc1\n"
    "s_and_b32 s43, s39, 6\n"
    "s_cmp_lg_u32 s43, 2\n"
    "s_cbranch_scc1 .Lpad5s%=\n"
    "s_mov_b64 exec, 0\n"
    "s_branch .Lpad5%=\n"
    ".Lpad5r%=:\n"
    "s_mov_b64 exec, -1\n"
    ".Lpad5s%=:\n"
    "s_waitcnt vmcnt(19)\n"
    "s_barrier\n"
    "s_add_u32 m0, s46, 81920\n"
    "s_mov_b32 s43, 3964928\n"
    "buffer_load_dwordx4 v3, s[20:23], s43 offen lds\n"
    "s_add_u32 m0, s46, 86016\n"
    "s_mov_b32 s43, 4227072\n"
    "buffer_load_dwordx4 v3, s[20:23], s43 offen lds\n"
    "s_add_u32 m0, s46, 90112\n"
    "s_mov_b32 s43, 4489216\n"
    "buffer_load_dwordx4 v3, s[20:23], s43 offen lds\n"
    "s_add_u32 m0, s46, 94208\n"
    "s_mov_b32 s43, 4751360\n"
    "buffer_load_dwordx4 v3, s[20:23], s43 offen lds\n"
    "s_add_u32 m0, s46, 98304\n"
    "s_mov_b32 s43, 5013504\n"
    "buffer_load_dwordx4 v3, s[20:23], s43 offen lds\n"
    "s_add_u32 m0, s46, 102400\n"
    "s_mov_b32 s43, 5275648\n"
    "buffer_load_dwordx4 v3, s[20:23], s43 offen lds\n"
    "s_add_u32 m0, s46, 0\n"
    "s_mov_b32 s43, 5537792\n"
    "buffer_load_dwordx4 v3, s[20:23], s43 offen lds\n"
    "s_and_b32 s43, s39, 6\n"
    "s_cmp_lg_u32 s43, 0\n"
    "s_cbranch_scc1 .Lpad1s%=\n"
    "s_mov_b64 exec, 0\n"
    "s_branch .Lpad1%=\n"
    ".Lpad1r%=:\n"
    "s_mov_b64 exec, -1\n"
    ".Lpad1s%=:\n"
    "s_waitcnt vmcnt(15)\n"
    "s_barrier\n"
    "s_add_u32 m0, s46, 4096\n"
    "s_mov_b32 s43, 5799936\n"
    "buffer_load_dwordx4 v3, s[20:23], s43 offen lds\n"
    "s_add_u32 m0, s46, 8192\n"
    "s_mov_b32 s43, 6062080\n"
    "buffer_load_dwordx4 v3, s[20:23], s43 offen lds\n"
    "s_add_u32 m0, s46, 12288\n"
    "s_mov_b32 s43, 6324224\n"
    "buffer_load_dwordx4 v3, s[20:23], s43 offen lds\n"
    "s_add_u32 m0, s46, 16384\n"
    "s_mov_b32 s43, 6586368\n"
    "buffer_load_dwordx4 v3, s[20:23], s43 offen lds\n"
    "s_waitcnt vmcnt(11)\n"
    "v_add_u32 v11, 1, v18\n"
    "s_mov_b64 exec, 1\n"
    "buffer_store_dword v11, v90, s[24:27], 0 offen sc1\n"
    "s_mov_b64 exec, -1\n"
    "s_and_b32 s43, s39, 6\n"
    "s_cmp_lg_u32 s43, 6\n"
    "s_cbranch_scc1 .Lpad14s%=\n"
    "s_mov_b64 exec, 0\n"
    "s_branch .Lpad14%=\n"
    ".Lpad14r%=:\n"
    "s_mov_b64 exec, -1\n"
    ".Lpad14s%=:\n"
    "s_waitcnt vmcnt(8)\n"
    "s_barrier\n"
    "s_add_u32 m0, s46, 20480\n"
    "s_mov_b32 s43, 6848512\n"
    "buffer_load_dwordx4 v3, s[20:23], s43 offen lds\n"
    "s_add_u32 m0, s46, 24576\n"
    "s_mov_b32 s43, 7110656\n"
    "buffer_load_dwordx4 v3, s[20:23], s43 offen lds\n"
    "s_add_u32 m0, s46, 28672\n"
    "s_mov_b32 s43, 7372800\n"
    "buffer_load_dwordx4 v3, s[20:23], s43 offen lds\n"
    "s_add_u32 m0, s46, 32768\n"
    "s_mov_b32 s43, 7634944\n"
    "buffer_load_dwordx4 v3, s[20:23], s43 offen lds\n"
    "s_add_u32 m0, s46, 36864\n"
    "s_mov_b32 s43, 7897088\n"
    "buffer_load_dwordx4 v3, s[20:23], s43 offen lds\n"
    "s_waitcnt vmcnt(6)\n"
    "s_barrier\n"
    "s_waitcnt vmcnt(0)\n"
    "s_barrier\n"
    "buffer_load_dword v10, v12, s[24:27], 0 offen sc1\n"
    "s_and_b32 s43, s39, 6\n"
    "s_cmp_lg_u32 s43, 4\n"
    "s_cbranch_scc1 .Lpad10s%=\n"
    "s_mov_b64 exec, 0\n"
    "s_branch .Lpad10%=\n"
    ".Lpad10r%=:\n"
    "s_mov_b64 exec, -1\n"
    ".Lpad10s%=:\n"
    "s_mov_b32 s52, 100000\n"
    "s_waitcnt vmcnt(0)\n"
    ".Lr%=:\n"
    "v_sub_u32 v10, v10, v11\n"
    "v_cmp_lt_u32 vcc, 1, v10\n"
    "s_cbranch_vccz .Lrok%=\n"
    "s_sleep 2\n"
    "buffer_load_dword v10, v12, s[24:27], 0 offen sc1\n"
    "s_waitcnt vmcnt(0)\n"
    "s_sub_u32 s52, s52, 1\n"
    "s_cbranch_scc0 .Lr%=\n"
    ".Lrok%=:\n"
    "v_add_u32 v84, 1, v11\n"
    "s_barrier\n"
    "s_memrealtime s[32:33]\n"
    "s_memtime s[34:35]\n"
    "s_mov_b64 exec, 1\n"
    "s_waitcnt vmcnt(0) lgkmcnt(0)\n"
    "v_mov_b32 v85, s32\n"
    "buffer_store_dwordx2 v[84:85], v90, s[24:27], 0 offen sc1\n"
    "s_mov_b64 exec, -1\n"
    "s_and_b32 s43, s39, 7\n"
    "s_cmp_lg_u32 s43, 1\n"
    "s_cbranch_scc1 .Lpad3s%=\n"
    "s_mov_b64 exec, 0\n"
    "s_branch .Lpad3%=\n"
    ".Lpad3r%=:\n"
    "s_mov_b64 exec, -1\n"
    ".Lpad3s%=:\n"
    "s_and_b32 s43, s39, 7\n"
    "s_cmp_lg_u32 s43, 3\n"
    "s_cbranch_scc1 .Lpad7s%=\n"
    "s_mov_b64 exec, 0\n"
    "s_branch .Lpad7%=\n"
    ".Lpad7r%=:\n"
    "s_mov_b64 exec, -1\n"
    ".Lpad7s%=:\n"
    "s_and_b32 s43, s39, 7\n"
    "s_cmp_lg_u32 s43, 5\n"
    "s_cbranch_scc1 .Lpad11s%=\n"
    "s_mov_b64 exec, 0\n"
    "s_branch .Lpad11%=\n"
    ".Lpad11r%=:\n"
    "s_mov_b64 exec, -1\n"
    ".Lpad11s%=:\n"
    "s_and_b32 s43, s39, 7\n"
    "s_cmp_lg_u32 s43, 7\n"
    "s_cbranch_scc1 .Lpad15s%=\n"
    "s_mov_b64 exec, 0\n"
    "s_branch .Lpad15%=\n"
    ".Lpad15r%=:\n"
    "s_mov_b64 exec, -1\n"
    ".Lpad15s%=:\n"
    "s_and_b32 s43, s39, 7\n"
    "s_cmp_lg_u32 s43, 7\n"
    "s_cbranch_scc1 .Lpad16s%=\n"
    "s_mov_b64 exec, 0\n"
    "s_branch .Lpad16%=\n"
    ".Lpad16r%=:\n"
    "s_mov_b64 exec, -1\n"
    ".Lpad16s%=:\n"
    "s_and_b32 s43, s39, 7\n"
    "s_cmp_lg_u32 s43, 2\n"
    "s_cbranch_scc1 .Lpad6s%=\n"
    "s_mov_b64 exec, 0\n"
    "s_branch .Lpad6%=\n"
    ".Lpad6r%=:\n"
    "s_mov_b64 exec, -1\n"
    ".Lpad6s%=:\n"
    "s_and_b32 s43, s39, 7\n"
    "s_cmp_lg_u32 s43, 5\n"
    "s_cbranch_scc1 .Lpad12s%=\n"
    "s_mov_b64 exec, 0\n"
    "s_branch .Lpad12%=\n"
    ".Lpad12r%=:\n"
    "s_mov_b64 exec, -1\n"
    ".Lpad12s%=:\n"
    "s_and_b32 s43, s39, 7\n"
    "s_cmp_lg_u32 s43, 0\n"
    "s_cbranch_scc1 .Lpad2s%=\n"
    "s_mov_b64 exec, 0\n"
    "s_branch .Lpad2%=\n"
    ".Lpad2r%=:\n"
    "s_mov_b64 exec, -1\n"
    ".Lpad2s%=:\n"
    "s_sleep 10\n"
    "s_mov_b32 s52, 100000\n"
    ".Lh%=:\n"
    "buffer_load_dwordx2 v[86:87], v12, s[24:27], 0 offen sc1\n"
    "s_waitcnt vmcnt(0)\n"
    "v_cmp_ne_u32 vcc, v86, v84\n"
    "s_cbranch_vccz .Lhok%=\n"
    "s_sleep 1\n"
    "s_sub_u32 s52, s52, 1\n"
    "s_cbranch_scc0 .Lh%=\n"
    ".Lhok%=:\n"
    "s_cmp_lg_u32 s39, 0\n"
    "s_cbranch_scc1 .Lnb%=\n"
    "s_mov_b64 exec, 1\n"
    "buffer_store_dword v84, v89, s[24:27], 0 offen sc1\n"
    "s_mov_b64 exec, -1\n"
    ".Lnb%=:\n"
    "v_subrev_u32 v87, s32, v87\n"
    "v_min_i32 v87, 1000, v87\n"
    "v_max_i32 v87, -1000, v87\n"
    "s_sub_i32 s43, s32, s10\n"
    "s_sub_i32 s44, s34, s12\n"
    "s_lshl_b32 s44, s44, 4\n"
    "v_cvt_f32_i32 v9, s43\n"
    "v_cvt_f32_i32 v86, s44\n"
    "v_rcp_f32 v9, v9\n"
    "s_nop 0\n"
    "v_mul_f32 v9, v9, v86\n"
    "v_cvt_i32_f32 v9, v9\n"
    "v_min_i32 v9, 400, v9\n"
    "v_max_i32 v9, 160, v9\n"
    "s_nop 0\n"
    "v_readfirstlane_b32 s44, v9\n"
    "v_readlane_b32 s16, v87, 6\n"
    "v_readlane_b32 s43, v87, 7\n"
    "s_nop 0\n"
    "s_max_i32 s16, s16, s43\n"
    "s_nop 0\n"
    "s_add_i32 s16, s16, 40\n"
    "s_mul_i32 s16, s16, s44\n"
    "s_ashr_i32 s16, s16, 4\n"
    "s_add_i32 s16, s16, 1271\n"
    "s_add_i32 s16, s16, s34\n"
    "v_readlane_b32 s17, v87, 4\n"
    "v_readlane_b32 s43, v87, 5\n"
    "s_nop 0\n"
    "s_max_i32 s17, s17, s43\n"
    "s_nop 0\n"
    "s_add_i32 s17, s17, 51\n"
    "s_mul_i32 s17, s17, s44\n"
    "s_ashr_i32 s17, s17, 4\n"
    "s_add_i32 s17, s17, 1926\n"
    "s_add_i32 s17, s17, s34\n"
    "v_readlane_b32 s18, v87, 2\n"
    "v_readlane_b32 s43, v87, 3\n"
    "s_nop 0\n"
    "s_max_i32 s18, s18, s43\n"
    "s_nop 0\n"
    "s_add_i32 s18, s18, 39\n"
    "s_mul_i32 s18, s18, s44\n"
    "s_ashr_i32 s18, s18, 4\n"
    "s_add_i32 s18, s18, 3972\n"
    "s_add_i32 s18, s18, s34\n"
    "v_readlane_b32 s19, v87, 1\n"
    "s_nop 0\n"
    "s_add_i32 s19, s19, 25\n"
    "s_mul_i32 s19, s19, s44\n"
    "s_ashr_i32 s19, s19, 4\n"
    "s_add_i32 s19, s19, 4845\n"
    "s_add_i32 s19, s19, s34\n"
    ".Lu0%=:\n"
    "s_memtime s[54:55]\n"
    "s_waitcnt lgkmcnt(0)\n"
    "s_sub_i32 s54, s54, s16\n"
    "s_cmp_lt_i32 s54, 0\n"
    "s_cbranch_scc1 .Lu0%=\n"
    "v_and_b32 v10, 1, v1\n"
    "v_lshlrev_b32 v10, 7, v10\n"
    "v_xor_b32 v10, v10, v90\n"
    "v_xor_b32 v10, 768, v10\n"
    "v_add_u32 v10, 131072, v10\n"
    "s_mov_b32 s52, 100000\n"
    ".Lf%=:\n"
    "buffer_load_dword v9, v10, s[24:27], 0 offen sc1\n"
    "s_waitcnt vmcnt(0)\n"
    "v_cmp_ne_u32 vcc, v9, v11\n"
    "s_cbranch_vccz .Lfk%=\n"
    "s_sleep 1\n"
    "s_sub_u32 s52, s52, 1\n"
    "s_cbranch_scc0 .Lf%=\n"
    ".Lfk%=:\n"
    "s_movk_i32 s53, 24576\n"
    "buffer_load_dwordx4 v[48:51], v8, s[24:27], s53 offen sc1\n"
    "s_movk_i32 s53, 28672\n"
    "buffer_load_dwordx4 v[52:55], v8, s[24:27], s53 offen sc1\n"
    ".Lu1%=:\n"
    "s_memtime s[54:55]\n"
    "s_waitcnt lgkmcnt(0)\n"
    "s_sub_i32 s54, s54, s17\n"
    "s_cmp_lt_i32 s54, 0\n"
    "s_cbranch_scc1 .Lu1%=\n"
    "s_movk_i32 s53, 16384\n"
    "buffer_load_dwordx4 v[40:43], v8, s[24:27], s53 offen sc1\n"
    "s_movk_i32 s53, 20480\n"
    "buffer_load_dwordx4 v[44:47], v8, s[24:27], s53 offen sc1\n"
    "s_waitcnt vmcnt(2)\n"
    "v_lshlrev_b32 v72, 16, v48\n"
    "v_and_b32 v73, s8, v48\n"
    "v_lshlrev_b32 v74, 16, v49\n"
    "v_and_b32 v75, s8, v49\n"
    "v_lshlrev_b32 v76, 16, v50\n"
    "v_and_b32 v77, s8, v50\n"
    "v_lshlrev_b32 v78, 16, v51\n"
    "v_and_b32 v79, s8, v51\n"
    "v_mul_f32 v56, s56, v72\n"
    "v_mul_f32 v57, s56, v73\n"
    "v_mul_f32 v58, s56, v74\n"
    "v_mul_f32 v59, s56, v75\n"
    "v_mul_f32 v60, s56, v76\n"
    "v_mul_f32 v61, s56, v77\n"
    "v_mul_f32 v62, s56, v78\n"
    "v_mul_f32 v63, s56, v79\n"
    "v_lshlrev_b32 v72, 16, v52\n"
    "v_and_b32 v73, s8, v52\n"
    "v_lshlrev_b32 v74, 16, v53\n"
    "v_and_b32 v75, s8, v53\n"
    "v_lshlrev_b32 v76, 16, v54\n"
    "v_and_b32 v77, s8, v54\n"
    "v_lshlrev_b32 v78, 16, v55\n"
    "v_and_b32 v79, s8, v55\n"
    "v_mul_f32 v64, s57, v72\n"
    "v_mul_f32 v65, s57, v73\n"
    "v_mul_f32 v66, s57, v74\n"
    "v_mul_f32 v67, s57, v75\n"
    "v_mul_f32 v68, s57, v76\n"
    "v_mul_f32 v69, s57, v77\n"
    "v_mul_f32 v70, s57, v78\n"
    "v_mul_f32 v71, s57, v79\n"
    ".Lu2%=:\n"
    "s_memtime s[54:55]\n"
    "s_waitcnt lgkmcnt(0)\n"
    "s_sub_i32 s54, s54, s18\n"
    "s_cmp_lt_i32 s54, 0\n"
    "s_cbranch_scc1 .Lu2%=\n"
    "s_movk_i32 s53, 8192\n"
    "buffer_load_dwordx4 v[32:35], v8, s[24:27], s53 offen sc1\n"
    "s_movk_i32 s53, 12288\n"
    "buffer_load_dwordx4 v[36:39], v8, s[24:27], s53 offen sc1\n"
    "s_setprio 2\n"
    "s_mov_b32 s52, 100000\n"
    "s_waitcnt vmcnt(2)\n"
    ".Lc1%=:\n"
    "v_max_u32 v9, v40, v41\n"
    "v_max3_u32 v9, v9, v42, v43\n"
    "v_max3_u32 v9, v9, v44, v45\n"
    "v_max3_u32 v9, v9, v46, v47\n"
    "v_cmp_eq_u32 vcc, -1, v9\n"
    "s_cbranch_vccz .Lc1k%=\n"
    "s_movk_i32 s53, 16384\n"
    "buffer_load_dwordx4 v[40:43], v8, s[24:27], s53 offen sc1\n"
    "s_movk_i32 s53, 20480\n"
    "buffer_load_dwordx4 v[44:47], v8, s[24:27], s53 offen sc1\n"
    "s_waitcnt vmcnt(0)\n"
    "s_sub_u32 s52, s52, 1\n"
    "s_cbranch_scc0 .Lc1%=\n"
    ".Lc1k%=:\n"
    "v_lshlrev_b32 v72, 16, v40\n"
    "v_and_b32 v73, s8, v40\n"
    "v_lshlrev_b32 v74, 16, v41\n"
    "v_and_b32 v75, s8, v41\n"
    "v_lshlrev_b32 v76, 16, v42\n"
    "v_and_b32 v77, s8, v42\n"
    "v_lshlrev_b32 v78, 16, v43\n"
    "v_and_b32 v79, s8, v43\n"
    "v_fmac_f32 v56, s56, v72\n"
    "v_fmac_f32 v57, s56, v73\n"
    "v_fmac_f32 v58, s56, v74\n"
    "v_fmac_f32 v59, s56, v75\n"
    "v_fmac_f32 v60, s56, v76\n"
    "v_fmac_f32 v61, s56, v77\n"
    "v_fmac_f32 v62, s56, v78\n"
    "v_fmac_f32 v63, s56, v79\n"
    "v_lshlrev_b32 v72, 16, v44\n"
    "v_and_b32 v73, s8, v44\n"
    "v_lshlrev_b32 v74, 16, v45\n"
    "v_and_b32 v75, s8, v45\n"
    "v_lshlrev_b32 v76, 16, v46\n"
    "v_and_b32 v77, s8, v46\n"
    "v_lshlrev_b32 v78, 16, v47\n"
    "v_and_b32 v79, s8, v47\n"
    "v_fmac_f32 v64, s57, v72\n"
    "v_fmac_f32 v65, s57, v73\n"
    "v_fmac_f32 v66, s57, v74\n"
    "v_fmac_f32 v67, s57, v75\n"
    "v_fmac_f32 v68, s57, v76\n"
    "v_fmac_f32 v69, s57, v77\n"
    "v_fmac_f32 v70, s57, v78\n"
    "v_fmac_f32 v71, s57, v79\n"
    ".Lu3%=:\n"
    "s_memtime s[54:55]\n"
    "s_waitcnt lgkmcnt(0)\n"
    "s_sub_i32 s54, s54, s19\n"
    "s_cmp_lt_i32 s54, 0\n"
    "s_cbranch_scc1 .Lu3%=\n"
    "s_movk_i32 s53, 4096\n"
    "buffer_load_dwordx4 v[28:31], v8, s[24:27], s53 offen sc1\n"
    "s_mov_b32 s52, 100000\n"
    "s_waitcnt vmcnt(1)\n"
    ".Lc2%=:\n"
    "v_max_u32 v9, v32, v33\n"
    "v_max3_u32 v9, v9, v34, v35\n"
    "v_max3_u32 v9, v9, v36, v37\n"
    "v_max3_u32 v9, v9, v38, v39\n"
    "v_cmp_eq_u32 vcc, -1, v9\n"
    "s_cbranch_vccz .Lc2k%=\n"
    "s_movk_i32 s53, 8192\n"
    "buffer_load_dwordx4 v[32:35], v8, s[24:27], s53 offen sc1\n"
    "s_movk_i32 s53, 12288\n"
    "buffer_load_dwordx4 v[36:39], v8, s[24:27], s53 offen sc1\n"
    "s_waitcnt vmcnt(0)\n"
    "s_sub_u32 s52, s52, 1\n"
    "s_cbranch_scc0 .Lc2%=\n"
    ".Lc2k%=:\n"
    "v_lshlrev_b32 v72, 16, v32\n"
    "v_and_b32 v73, s8, v32\n"
    "v_lshlrev_b32 v74, 16, v33\n"
    "v_and_b32 v75, s8, v33\n"
    "v_lshlrev_b32 v76, 16, v34\n"
    "v_and_b32 v77, s8, v34\n"
    "v_lshlrev_b32 v78, 16, v35\n"
    "v_and_b32 v79, s8, v35\n"
    "v_fmac_f32 v56, s56, v72\n"
    "v_fmac_f32 v57, s56, v73\n"
    "v_fmac_f32 v58, s56, v74\n"
    "v_fmac_f32 v59, s56, v75\n"
    "v_fmac_f32 v60, s56, v76\n"
    "v_fmac_f32 v61, s56, v77\n"
    "v_fmac_f32 v62, s56, v78\n"
    "v_fmac_f32 v63, s56, v79\n"
    "ds_write_b128 v16, v[56:59] offset:2176\n"
    "ds_write_b128 v16, v[60:63] offset:3264\n"
    "v_lshlrev_b32 v72, 16, v36\n"
    "v_and_b32 v73, s8, v36\n"
    "v_lshlrev_b32 v74, 16, v37\n"
    "v_and_b32 v75, s8, v37\n"
    "v_lshlrev_b32 v76, 16, v38\n"
    "v_and_b32 v77, s8, v38\n"
    "v_lshlrev_b32 v78, 16, v39\n"
    "v_and_b32 v79, s8, v39\n"
    "v_fmac_f32 v64, s57, v72\n"
    "v_fmac_f32 v65, s57, v73\n"
    "v_fmac_f32 v66, s57, v74\n"
    "v_fmac_f32 v67, s57, v75\n"
    "v_fmac_f32 v68, s57, v76\n"
    "v_fmac_f32 v69, s57, v77\n"
    "v_fmac_f32 v70, s57, v78\n"
    "v_fmac_f32 v71, s57, v79\n"
    "s_mov_b32 s52, 100000\n"
    "s_waitcnt vmcnt(0)\n"
    ".Lcl%=:\n"
    "v_max_u32 v9, v28, v29\n"
    "v_max3_u32 v9, v9, v30, v31\n"
    "v_cmp_eq_u32 vcc, -1, v9\n"
    "s_cbranch_vccz .Lclk%=\n"
    "s_movk_i32 s53, 4096\n"
    "buffer_load_dwordx4 v[28:31], v8, s[24:27], s53 offen sc1\n"
    "s_waitcnt vmcnt(0)\n"
    "s_sub_u32 s52, s52, 1\n"
    "s_cbranch_scc0 .Lcl%=\n"
    ".Lclk%=:\n"
    "v_lshlrev_b32 v72, 16, v28\n"
    "v_and_b32 v73, s8, v28\n"
    "v_lshlrev_b32 v74, 16, v29\n"
    "v_and_b32 v75, s8, v29\n"
    "v_lshlrev_b32 v76, 16, v30\n"
    "v_and_b32 v77, s8, v30\n"
    "v_lshlrev_b32 v78, 16, v31\n"
    "v_and_b32 v79, s8, v31\n"
    "v_fmac_f32 v64, s57, v72\n"
    "v_fmac_f32 v65, s57, v73\n"
    "v_fmac_f32 v66, s57, v74\n"
    "v_fmac_f32 v67, s57, v75\n"
    "v_fmac_f32 v68, s57, v76\n"
    "v_fmac_f32 v69, s57, v77\n"
    "v_fmac_f32 v70, s57, v78\n"
    "v_fmac_f32 v71, s57, v79\n"
    "v_exp_f32 v64, v64\n"
    "v_exp_f32 v65, v65\n"
    "v_exp_f32 v66, v66\n"
    "v_exp_f32 v67, v67\n"
    "v_exp_f32 v68, v68\n"
    "v_exp_f32 v69, v69\n"
    "v_exp_f32 v70, v70\n"
    "v_exp_f32 v71, v71\n"
    "v_add_f32 v64, 1.0, v64\n"
    "v_add_f32 v65, 1.0, v65\n"
    "v_add_f32 v66, 1.0, v66\n"
    "v_add_f32 v67, 1.0, v67\n"
    "v_add_f32 v68, 1.0, v68\n"
    "v_add_f32 v69, 1.0, v69\n"
    "v_add_f32 v70, 1.0, v70\n"
    "v_add_f32 v71, 1.0, v71\n"
    "v_rcp_f32 v64, v64\n"
    "v_rcp_f32 v65, v65\n"
    "v_rcp_f32 v66, v66\n"
    "v_rcp_f32 v67, v67\n"
    "v_rcp_f32 v68, v68\n"
    "v_rcp_f32 v69, v69\n"
    "v_rcp_f32 v70, v70\n"
    "v_rcp_f32 v71, v71\n"
    "v_fma_f32 v64, v64, s59, v81\n"
    "v_fma_f32 v65, v65, s59, v81\n"
    "v_fma_f32 v66, v66, s59, v81\n"
    "v_fma_f32 v67, v67, s59, v81\n"
    "ds_write_b128 v16, v[64:67] offset:4352\n"
    "v_fma_f32 v68, v68, s59, v81\n"
    "v_fma_f32 v69, v69, s59, v81\n"
    "v_fma_f32 v70, v70, s59, v81\n"
    "v_fma_f32 v71, v71, s59, v81\n"
    "ds_write_b128 v16, v[68:71] offset:5440\n"
    "s_waitcnt lgkmcnt(0)\n"
    "s_barrier\n"
    "ds_read_b128 v[96:99], v14\n"
    "ds_read_b128 v[100:103], v14 offset:2176\n"
    "ds_read_b128 v[104:107], v14 offset:4352\n"
    "s_waitcnt lgkmcnt(1)\n"
    "v_fmac_f32 v100, s56, v96\n"
    "v_fmac_f32 v101, s56, v97\n"
    "v_fmac_f32 v102, s56, v98\n"
    "v_fmac_f32 v103, s56, v99\n"
    "v_exp_f32 v100, v100\n"
    "v_exp_f32 v101, v101\n"
    "v_exp_f32 v102, v102\n"
    "v_exp_f32 v103, v103\n"
    "v_add_f32 v100, 1.0, v100\n"
    "v_add_f32 v101, 1.0, v101\n"
    "v_add_f32 v102, 1.0, v102\n"
    "v_add_f32 v103, 1.0, v103\n"
    "v_rcp_f32 v100, v100\n"
    "v_rcp_f32 v101, v101\n"
    "v_rcp_f32 v102, v102\n"
    "v_rcp_f32 v103, v103\n"
    "v_fma_f32 v100, v100, s58, v17\n"
    "v_fma_f32 v101, v101, s58, v17\n"
    "v_fma_f32 v102, v102, s58, v17\n"
    "v_fma_f32 v103, v103, s58, v17\n"
    "s_waitcnt lgkmcnt(0)\n"
    "v_mul_f32 v100, v100, v104\n"
    "v_mul_f32 v101, v101, v105\n"
    "v_mul_f32 v102, v102, v106\n"
    "v_mul_f32 v103, v103, v107\n"
    "v_cvt_pk_bf16_f32 v96, v100, v101\n"
    "v_cvt_pk_bf16_f32 v97, v102, v103\n"
    "buffer_store_dwordx2 v[96:97], v15, s[28:31], 0 offen\n"
    "s_endpgm\n"
    ".p2align 12\n"
    ".Lcons%=:\n"
    "v_and_b32 v9, 15, v1\n"
    "v_lshrrev_b32 v10, 4, v1\n"
    "v_mul_u32_u24 v5, 2080, v9\n"
    "v_lshl_add_u32 v5, v10, 4, v5\n"
    "v_bfe_u32 v7, v1, 3, 2\n"
    "v_xor_b32 v7, s38, v7\n"
    "v_lshlrev_b32 v7, 5, v7\n"
    "v_lshl_add_u32 v7, v10, 10, v7\n"
    "v_bfe_u32 v6, v1, 2, 2\n"
    "v_lshl_add_u32 v7, v6, 7, v7\n"
    "v_and_b32 v6, 3, v1\n"
    "v_lshl_add_u32 v7, v6, 3, v7\n"
    "v_add_u32 v6, 56576, v7\n"
    "v_add_u32 v7, 122112, v7\n"
    "v_mul_u32_u24 v13, 1056, v9\n"
    "v_lshl_add_u32 v13, v10, 4, v13\n"
    "v_add_u32 v13, 37440, v13\n"
    "v_lshrrev_b32 v11, 2, v9\n"
    "v_cmp_eq_u32 vcc, v11, v10\n"
    "v_and_b32 v11, 1, v1\n"
    "v_lshlrev_b32 v11, 4, v11\n"
    "v_mov_b32 v232, 0x3f80\n"
    "v_lshlrev_b32 v11, v11, v232\n"
    "v_cndmask_b32 v11, 0, v11, vcc\n"
    "v_and_b32 v232, 2, v1\n"
    "v_cmp_eq_u32 vcc, 0, v232\n"
    "s_nop 1\n"
    "v_cndmask_b32 v232, 0, v11, vcc\n"
    "v_sub_u32 v233, v11, v232\n"
    "v_mov_b32 v234, v232\n"
    "v_mov_b32 v235, v233\n"
    "s_lshl_b32 s39, s47, 1\n"
    "s_add_i32 s39, s39, s48\n"
    "s_lshl_b32 s41, s39, 12\n"
    "s_lshl_b32 s43, s51, 8\n"
    "s_add_i32 s43, s43, s38\n"
    "s_lshl_b32 s43, s43, 10\n"
    "s_add_i32 s43, s43, 294912\n"
    "v_lshlrev_b32 v16, 4, v1\n"
    "v_add_u32 v8, s43, v16\n"
    "s_mul_i32 s43, s38, 6720\n"
    "s_add_i32 s43, s43, 97536\n"
    "v_lshrrev_b32 v10, 4, v1\n"
    "v_lshl_add_u32 v16, v10, 4, v16\n"
    "v_add_u32 v16, s43, v16\n"
    "s_lshl_b32 s43, s38, 2\n"
    "v_add_u32 v9, s43, v10\n"
    "v_bfe_u32 v14, v1, 2, 2\n"
    "v_mul_u32_u24 v14, 6720, v14\n"
    "v_lshl_add_u32 v14, v9, 4, v14\n"
    "v_and_b32 v10, 3, v1\n"
    "v_mul_u32_u24 v10, 272, v10\n"
    "s_lshr_b32 s52, s37, 2\n"
    "s_mul_i32 s43, s52, 1088\n"
    "s_add_i32 s43, s43, 97536\n"
    "v_add3_u32 v14, v14, v10, s43\n"
    "s_mov_b32 s28, s12\n"
    "s_and_b32 s29, s13, 0xffff\n"
    "s_mov_b32 s30, 1024000\n"
    "s_mov_b32 s31, 0x20000\n"
    "s_lshr_b32 s44, s36, 7\n"
    "s_mul_i32 s44, s44, 512000\n"
    "s_lshl_b32 s43, s50, 1\n"
    "s_add_i32 s43, s43, s49\n"
    "s_lshl_b32 s43, s43, 2\n"
    "s_add_i32 s43, s43, s40\n"
    "s_lshl_b32 s43, s43, 7\n"
    "s_add_i32 s43, s43, s44\n"
    "s_lshl_b32 s44, s39, 1\n"
    "s_add_i32 s44, s44, s52\n"
    "v_lshlrev_b32 v9, 4, v9\n"
    "v_add_u32 v9, s44, v9\n"
    "v_and_b32 v10, 15, v1\n"
    "v_lshlrev_b32 v15, 10, v9\n"
    "v_lshl_add_u32 v15, v10, 3, v15\n"
    "v_add_u32 v15, s43, v15\n"
    "v_cmp_gt_u32 vcc, 250, v9\n"
    "v_mov_b32 v10, -1\n"
    "s_nop 0\n"
    "v_cndmask_b32 v15, v10, v15, vcc\n"
    "s_mov_b32 s43, 0x4038aa3b\n"
    "s_mov_b32 s44, 0xbfb8aa3b\n"
    "s_cmp_eq_u32 s48, 0\n"
    "s_cselect_b32 s56, s43, s44\n"
    "s_cselect_b32 s57, s44, s43\n"
    "s_cselect_b32 s58, -2.0, 1.0\n"
    "s_cselect_b32 s59, 1.0, -2.0\n"
    "s_cselect_b32 s43, 1.0, 0\n"
    "s_cselect_b32 s44, 0, 1.0\n"
    "v_mov_b32 v17, s43\n"
    "s_lshl_b32 s43, s51, 2\n"
    "s_add_i32 s43, s43, s38\n"
    "s_lshl_b32 s43, s43, 10\n"
    "s_add_i32 s43, s43, 163840\n"
    "s_lshl_b32 s44, s39, 7\n"
    "s_add_i32 s43, s43, s44\n"
    "v_lshlrev_b32 v19, 26, v1\n"
    "v_add_u32 v19, s43, v19\n"
    "s_waitcnt vmcnt(3)\n"
    "s_barrier\n"
    "ds_read_b64_tr_b16 v[84:85], v6 offset:0\n"
    "ds_read_b64_tr_b16 v[86:87], v6 offset:512\n"
    "ds_read_b128 v[88:91], v5 offset:0\n"
    "ds_read_b128 v[92:95], v5 offset:128\n"
    "ds_read_b64_tr_b16 v[96:97], v6 offset:4096\n"
    "ds_read_b64_tr_b16 v[98:99], v6 offset:4608\n"
    "ds_read_b128 v[100:103], v5 offset:256\n"
    "s_waitcnt lgkmcnt(5)\n"
    "s_waitcnt lgkmcnt(4)\n"
    "v_mfma_f32_16x16x32_bf16 v[20:23], v[84:87], v[88:91], 0\n"
    "ds_read_b128 v[104:107], v5 offset:384\n"
    "s_waitcnt lgkmcnt(4)\n"
    "v_mfma_f32_16x16x32_bf16 v[24:27], v[84:87], v[92:95], 0\n"
    "ds_read_b128 v[108:111], v5 offset:512\n"
    "s_waitcnt lgkmcnt(3)\n"
    "v_mfma_f32_16x16x32_bf16 v[20:23], v[96:99], v[92:95], v[20:23]\n"
    "s_waitcnt lgkmcnt(2)\n"
    "v_mfma_f32_16x16x32_bf16 v[28:31], v[84:87], v[100:103], 0\n"
    "ds_read_b128 v[112:115], v5 offset:640\n"
    "v_mfma_f32_16x16x32_bf16 v[24:27], v[96:99], v[100:103], v[24:27]\n"
    "s_waitcnt lgkmcnt(2)\n"
    "v_mfma_f32_16x16x32_bf16 v[32:35], v[84:87], v[104:107], 0\n"
    "ds_read_b128 v[116:119], v5 offset:768\n"
    "v_mfma_f32_16x16x32_bf16 v[28:31], v[96:99], v[104:107], v[28:31]\n"
    "s_waitcnt lgkmcnt(2)\n"
    "v_mfma_f32_16x16x32_bf16 v[36:39], v[84:87], v[108:111], 0\n"
    "ds_read_b128 v[120:123], v5 offset:896\n"
    "v_mfma_f32_16x16x32_bf16 v[32:35], v[96:99], v[108:111], v[32:35]\n"
    "s_waitcnt lgkmcnt(2)\n"
    "v_mfma_f32_16x16x32_bf16 v[40:43], v[84:87], v[112:115], 0\n"
    "s_waitcnt vmcnt(0)\n"
    "s_barrier\n"
    "ds_read_b128 v[124:127], v5 offset:1024\n"
    "v_mfma_f32_16x16x32_bf16 v[36:39], v[96:99], v[112:115], v[36:39]\n"
    "s_waitcnt lgkmcnt(2)\n"
    "v_mfma_f32_16x16x32_bf16 v[44:47], v[84:87], v[116:119], 0\n"
    "ds_read_b128 v[128:131], v5 offset:1152\n"
    "v_mfma_f32_16x16x32_bf16 v[40:43], v[96:99], v[116:119], v[40:43]\n"
    "s_waitcnt lgkmcnt(2)\n"
    "v_mfma_f32_16x16x32_bf16 v[48:51], v[84:87], v[120:123], 0\n"
    "ds_read_b128 v[132:135], v5 offset:1280\n"
    "v_mfma_f32_16x16x32_bf16 v[44:47], v[96:99], v[120:123], v[44:47]\n"
    "s_waitcnt lgkmcnt(2)\n"
    "v_mfma_f32_16x16x32_bf16 v[52:55], v[84:87], v[124:127], 0\n"
    "ds_read_b128 v[136:139], v5 offset:1408\n"
    "v_mfma_f32_16x16x32_bf16 v[48:51], v[96:99], v[124:127], v[48:51]\n"
    "s_waitcnt lgkmcnt(2)\n"
    "v_mfma_f32_16x16x32_bf16 v[56:59], v[84:87], v[128:131], 0\n"
    "ds_read_b128 v[140:143], v5 offset:1536\n"
    "v_mfma_f32_16x16x32_bf16 v[52:55], v[96:99], v[128:131], v[52:55]\n"
    "s_waitcnt lgkmcnt(2)\n"
    "v_mfma_f32_16x16x32_bf16 v[60:63], v[84:87], v[132:135], 0\n"
    "ds_read_b128 v[144:147], v5 offset:1664\n"
    "v_mfma_f32_16x16x32_bf16 v[56:59], v[96:99], v[132:135], v[56:59]\n"
    "s_waitcnt lgkmcnt(2)\n"
    "v_mfma_f32_16x16x32_bf16 v[64:67], v[84:87], v[136:139], 0\n"
    "ds_read_b128 v[148:151], v5 offset:1792\n"
    "v_mfma_f32_16x16x32_bf16 v[60:63], v[96:99], v[136:139], v[60:63]\n"
    "s_waitcnt lgkmcnt(2)\n"
    "v_mfma_f32_16x16x32_bf16 v[68:71], v[84:87], v[140:143], 0\n"
    "ds_read_b128 v[152:155], v5 offset:1920\n"
    "v_mfma_f32_16x16x32_bf16 v[64:67], v[96:99], v[140:143], v[64:67]\n"
    "s_waitcnt lgkmcnt(2)\n"
    "v_mfma_f32_16x16x32_bf16 v[72:75], v[84:87], v[144:147], 0\n"
    "ds_read_b128 v[156:159], v5 offset:2080\n"
    "v_mfma_f32_16x16x32_bf16 v[68:71], v[96:99], v[144:147], v[68:71]\n"
    "ds_read_b64_tr_b16 v[160:161], v6 offset:8192\n"
    "s_waitcnt lgkmcnt(3)\n"
    "v_mfma_f32_16x16x32_bf16 v[76:79], v[84:87], v[148:151], 0\n"
    "ds_read_b64_tr_b16 v[162:163], v6 offset:8704\n"
    "v_mfma_f32_16x16x32_bf16 v[72:75], v[96:99], v[148:151], v[72:75]\n"
    "s_waitcnt lgkmcnt(3)\n"
    "v_mfma_f32_16x16x32_bf16 v[80:83], v[84:87], v[152:155], 0\n"
    "v_mfma_f32_16x16x32_bf16 v[76:79], v[96:99], v[152:155], v[76:79]\n"
    "s_waitcnt lgkmcnt(2)\n"
    "v_mfma_f32_16x16x32_bf16 v[80:83], v[96:99], v[156:159], v[80:83]\n"
    "s_waitcnt lgkmcnt(0)\n"
    "v_mfma_f32_16x16x32_bf16 v[20:23], v[160:163], v[100:103], v[20:23]\n"
    "v_mfma_f32_16x16x32_bf16 v[24:27], v[160:163], v[104:107], v[24:27]\n"
    "v_mfma_f32_16x16x32_bf16 v[28:31], v[160:163], v[108:111], v[28:31]\n"
    "v_mfma_f32_16x16x32_bf16 v[32:35], v[160:163], v[112:115], v[32:35]\n"
    "ds_read_b128 v[164:167], v5 offset:2208\n"
    "v_mfma_f32_16x16x32_bf16 v[36:39], v[160:163], v[116:119], v[36:39]\n"
    "ds_read_b64_tr_b16 v[168:169], v6 offset:12288\n"
    "ds_read_b64_tr_b16 v[170:171], v6 offset:12800\n"
    "v_mfma_f32_16x16x32_bf16 v[40:43], v[160:163], v[120:123], v[40:43]\n"
    "v_mfma_f32_16x16x32_bf16 v[44:47], v[160:163], v[124:127], v[44:47]\n"
    "v_mfma_f32_16x16x32_bf16 v[48:51], v[160:163], v[128:131], v[48:51]\n"
    "v_mfma_f32_16x16x32_bf16 v[52:55], v[160:163], v[132:135], v[52:55]\n"
    "v_mfma_f32_16x16x32_bf16 v[56:59], v[160:163], v[136:139], v[56:59]\n"
    "v_mfma_f32_16x16x32_bf16 v[60:63], v[160:163], v[140:143], v[60:63]\n"
    "v_mfma_f32_16x16x32_bf16 v[64:67], v[160:163], v[144:147], v[64:67]\n"
    "v_mfma_f32_16x16x32_bf16 v[68:71], v[160:163], v[148:151], v[68:71]\n"
    "v_mfma_f32_16x16x32_bf16 v[72:75], v[160:163], v[152:155], v[72:75]\n"
    "v_mfma_f32_16x16x32_bf16 v[76:79], v[160:163], v[156:159], v[76:79]\n"
    "s_waitcnt lgkmcnt(0)\n"
    "v_mfma_f32_16x16x32_bf16 v[80:83], v[160:163], v[164:167], v[80:83]\n"
    "v_mfma_f32_16x16x32_bf16 v[20:23], v[168:171], v[104:107], v[20:23]\n"
    "v_mfma_f32_16x16x32_bf16 v[24:27], v[168:171], v[108:111], v[24:27]\n"
    "v_mfma_f32_16x16x32_bf16 v[28:31], v[168:171], v[112:115], v[28:31]\n"
    "v_mfma_f32_16x16x32_bf16 v[32:35], v[168:171], v[116:119], v[32:35]\n"
    "ds_read_b128 v[172:175], v5 offset:2336\n"
    "v_mfma_f32_16x16x32_bf16 v[36:39], v[168:171], v[120:123], v[36:39]\n"
    "ds_read_b64_tr_b16 v[176:177], v6 offset:16384\n"
    "ds_read_b64_tr_b16 v[178:179], v6 offset:16896\n"
    "v_mfma_f32_16x16x32_bf16 v[40:43], v[168:171], v[124:127], v[40:43]\n"
    "v_mfma_f32_16x16x32_bf16 v[44:47], v[168:171], v[128:131], v[44:47]\n"
    "v_mfma_f32_16x16x32_bf16 v[48:51], v[168:171], v[132:135], v[48:51]\n"
    "v_mfma_f32_16x16x32_bf16 v[52:55], v[168:171], v[136:139], v[52:55]\n"
    "v_mfma_f32_16x16x32_bf16 v[56:59], v[168:171], v[140:143], v[56:59]\n"
    "v_mfma_f32_16x16x32_bf16 v[60:63], v[168:171], v[144:147], v[60:63]\n"
    "v_mfma_f32_16x16x32_bf16 v[64:67], v[168:171], v[148:151], v[64:67]\n"
    "v_mfma_f32_16x16x32_bf16 v[68:71], v[168:171], v[152:155], v[68:71]\n"
    "v_mfma_f32_16x16x32_bf16 v[72:75], v[168:171], v[156:159], v[72:75]\n"
    "v_mfma_f32_16x16x32_bf16 v[76:79], v[168:171], v[164:167], v[76:79]\n"
    "s_waitcnt lgkmcnt(0)\n"
    "v_mfma_f32_16x16x32_bf16 v[80:83], v[168:171], v[172:175], v[80:83]\n"
    "v_mfma_f32_16x16x32_bf16 v[20:23], v[176:179], v[108:111], v[20:23]\n"
    "v_mfma_f32_16x16x32_bf16 v[24:27], v[176:179], v[112:115], v[24:27]\n"
    "v_mfma_f32_16x16x32_bf16 v[28:31], v[176:179], v[116:119], v[28:31]\n"
    "v_mfma_f32_16x16x32_bf16 v[32:35], v[176:179], v[120:123], v[32:35]\n"
    "ds_read_b128 v[180:183], v5 offset:2464\n"
    "v_mfma_f32_16x16x32_bf16 v[36:39], v[176:179], v[124:127], v[36:39]\n"
    "ds_read_b64_tr_b16 v[184:185], v6 offset:20480\n"
    "ds_read_b64_tr_b16 v[186:187], v6 offset:20992\n"
    "v_mfma_f32_16x16x32_bf16 v[40:43], v[176:179], v[128:131], v[40:43]\n"
    "v_mfma_f32_16x16x32_bf16 v[44:47], v[176:179], v[132:135], v[44:47]\n"
    "v_mfma_f32_16x16x32_bf16 v[48:51], v[176:179], v[136:139], v[48:51]\n"
    "v_mfma_f32_16x16x32_bf16 v[52:55], v[176:179], v[140:143], v[52:55]\n"
    "v_mfma_f32_16x16x32_bf16 v[56:59], v[176:179], v[144:147], v[56:59]\n"
    "v_mfma_f32_16x16x32_bf16 v[60:63], v[176:179], v[148:151], v[60:63]\n"
    "v_mfma_f32_16x16x32_bf16 v[64:67], v[176:179], v[152:155], v[64:67]\n"
    "v_mfma_f32_16x16x32_bf16 v[68:71], v[176:179], v[156:159], v[68:71]\n"
    "v_mfma_f32_16x16x32_bf16 v[72:75], v[176:179], v[164:167], v[72:75]\n"
    "v_mfma_f32_16x16x32_bf16 v[76:79], v[176:179], v[172:175], v[76:79]\n"
    "s_waitcnt lgkmcnt(0)\n"
    "v_mfma_f32_16x16x32_bf16 v[80:83], v[176:179], v[180:183], v[80:83]\n"
    "v_mfma_f32_16x16x32_bf16 v[20:23], v[184:187], v[112:115], v[20:23]\n"
    "v_mfma_f32_16x16x32_bf16 v[24:27], v[184:187], v[116:119], v[24:27]\n"
    "v_mfma_f32_16x16x32_bf16 v[28:31], v[184:187], v[120:123], v[28:31]\n"
    "v_mfma_f32_16x16x32_bf16 v[32:35], v[184:187], v[124:127], v[32:35]\n"
    "ds_read_b128 v[188:191], v5 offset:2592\n"
    "v_mfma_f32_16x16x32_bf16 v[36:39], v[184:187], v[128:131], v[36:39]\n"
    "ds_read_b64_tr_b16 v[88:89], v6 offset:24576\n"
    "ds_read_b64_tr_b16 v[90:91], v6 offset:25088\n"
    "v_mfma_f32_16x16x32_bf16 v[40:43], v[184:187], v[132:135], v[40:43]\n"
    "v_mfma_f32_16x16x32_bf16 v[44:47], v[184:187], v[136:139], v[44:47]\n"
    "v_mfma_f32_16x16x32_bf16 v[48:51], v[184:187], v[140:143], v[48:51]\n"
    "v_mfma_f32_16x16x32_bf16 v[52:55], v[184:187], v[144:147], v[52:55]\n"
    "v_mfma_f32_16x16x32_bf16 v[56:59], v[184:187], v[148:151], v[56:59]\n"
    "v_mfma_f32_16x16x32_bf16 v[60:63], v[184:187], v[152:155], v[60:63]\n"
    "v_mfma_f32_16x16x32_bf16 v[64:67], v[184:187], v[156:159], v[64:67]\n"
    "v_mfma_f32_16x16x32_bf16 v[68:71], v[184:187], v[164:167], v[68:71]\n"
    "v_mfma_f32_16x16x32_bf16 v[72:75], v[184:187], v[172:175], v[72:75]\n"
    "v_mfma_f32_16x16x32_bf16 v[76:79], v[184:187], v[180:183], v[76:79]\n"
    "s_waitcnt lgkmcnt(0)\n"
    "v_mfma_f32_16x16x32_bf16 v[80:83], v[184:187], v[188:191], v[80:83]\n"
    "v_mfma_f32_16x16x32_bf16 v[20:23], v[88:91], v[116:119], v[20:23]\n"
    "v_mfma_f32_16x16x32_bf16 v[24:27], v[88:91], v[120:123], v[24:27]\n"
    "v_mfma_f32_16x16x32_bf16 v[28:31], v[88:91], v[124:127], v[28:31]\n"
    "v_mfma_f32_16x16x32_bf16 v[32:35], v[88:91], v[128:131], v[32:35]\n"
    "ds_read_b128 v[92:95], v5 offset:2720\n"
    "v_mfma_f32_16x16x32_bf16 v[36:39], v[88:91], v[132:135], v[36:39]\n"
    "s_barrier\n"
    "ds_read_b64_tr_b16 v[238:239], v6 offset:28672\n"
    "v_mfma_f32_16x16x32_bf16 v[40:43], v[88:91], v[136:139], v[40:43]\n"
    "ds_read_b64_tr_b16 v[240:241], v6 offset:29184\n"
    "ds_read_b64_tr_b16 v[236:237], v6 offset:32768\n"
    "v_mfma_f32_16x16x32_bf16 v[44:47], v[88:91], v[140:143], v[44:47]\n"
    "ds_read_b64_tr_b16 v[242:243], v6 offset:33280\n"
    "ds_read_b64_tr_b16 v[244:245], v6 offset:32768\n"
    "v_mfma_f32_16x16x32_bf16 v[48:51], v[88:91], v[144:147], v[48:51]\n"
    "ds_read_b128 v[84:87], v13 offset:448\n"
    "ds_read_b128 v[96:99], v13 offset:576\n"
    "v_mfma_f32_16x16x32_bf16 v[52:55], v[88:91], v[148:151], v[52:55]\n"
    "ds_read_b128 v[100:103], v13 offset:704\n"
    "v_mfma_f32_16x16x32_bf16 v[56:59], v[88:91], v[152:155], v[56:59]\n"
    "ds_read_b128 v[160:163], v13 offset:832\n"
    "v_mfma_f32_16x16x32_bf16 v[60:63], v[88:91], v[156:159], v[60:63]\n"
    "ds_read_b128 v[104:107], v13 offset:960\n"
    "v_mfma_f32_16x16x32_bf16 v[64:67], v[88:91], v[164:167], v[64:67]\n"
    "ds_read_b128 v[168:171], v13 offset:1120\n"
    "v_mfma_f32_16x16x32_bf16 v[68:71], v[88:91], v[172:175], v[68:71]\n"
    "ds_read_b128 v[108:111], v13 offset:1248\n"
    "v_mfma_f32_16x16x32_bf16 v[72:75], v[88:91], v[180:183], v[72:75]\n"
    "ds_read_b128 v[176:179], v13 offset:1376\n"
    "v_mfma_f32_16x16x32_bf16 v[76:79], v[88:91], v[188:191], v[76:79]\n"
    "ds_read_b128 v[112:115], v13 offset:512\n"
    "s_waitcnt lgkmcnt(7)\n"
    "v_mfma_f32_16x16x32_bf16 v[80:83], v[88:91], v[92:95], v[80:83]\n"
    "ds_read_b128 v[184:187], v13 offset:640\n"
    "v_mfma_f32_16x16x32_bf16 v[224:227], v[232:235], v[236:239], 0\n"
    "ds_read_b128 v[116:119], v13 offset:768\n"
    "v_mfma_f32_16x16x32_bf16 v[228:231], v[232:235], v[240:243], 0\n"
    "ds_read_b128 v[120:123], v13 offset:896\n"
    "v_mfma_f32_16x16x32_bf16 v[20:23], v[238:241], v[84:87], v[20:23]\n"
    "ds_read_b128 v[128:131], v13 offset:1056\n"
    "v_mfma_f32_16x16x32_bf16 v[28:31], v[238:241], v[96:99], v[28:31]\n"
    "ds_read_b128 v[136:139], v13 offset:1184\n"
    "s_waitcnt lgkmcnt(7)\n"
    "v_mfma_f32_16x16x32_bf16 v[36:39], v[238:241], v[100:103], v[36:39]\n"
    "ds_read_b128 v[144:147], v13 offset:1312\n"
    "v_mfma_f32_16x16x32_bf16 v[44:47], v[238:241], v[160:163], v[44:47]\n"
    "ds_read_b128 v[152:155], v13 offset:1440\n"
    "v_mfma_f32_16x16x32_bf16 v[52:55], v[238:241], v[104:107], v[52:55]\n"
    "v_mfma_f32_16x16x32_bf16 v[60:63], v[238:241], v[168:171], v[60:63]\n"
    "v_mfma_f32_16x16x32_bf16 v[68:71], v[238:241], v[108:111], v[68:71]\n"
    "v_cvt_pk_bf16_f32 v224, v224, v225\n"
    "s_waitcnt lgkmcnt(4)\n"
    "v_mfma_f32_16x16x32_bf16 v[76:79], v[238:241], v[176:179], v[76:79]\n"
    "v_cvt_pk_bf16_f32 v225, v226, v227\n"
    "v_mfma_f32_16x16x32_bf16 v[24:27], v[242:245], v[112:115], v[24:27]\n"
    "v_cvt_pk_bf16_f32 v226, v228, v229\n"
    "v_mfma_f32_16x16x32_bf16 v[32:35], v[242:245], v[184:187], v[32:35]\n"
    "v_cvt_pk_bf16_f32 v227, v230, v231\n"
    "v_mfma_f32_16x16x32_bf16 v[40:43], v[242:245], v[116:119], v[40:43]\n"
    "v_mfma_f32_16x16x32_bf16 v[48:51], v[242:245], v[120:123], v[48:51]\n"
    "ds_read_b128 v[164:167], v5 offset:2848\n"
    "s_waitcnt lgkmcnt(1)\n"
    "v_mfma_f32_16x16x32_bf16 v[56:59], v[242:245], v[128:131], v[56:59]\n"
    "ds_read_b64_tr_b16 v[248:249], v6 offset:36864\n"
    "ds_read_b64_tr_b16 v[250:251], v6 offset:37376\n"
    "v_mfma_f32_16x16x32_bf16 v[64:67], v[242:245], v[136:139], v[64:67]\n"
    "ds_read_b64_tr_b16 v[246:247], v6 offset:40960\n"
    "ds_read_b64_tr_b16 v[252:253], v6 offset:41472\n"
    "v_mfma_f32_16x16x32_bf16 v[72:75], v[242:245], v[144:147], v[72:75]\n"
    "ds_read_b64_tr_b16 v[254:255], v6 offset:40960\n"
    "v_mfma_f32_16x16x32_bf16 v[80:83], v[242:245], v[152:155], v[80:83]\n"
    "v_mfma_f32_16x16x32_bf16 v[192:195], v[224:227], v[124:127], 0\n"
    "v_mfma_f32_16x16x32_bf16 v[196:199], v[224:227], v[132:135], 0\n"
    "v_mfma_f32_16x16x32_bf16 v[200:203], v[224:227], v[140:143], 0\n"
    "v_mfma_f32_16x16x32_bf16 v[204:207], v[224:227], v[148:151], 0\n"
    "v_mfma_f32_16x16x32_bf16 v[208:211], v[224:227], v[156:159], 0\n"
    "v_mfma_f32_16x16x32_bf16 v[212:215], v[224:227], v[172:175], 0\n"
    "ds_read_b128 v[180:183], v13 offset:1504\n"
    "v_mfma_f32_16x16x32_bf16 v[216:219], v[224:227], v[188:191], 0\n"
    "s_waitcnt lgkmcnt(1)\n"
    "v_mfma_f32_16x16x32_bf16 v[220:223], v[224:227], v[164:167], 0\n"
    "v_mfma_f32_16x16x32_bf16 v[224:227], v[232:235], v[246:249], 0\n"
    "v_mfma_f32_16x16x32_bf16 v[228:231], v[232:235], v[250:253], 0\n"
    "v_mfma_f32_16x16x32_bf16 v[20:23], v[248:251], v[96:99], v[20:23]\n"
    "v_mfma_f32_16x16x32_bf16 v[28:31], v[248:251], v[100:103], v[28:31]\n"
    "v_mfma_f32_16x16x32_bf16 v[36:39], v[248:251], v[160:163], v[36:39]\n"
    "v_mfma_f32_16x16x32_bf16 v[44:47], v[248:251], v[104:107], v[44:47]\n"
    "ds_read_b128 v[88:91], v13 offset:1568\n"
    "v_mfma_f32_16x16x32_bf16 v[52:55], v[248:251], v[168:171], v[52:55]\n"
    "v_mfma_f32_16x16x32_bf16 v[60:63], v[248:251], v[108:111], v[60:63]\n"
    "v_mfma_f32_16x16x32_bf16 v[68:71], v[248:251], v[176:179], v[68:71]\n"
    "v_cvt_pk_bf16_f32 v224, v224, v225\n"
    "s_waitcnt lgkmcnt(1)\n"
    "v_mfma_f32_16x16x32_bf16 v[76:79], v[248:251], v[180:183], v[76:79]\n"
    "v_cvt_pk_bf16_f32 v225, v226, v227\n"
    "v_mfma_f32_16x16x32_bf16 v[24:27], v[252:255], v[184:187], v[24:27]\n"
    "v_cvt_pk_bf16_f32 v226, v228, v229\n"
    "v_mfma_f32_16x16x32_bf16 v[32:35], v[252:255], v[116:119], v[32:35]\n"
    "v_cvt_pk_bf16_f32 v227, v230, v231\n"
    "v_mfma_f32_16x16x32_bf16 v[40:43], v[252:255], v[120:123], v[40:43]\n"
    "v_mfma_f32_16x16x32_bf16 v[48:51], v[252:255], v[128:131], v[48:51]\n"
    "ds_read_b128 v[92:95], v5 offset:3104\n"
    "v_mfma_f32_16x16x32_bf16 v[56:59], v[252:255], v[136:139], v[56:59]\n"
    "ds_read_b64_tr_b16 v[238:239], v6 offset:45056\n"
    "ds_read_b64_tr_b16 v[240:241], v6 offset:45568\n"
    "v_mfma_f32_16x16x32_bf16 v[64:67], v[252:255], v[144:147], v[64:67]\n"
    "ds_read_b64_tr_b16 v[236:237], v6 offset:49152\n"
    "ds_read_b64_tr_b16 v[242:243], v6 offset:49664\n"
    "v_mfma_f32_16x16x32_bf16 v[72:75], v[252:255], v[152:155], v[72:75]\n"
    "ds_read_b64_tr_b16 v[244:245], v6 offset:49152\n"
    "s_waitcnt lgkmcnt(6)\n"
    "v_mfma_f32_16x16x32_bf16 v[80:83], v[252:255], v[88:91], v[80:83]\n"
    "v_mfma_f32_16x16x32_bf16 v[192:195], v[224:227], v[132:135], v[192:195]\n"
    "v_mfma_f32_16x16x32_bf16 v[196:199], v[224:227], v[140:143], v[196:199]\n"
    "v_mfma_f32_16x16x32_bf16 v[200:203], v[224:227], v[148:151], v[200:203]\n"
    "v_mfma_f32_16x16x32_bf16 v[204:207], v[224:227], v[156:159], v[204:207]\n"
    "v_mfma_f32_16x16x32_bf16 v[208:211], v[224:227], v[172:175], v[208:211]\n"
    "v_mfma_f32_16x16x32_bf16 v[212:215], v[224:227], v[188:191], v[212:215]\n"
    "ds_read_b128 v[84:87], v13 offset:1632\n"
    "v_mfma_f32_16x16x32_bf16 v[216:219], v[224:227], v[164:167], v[216:219]\n"
    "s_waitcnt lgkmcnt(1)\n"
    "v_mfma_f32_16x16x32_bf16 v[220:223], v[224:227], v[92:95], v[220:223]\n"
    "v_mfma_f32_16x16x32_bf16 v[224:227], v[232:235], v[236:239], 0\n"
    "v_mfma_f32_16x16x32_bf16 v[228:231], v[232:235], v[240:243], 0\n"
    "v_mfma_f32_16x16x32_bf16 v[20:23], v[238:241], v[100:103], v[20:23]\n"
    "v_mfma_f32_16x16x32_bf16 v[28:31], v[238:241], v[160:163], v[28:31]\n"
    "v_mfma_f32_16x16x32_bf16 v[36:39], v[238:241], v[104:107], v[36:39]\n"
    "v_mfma_f32_16x16x32_bf16 v[44:47], v[238:241], v[168:171], v[44:47]\n"
    "ds_read_b128 v[112:115], v13 offset:1696\n"
    "v_mfma_f32_16x16x32_bf16 v[52:55], v[238:241], v[108:111], v[52:55]\n"
    "v_mfma_f32_16x16x32_bf16 v[60:63], v[238:241], v[176:179], v[60:63]\n"
    "v_mfma_f32_16x16x32_bf16 v[68:71], v[238:241], v[180:183], v[68:71]\n"
    "v_cvt_pk_bf16_f32 v224, v224, v225\n"
    "s_waitcnt lgkmcnt(1)\n"
    "v_mfma_f32_16x16x32_bf16 v[76:79], v[238:241], v[84:87], v[76:79]\n"
    "v_cvt_pk_bf16_f32 v225, v226, v227\n"
    "v_mfma_f32_16x16x32_bf16 v[24:27], v[242:245], v[116:119], v[24:27]\n"
    "v_cvt_pk_bf16_f32 v226, v228, v229\n"
    "v_mfma_f32_16x16x32_bf16 v[32:35], v[242:245], v[120:123], v[32:35]\n"
    "v_cvt_pk_bf16_f32 v227, v230, v231\n"
    "v_mfma_f32_16x16x32_bf16 v[40:43], v[242:245], v[128:131], v[40:43]\n"
    "v_mfma_f32_16x16x32_bf16 v[48:51], v[242:245], v[136:139], v[48:51]\n"
    "ds_read_b128 v[124:127], v5 offset:3360\n"
    "v_mfma_f32_16x16x32_bf16 v[56:59], v[242:245], v[144:147], v[56:59]\n"
    "ds_read_b64_tr_b16 v[248:249], v6 offset:53248\n"
    "ds_read_b64_tr_b16 v[250:251], v6 offset:53760\n"
    "v_mfma_f32_16x16x32_bf16 v[64:67], v[242:245], v[152:155], v[64:67]\n"
    "ds_read_b64_tr_b16 v[246:247], v6 offset:57344\n"
    "ds_read_b64_tr_b16 v[252:253], v6 offset:57856\n"
    "v_mfma_f32_16x16x32_bf16 v[72:75], v[242:245], v[88:91], v[72:75]\n"
    "ds_read_b64_tr_b16 v[254:255], v6 offset:57344\n"
    "s_waitcnt lgkmcnt(6)\n"
    "v_mfma_f32_16x16x32_bf16 v[80:83], v[242:245], v[112:115], v[80:83]\n"
    "v_mfma_f32_16x16x32_bf16 v[192:195], v[224:227], v[140:143], v[192:195]\n"
    "v_mfma_f32_16x16x32_bf16 v[196:199], v[224:227], v[148:151], v[196:199]\n"
    "v_mfma_f32_16x16x32_bf16 v[200:203], v[224:227], v[156:159], v[200:203]\n"
    "v_mfma_f32_16x16x32_bf16 v[204:207], v[224:227], v[172:175], v[204:207]\n"
    "v_mfma_f32_16x16x32_bf16 v[208:211], v[224:227], v[188:191], v[208:211]\n"
    "v_mfma_f32_16x16x32_bf16 v[212:215], v[224:227], v[164:167], v[212:215]\n"
    "ds_read_b128 v[96:99], v13 offset:1760\n"
    "v_mfma_f32_16x16x32_bf16 v[216:219], v[224:227], v[92:95], v[216:219]\n"
    "s_waitcnt lgkmcnt(1)\n"
    "v_mfma_f32_16x16x32_bf16 v[220:223], v[224:227], v[124:127], v[220:223]\n"
    "v_mfma_f32_16x16x32_bf16 v[224:227], v[232:235], v[246:249], 0\n"
    "v_mfma_f32_16x16x32_bf16 v[228:231], v[232:235], v[250:253], 0\n"
    "v_mfma_f32_16x16x32_bf16 v[20:23], v[248:251], v[160:163], v[20:23]\n"
    "v_mfma_f32_16x16x32_bf16 v[28:31], v[248:251], v[104:107], v[28:31]\n"
    "v_mfma_f32_16x16x32_bf16 v[36:39], v[248:251], v[168:171], v[36:39]\n"
    "v_mfma_f32_16x16x32_bf16 v[44:47], v[248:251], v[108:111], v[44:47]\n"
    "ds_read_b128 v[184:187], v13 offset:1824\n"
    "v_mfma_f32_16x16x32_bf16 v[52:55], v[248:251], v[176:179], v[52:55]\n"
    "v_mfma_f32_16x16x32_bf16 v[60:63], v[248:251], v[180:183], v[60:63]\n"
    "v_mfma_f32_16x16x32_bf16 v[68:71], v[248:251], v[84:87], v[68:71]\n"
    "v_cvt_pk_bf16_f32 v224, v224, v225\n"
    "s_waitcnt lgkmcnt(1)\n"
    "v_mfma_f32_16x16x32_bf16 v[76:79], v[248:251], v[96:99], v[76:79]\n"
    "v_cvt_pk_bf16_f32 v225, v226, v227\n"
    "v_mfma_f32_16x16x32_bf16 v[24:27], v[252:255], v[120:123], v[24:27]\n"
    "v_cvt_pk_bf16_f32 v226, v228, v229\n"
    "v_mfma_f32_16x16x32_bf16 v[32:35], v[252:255], v[128:131], v[32:35]\n"
    "v_cvt_pk_bf16_f32 v227, v230, v231\n"
    "v_mfma_f32_16x16x32_bf16 v[40:43], v[252:255], v[136:139], v[40:43]\n"
    "v_mfma_f32_16x16x32_bf16 v[48:51], v[252:255], v[144:147], v[48:51]\n"
    "ds_read_b128 v[132:135], v5 offset:3616\n"
    "v_mfma_f32_16x16x32_bf16 v[56:59], v[252:255], v[152:155], v[56:59]\n"
    "s_barrier\n"
    "ds_read_b64_tr_b16 v[238:239], v6 offset:61440\n"
    "v_mfma_f32_16x16x32_bf16 v[64:67], v[252:255], v[88:91], v[64:67]\n"
    "ds_read_b64_tr_b16 v[240:241], v6 offset:61952\n"
    "ds_read_b64_tr_b16 v[236:237], v7 offset:0\n"
    "v_mfma_f32_16x16x32_bf16 v[72:75], v[252:255], v[112:115], v[72:75]\n"
    "ds_read_b64_tr_b16 v[242:243], v7 offset:512\n"
    "ds_read_b64_tr_b16 v[244:245], v7 offset:0\n"
    "s_waitcnt lgkmcnt(6)\n"
    "v_mfma_f32_16x16x32_bf16 v[80:83], v[252:255], v[184:187], v[80:83]\n"
    "v_mfma_f32_16x16x32_bf16 v[192:195], v[224:227], v[148:151], v[192:195]\n"
    "v_mfma_f32_16x16x32_bf16 v[196:199], v[224:227], v[156:159], v[196:199]\n"
    "v_mfma_f32_16x16x32_bf16 v[200:203], v[224:227], v[172:175], v[200:203]\n"
    "v_mfma_f32_16x16x32_bf16 v[204:207], v[224:227], v[188:191], v[204:207]\n"
    "v_mfma_f32_16x16x32_bf16 v[208:211], v[224:227], v[164:167], v[208:211]\n"
    "v_mfma_f32_16x16x32_bf16 v[212:215], v[224:227], v[92:95], v[212:215]\n"
    "ds_read_b128 v[100:103], v13 offset:1888\n"
    "v_mfma_f32_16x16x32_bf16 v[216:219], v[224:227], v[124:127], v[216:219]\n"
    "s_waitcnt lgkmcnt(1)\n"
    "v_mfma_f32_16x16x32_bf16 v[220:223], v[224:227], v[132:135], v[220:223]\n"
    "v_mfma_f32_16x16x32_bf16 v[224:227], v[232:235], v[236:239], 0\n"
    "v_mfma_f32_16x16x32_bf16 v[228:231], v[232:235], v[240:243], 0\n"
    "v_mfma_f32_16x16x32_bf16 v[20:23], v[238:241], v[104:107], v[20:23]\n"
    "v_mfma_f32_16x16x32_bf16 v[28:31], v[238:241], v[168:171], v[28:31]\n"
    "v_mfma_f32_16x16x32_bf16 v[36:39], v[238:241], v[108:111], v[36:39]\n"
    "v_mfma_f32_16x16x32_bf16 v[44:47], v[238:241], v[176:179], v[44:47]\n"
    "ds_read_b128 v[116:119], v13 offset:1952\n"
    "v_mfma_f32_16x16x32_bf16 v[52:55], v[238:241], v[180:183], v[52:55]\n"
    "v_mfma_f32_16x16x32_bf16 v[60:63], v[238:241], v[84:87], v[60:63]\n"
    "v_mfma_f32_16x16x32_bf16 v[68:71], v[238:241], v[96:99], v[68:71]\n"
    "v_cvt_pk_bf16_f32 v224, v224, v225\n"
    "s_waitcnt lgkmcnt(1)\n"
    "v_mfma_f32_16x16x32_bf16 v[76:79], v[238:241], v[100:103], v[76:79]\n"
    "v_cvt_pk_bf16_f32 v225, v226, v227\n"
    "v_mfma_f32_16x16x32_bf16 v[24:27], v[242:245], v[128:131], v[24:27]\n"
    "v_cvt_pk_bf16_f32 v226, v228, v229\n"
    "v_mfma_f32_16x16x32_bf16 v[32:35], v[242:245], v[136:139], v[32:35]\n"
    "v_cvt_pk_bf16_f32 v227, v230, v231\n"
    "v_mfma_f32_16x16x32_bf16 v[40:43], v[242:245], v[144:147], v[40:43]\n"
    "v_mfma_f32_16x16x32_bf16 v[48:51], v[242:245], v[152:155], v[48:51]\n"
    "ds_read_b128 v[140:143], v5 offset:3872\n"
    "v_mfma_f32_16x16x32_bf16 v[56:59], v[242:245], v[88:91], v[56:59]\n"
    "ds_read_b64_tr_b16 v[248:249], v7 offset:4096\n"
    "ds_read_b64_tr_b16 v[250:251], v7 offset:4608\n"
    "v_mfma_f32_16x16x32_bf16 v[64:67], v[242:245], v[112:115], v[64:67]\n"
    "ds_read_b64_tr_b16 v[246:247], v7 offset:8192\n"
    "ds_read_b64_tr_b16 v[252:253], v7 offset:8704\n"
    "v_mfma_f32_16x16x32_bf16 v[72:75], v[242:245], v[184:187], v[72:75]\n"
    "ds_read_b64_tr_b16 v[254:255], v7 offset:8192\n"
    "s_waitcnt lgkmcnt(6)\n"
    "v_mfma_f32_16x16x32_bf16 v[80:83], v[242:245], v[116:119], v[80:83]\n"
    "v_mfma_f32_16x16x32_bf16 v[192:195], v[224:227], v[156:159], v[192:195]\n"
    "v_mfma_f32_16x16x32_bf16 v[196:199], v[224:227], v[172:175], v[196:199]\n"
    "v_mfma_f32_16x16x32_bf16 v[200:203], v[224:227], v[188:191], v[200:203]\n"
    "v_mfma_f32_16x16x32_bf16 v[204:207], v[224:227], v[164:167], v[204:207]\n"
    "v_mfma_f32_16x16x32_bf16 v[208:211], v[224:227], v[92:95], v[208:211]\n"
    "v_mfma_f32_16x16x32_bf16 v[212:215], v[224:227], v[124:127], v[212:215]\n"
    "ds_read_b128 v[160:163], v13 offset:2016\n"
    "v_mfma_f32_16x16x32_bf16 v[216:219], v[224:227], v[132:135], v[216:219]\n"
    "s_waitcnt lgkmcnt(1)\n"
    "v_mfma_f32_16x16x32_bf16 v[220:223], v[224:227], v[140:143], v[220:223]\n"
    "v_mfma_f32_16x16x32_bf16 v[224:227], v[232:235], v[246:249], 0\n"
    "v_mfma_f32_16x16x32_bf16 v[228:231], v[232:235], v[250:253], 0\n"
    "v_mfma_f32_16x16x32_bf16 v[20:23], v[248:251], v[168:171], v[20:23]\n"
    "v_mfma_f32_16x16x32_bf16 v[28:31], v[248:251], v[108:111], v[28:31]\n"
    "v_mfma_f32_16x16x32_bf16 v[36:39], v[248:251], v[176:179], v[36:39]\n"
    "v_mfma_f32_16x16x32_bf16 v[44:47], v[248:251], v[180:183], v[44:47]\n"
    "ds_read_b128 v[120:123], v13 offset:2112\n"
    "v_mfma_f32_16x16x32_bf16 v[52:55], v[248:251], v[84:87], v[52:55]\n"
    "v_mfma_f32_16x16x32_bf16 v[60:63], v[248:251], v[96:99], v[60:63]\n"
    "v_mfma_f32_16x16x32_bf16 v[68:71], v[248:251], v[100:103], v[68:71]\n"
    "v_cvt_pk_bf16_f32 v224, v224, v225\n"
    "s_waitcnt lgkmcnt(1)\n"
    "v_mfma_f32_16x16x32_bf16 v[76:79], v[248:251], v[160:163], v[76:79]\n"
    "v_cvt_pk_bf16_f32 v225, v226, v227\n"
    "v_mfma_f32_16x16x32_bf16 v[24:27], v[252:255], v[136:139], v[24:27]\n"
    "v_cvt_pk_bf16_f32 v226, v228, v229\n"
    "v_mfma_f32_16x16x32_bf16 v[32:35], v[252:255], v[144:147], v[32:35]\n"
    "v_cvt_pk_bf16_f32 v227, v230, v231\n"
    "v_mfma_f32_16x16x32_bf16 v[40:43], v[252:255], v[152:155], v[40:43]\n"
    "v_mfma_f32_16x16x32_bf16 v[48:51], v[252:255], v[88:91], v[48:51]\n"
    "ds_read_b128 v[148:151], v5 offset:4160\n"
    "v_mfma_f32_16x16x32_bf16 v[56:59], v[252:255], v[112:115], v[56:59]\n"
    "s_barrier\n"
    "ds_read_b64_tr_b16 v[238:239], v7 offset:12288\n"
    "v_mfma_f32_16x16x32_bf16 v[64:67], v[252:255], v[184:187], v[64:67]\n"
    "ds_read_b64_tr_b16 v[240:241], v7 offset:12800\n"
    "ds_read_b64_tr_b16 v[236:237], v7 offset:16384\n"
    "v_mfma_f32_16x16x32_bf16 v[72:75], v[252:255], v[116:119], v[72:75]\n"
    "ds_read_b64_tr_b16 v[242:243], v7 offset:16896\n"
    "ds_read_b64_tr_b16 v[244:245], v7 offset:16384\n"
    "s_waitcnt lgkmcnt(6)\n"
    "v_mfma_f32_16x16x32_bf16 v[80:83], v[252:255], v[120:123], v[80:83]\n"
    "v_mfma_f32_16x16x32_bf16 v[192:195], v[224:227], v[172:175], v[192:195]\n"
    "v_mfma_f32_16x16x32_bf16 v[196:199], v[224:227], v[188:191], v[196:199]\n"
    "v_mfma_f32_16x16x32_bf16 v[200:203], v[224:227], v[164:167], v[200:203]\n"
    "v_mfma_f32_16x16x32_bf16 v[204:207], v[224:227], v[92:95], v[204:207]\n"
    "v_mfma_f32_16x16x32_bf16 v[208:211], v[224:227], v[124:127], v[208:211]\n"
    "v_mfma_f32_16x16x32_bf16 v[212:215], v[224:227], v[132:135], v[212:215]\n"
    "ds_read_b128 v[104:107], v13 offset:2176\n"
    "v_mfma_f32_16x16x32_bf16 v[216:219], v[224:227], v[140:143], v[216:219]\n"
    "s_waitcnt lgkmcnt(1)\n"
    "v_mfma_f32_16x16x32_bf16 v[220:223], v[224:227], v[148:151], v[220:223]\n"
    "v_mfma_f32_16x16x32_bf16 v[224:227], v[232:235], v[236:239], 0\n"
    "v_mfma_f32_16x16x32_bf16 v[228:231], v[232:235], v[240:243], 0\n"
    "v_mfma_f32_16x16x32_bf16 v[20:23], v[238:241], v[108:111], v[20:23]\n"
    "v_mfma_f32_16x16x32_bf16 v[28:31], v[238:241], v[176:179], v[28:31]\n"
    "v_mfma_f32_16x16x32_bf16 v[36:39], v[238:241], v[180:183], v[36:39]\n"
    "v_mfma_f32_16x16x32_bf16 v[44:47], v[238:241], v[84:87], v[44:47]\n"
    "ds_read_b128 v[128:131], v13 offset:2240\n"
    "v_mfma_f32_16x16x32_bf16 v[52:55], v[238:241], v[96:99], v[52:55]\n"
    "v_mfma_f32_16x16x32_bf16 v[60:63], v[238:241], v[100:103], v[60:63]\n"
    "v_mfma_f32_16x16x32_bf16 v[68:71], v[238:241], v[160:163], v[68:71]\n"
    "v_cvt_pk_bf16_f32 v224, v224, v225\n"
    "s_waitcnt lgkmcnt(1)\n"
    "v_mfma_f32_16x16x32_bf16 v[76:79], v[238:241], v[104:107], v[76:79]\n"
    "v_cvt_pk_bf16_f32 v225, v226, v227\n"
    "v_mfma_f32_16x16x32_bf16 v[24:27], v[242:245], v[144:147], v[24:27]\n"
    "v_cvt_pk_bf16_f32 v226, v228, v229\n"
    "v_mfma_f32_16x16x32_bf16 v[32:35], v[242:245], v[152:155], v[32:35]\n"
    "v_cvt_pk_bf16_f32 v227, v230, v231\n"
    "v_mfma_f32_16x16x32_bf16 v[40:43], v[242:245], v[88:91], v[40:43]\n"
    "v_mfma_f32_16x16x32_bf16 v[48:51], v[242:245], v[112:115], v[48:51]\n"
    "ds_read_b128 v[156:159], v5 offset:4416\n"
    "v_mfma_f32_16x16x32_bf16 v[56:59], v[242:245], v[184:187], v[56:59]\n"
    "ds_read_b64_tr_b16 v[248:249], v7 offset:20480\n"
    "ds_read_b64_tr_b16 v[250:251], v7 offset:20992\n"
    "v_mfma_f32_16x16x32_bf16 v[64:67], v[242:245], v[116:119], v[64:67]\n"
    "ds_read_b64_tr_b16 v[246:247], v7 offset:24576\n"
    ".Lpad0%=:\n"
    "s_cbranch_execz .Lpad0r%=\n"
    "ds_read_b64_tr_b16 v[252:253], v7 offset:25088\n"
    "v_mfma_f32_16x16x32_bf16 v[72:75], v[242:245], v[120:123], v[72:75]\n"
    "ds_read_b64_tr_b16 v[254:255], v7 offset:24576\n"
    "s_waitcnt lgkmcnt(6)\n"
    "v_mfma_f32_16x16x32_bf16 v[80:83], v[242:245], v[128:131], v[80:83]\n"
    "v_mfma_f32_16x16x32_bf16 v[192:195], v[224:227], v[188:191], v[192:195]\n"
    "v_mfma_f32_16x16x32_bf16 v[196:199], v[224:227], v[164:167], v[196:199]\n"
    "v_mfma_f32_16x16x32_bf16 v[200:203], v[224:227], v[92:95], v[200:203]\n"
    "v_mfma_f32_16x16x32_bf16 v[204:207], v[224:227], v[124:127], v[204:207]\n"
    "v_mfma_f32_16x16x32_bf16 v[208:211], v[224:227], v[132:135], v[208:211]\n"
    "v_mfma_f32_16x16x32_bf16 v[212:215], v[224:227], v[140:143], v[212:215]\n"
    "ds_read_b128 v[168:171], v13 offset:2304\n"
    "v_mfma_f32_16x16x32_bf16 v[216:219], v[224:227], v[148:151], v[216:219]\n"
    "s_waitcnt lgkmcnt(1)\n"
    "v_mfma_f32_16x16x32_bf16 v[220:223], v[224:227], v[156:159], v[220:223]\n"
    "v_mfma_f32_16x16x32_bf16 v[224:227], v[232:235], v[246:249], 0\n"
    "v_mfma_f32_16x16x32_bf16 v[228:231], v[232:235], v[250:253], 0\n"
    "v_mfma_f32_16x16x32_bf16 v[20:23], v[248:251], v[176:179], v[20:23]\n"
    "v_mfma_f32_16x16x32_bf16 v[28:31], v[248:251], v[180:183], v[28:31]\n"
    "v_mfma_f32_16x16x32_bf16 v[36:39], v[248:251], v[84:87], v[36:39]\n"
    "v_mfma_f32_16x16x32_bf16 v[44:47], v[248:251], v[96:99], v[44:47]\n"
    "ds_read_b128 v[136:139], v13 offset:2368\n"
    "v_mfma_f32_16x16x32_bf16 v[52:55], v[248:251], v[100:103], v[52:55]\n"
    "v_mfma_f32_16x16x32_bf16 v[60:63], v[248:251], v[160:163], v[60:63]\n"
    "v_mfma_f32_16x16x32_bf16 v[68:71], v[248:251], v[104:107], v[68:71]\n"
    "v_cvt_pk_bf16_f32 v224, v224, v225\n"
    "s_waitcnt lgkmcnt(1)\n"
    "v_mfma_f32_16x16x32_bf16 v[76:79], v[248:251], v[168:171], v[76:79]\n"
    "v_cvt_pk_bf16_f32 v225, v226, v227\n"
    "v_mfma_f32_16x16x32_bf16 v[24:27], v[252:255], v[152:155], v[24:27]\n"
    "v_cvt_pk_bf16_f32 v226, v228, v229\n"
    "v_mfma_f32_16x16x32_bf16 v[32:35], v[252:255], v[88:91], v[32:35]\n"
    "v_cvt_pk_bf16_f32 v227, v230, v231\n"
    "v_mfma_f32_16x16x32_bf16 v[40:43], v[252:255], v[112:115], v[40:43]\n"
    "v_mfma_f32_16x16x32_bf16 v[48:51], v[252:255], v[184:187], v[48:51]\n"
    "ds_read_b128 v[172:175], v5 offset:4672\n"
    "v_mfma_f32_16x16x32_bf16 v[56:59], v[252:255], v[116:119], v[56:59]\n"
    "s_barrier\n"
    "ds_read_b64_tr_b16 v[238:239], v7 offset:28672\n"
    "v_mfma_f32_16x16x32_bf16 v[64:67], v[252:255], v[120:123], v[64:67]\n"
    "ds_read_b64_tr_b16 v[240:241], v7 offset:29184\n"
    "ds_read_b64_tr_b16 v[236:237], v7 offset:32768\n"
    "v_mfma_f32_16x16x32_bf16 v[72:75], v[252:255], v[128:131], v[72:75]\n"
    "ds_read_b64_tr_b16 v[242:243], v7 offset:33280\n"
    "ds_read_b64_tr_b16 v[244:245], v7 offset:32768\n"
    "s_waitcnt lgkmcnt(6)\n"
    "v_mfma_f32_16x16x32_bf16 v[80:83], v[252:255], v[136:139], v[80:83]\n"
    "v_mfma_f32_16x16x32_bf16 v[192:195], v[224:227], v[164:167], v[192:195]\n"
    "v_mfma_f32_16x16x32_bf16 v[196:199], v[224:227], v[92:95], v[196:199]\n"
    "v_mfma_f32_16x16x32_bf16 v[200:203], v[224:227], v[124:127], v[200:203]\n"
    "v_mfma_f32_16x16x32_bf16 v[204:207], v[224:227], v[132:135], v[204:207]\n"
    "v_mfma_f32_16x16x32_bf16 v[208:211], v[224:227], v[140:143], v[208:211]\n"
    "v_mfma_f32_16x16x32_bf16 v[212:215], v[224:227], v[148:151], v[212:215]\n"
    "ds_read_b128 v[108:111], v13 offset:2432\n"
    "v_mfma_f32_16x16x32_bf16 v[216:219], v[224:227], v[156:159], v[216:219]\n"
    "s_waitcnt lgkmcnt(1)\n"
    "v_mfma_f32_16x16x32_bf16 v[220:223], v[224:227], v[172:175], v[220:223]\n"
    "v_mfma_f32_16x16x32_bf16 v[224:227], v[232:235], v[236:239], 0\n"
    "v_mfma_f32_16x16x32_bf16 v[228:231], v[232:235], v[240:243], 0\n"
    "v_mfma_f32_16x16x32_bf16 v[20:23], v[238:241], v[180:183], v[20:23]\n"
    "v_mfma_f32_16x16x32_bf16 v[28:31], v[238:241], v[84:87], v[28:31]\n"
    "v_mfma_f32_16x16x32_bf16 v[36:39], v[238:241], v[96:99], v[36:39]\n"
    "v_mfma_f32_16x16x32_bf16 v[44:47], v[238:241], v[100:103], v[44:47]\n"
    "ds_read_b128 v[144:147], v13 offset:2496\n"
    "v_mfma_f32_16x16x32_bf16 v[52:55], v[238:241], v[160:163], v[52:55]\n"
    "v_mfma_f32_16x16x32_bf16 v[60:63], v[238:241], v[104:107], v[60:63]\n"
    "v_mfma_f32_16x16x32_bf16 v[68:71], v[238:241], v[168:171], v[68:71]\n"
    "v_cvt_pk_bf16_f32 v224, v224, v225\n"
    "s_waitcnt lgkmcnt(1)\n"
    "v_mfma_f32_16x16x32_bf16 v[76:79], v[238:241], v[108:111], v[76:79]\n"
    "v_cvt_pk_bf16_f32 v225, v226, v227\n"
    "v_mfma_f32_16x16x32_bf16 v[24:27], v[242:245], v[88:91], v[24:27]\n"
    "v_cvt_pk_bf16_f32 v226, v228, v229\n"
    "v_mfma_f32_16x16x32_bf16 v[32:35], v[242:245], v[112:115], v[32:35]\n"
    "v_cvt_pk_bf16_f32 v227, v230, v231\n"
    "v_mfma_f32_16x16x32_bf16 v[40:43], v[242:245], v[184:187], v[40:43]\n"
    "v_mfma_f32_16x16x32_bf16 v[48:51], v[242:245], v[116:119], v[48:51]\n"
    "ds_read_b128 v[188:191], v5 offset:4928\n"
    "v_mfma_f32_16x16x32_bf16 v[56:59], v[242:245], v[120:123], v[56:59]\n"
    "ds_read_b64_tr_b16 v[248:249], v7 offset:36864\n"
    "ds_read_b64_tr_b16 v[250:251], v7 offset:37376\n"
    "v_mfma_f32_16x16x32_bf16 v[64:67], v[242:245], v[128:131], v[64:67]\n"
    "ds_read_b64_tr_b16 v[246:247], v6 offset:0\n"
    "ds_read_b64_tr_b16 v[252:253], v6 offset:512\n"
    "v_mfma_f32_16x16x32_bf16 v[72:75], v[242:245], v[136:139], v[72:75]\n"
    "ds_read_b64_tr_b16 v[254:255], v6 offset:0\n"
    "s_waitcnt lgkmcnt(6)\n"
    "v_mfma_f32_16x16x32_bf16 v[80:83], v[242:245], v[144:147], v[80:83]\n"
    "v_mfma_f32_16x16x32_bf16 v[192:195], v[224:227], v[92:95], v[192:195]\n"
    "v_mfma_f32_16x16x32_bf16 v[196:199], v[224:227], v[124:127], v[196:199]\n"
    "v_mfma_f32_16x16x32_bf16 v[200:203], v[224:227], v[132:135], v[200:203]\n"
    "v_mfma_f32_16x16x32_bf16 v[204:207], v[224:227], v[140:143], v[204:207]\n"
    "v_mfma_f32_16x16x32_bf16 v[208:211], v[224:227], v[148:151], v[208:211]\n"
    "v_mfma_f32_16x16x32_bf16 v[212:215], v[224:227], v[156:159], v[212:215]\n"
    "ds_read_b128 v[176:179], v13 offset:2560\n"
    "v_mfma_f32_16x16x32_bf16 v[216:219], v[224:227], v[172:175], v[216:219]\n"
    "s_waitcnt lgkmcnt(1)\n"
    "v_mfma_f32_16x16x32_bf16 v[220:223], v[224:227], v[188:191], v[220:223]\n"
    "v_mfma_f32_16x16x32_bf16 v[224:227], v[232:235], v[246:249], 0\n"
    "v_mfma_f32_16x16x32_bf16 v[228:231], v[232:235], v[250:253], 0\n"
    "v_mfma_f32_16x16x32_bf16 v[20:23], v[248:251], v[84:87], v[20:23]\n"
    "v_mfma_f32_16x16x32_bf16 v[28:31], v[248:251], v[96:99], v[28:31]\n"
    "v_mfma_f32_16x16x32_bf16 v[36:39], v[248:251], v[100:103], v[36:39]\n"
    "v_mfma_f32_16x16x32_bf16 v[44:47], v[248:251], v[160:163], v[44:47]\n"
    "ds_read_b128 v[152:155], v13 offset:2624\n"
    "v_mfma_f32_16x16x32_bf16 v[52:55], v[248:251], v[104:107], v[52:55]\n"
    "v_mfma_f32_16x16x32_bf16 v[60:63], v[248:251], v[168:171], v[60:63]\n"
    "v_mfma_f32_16x16x32_bf16 v[68:71], v[248:251], v[108:111], v[68:71]\n"
    "v_cvt_pk_bf16_f32 v224, v224, v225\n"
    "s_waitcnt lgkmcnt(1)\n"
    "v_mfma_f32_16x16x32_bf16 v[76:79], v[248:251], v[176:179], v[76:79]\n"
    "v_cvt_pk_bf16_f32 v225, v226, v227\n"
    "v_mfma_f32_16x16x32_bf16 v[24:27], v[252:255], v[112:115], v[24:27]\n"
    "v_cvt_pk_bf16_f32 v226, v228, v229\n"
    "v_mfma_f32_16x16x32_bf16 v[32:35], v[252:255], v[184:187], v[32:35]\n"
    "v_cvt_pk_bf16_f32 v227, v230, v231\n"
    "v_mfma_f32_16x16x32_bf16 v[40:43], v[252:255], v[116:119], v[40:43]\n"
    "v_mfma_f32_16x16x32_bf16 v[48:51], v[252:255], v[120:123], v[48:51]\n"
    "ds_read_b128 v[164:167], v5 offset:5184\n"
    "v_mfma_f32_16x16x32_bf16 v[56:59], v[252:255], v[128:131], v[56:59]\n"
    "s_barrier\n"
    "ds_read_b64_tr_b16 v[238:239], v6 offset:4096\n"
    "v_mfma_f32_16x16x32_bf16 v[64:67], v[252:255], v[136:139], v[64:67]\n"
    "ds_read_b64_tr_b16 v[240:241], v6 offset:4608\n"
    "ds_read_b64_tr_b16 v[236:237], v6 offset:8192\n"
    "v_mfma_f32_16x16x32_bf16 v[72:75], v[252:255], v[144:147], v[72:75]\n"
    "ds_read_b64_tr_b16 v[242:243], v6 offset:8704\n"
    "ds_read_b64_tr_b16 v[244:245], v6 offset:8192\n"
    "s_waitcnt lgkmcnt(6)\n"
    "v_mfma_f32_16x16x32_bf16 v[80:83], v[252:255], v[152:155], v[80:83]\n"
    "v_mfma_f32_16x16x32_bf16 v[192:195], v[224:227], v[124:127], v[192:195]\n"
    "v_mfma_f32_16x16x32_bf16 v[196:199], v[224:227], v[132:135], v[196:199]\n"
    "v_mfma_f32_16x16x32_bf16 v[200:203], v[224:227], v[140:143], v[200:203]\n"
    "v_mfma_f32_16x16x32_bf16 v[204:207], v[224:227], v[148:151], v[204:207]\n"
    "v_mfma_f32_16x16x32_bf16 v[208:211], v[224:227], v[156:159], v[208:211]\n"
    "v_mfma_f32_16x16x32_bf16 v[212:215], v[224:227], v[172:175], v[212:215]\n"
    "ds_read_b128 v[180:183], v13 offset:2688\n"
    "v_mfma_f32_16x16x32_bf16 v[216:219], v[224:227], v[188:191], v[216:219]\n"
    "s_waitcnt lgkmcnt(1)\n"
    "v_mfma_f32_16x16x32_bf16 v[220:223], v[224:227], v[164:167], v[220:223]\n"
    "v_mfma_f32_16x16x32_bf16 v[224:227], v[232:235], v[236:239], 0\n"
    "v_mfma_f32_16x16x32_bf16 v[228:231], v[232:235], v[240:243], 0\n"
    "v_mfma_f32_16x16x32_bf16 v[20:23], v[238:241], v[96:99], v[20:23]\n"
    "v_mfma_f32_16x16x32_bf16 v[28:31], v[238:241], v[100:103], v[28:31]\n"
    "v_mfma_f32_16x16x32_bf16 v[36:39], v[238:241], v[160:163], v[36:39]\n"
    "v_mfma_f32_16x16x32_bf16 v[44:47], v[238:241], v[104:107], v[44:47]\n"
    "ds_read_b128 v[88:91], v13 offset:2752\n"
    "v_mfma_f32_16x16x32_bf16 v[52:55], v[238:241], v[168:171], v[52:55]\n"
    "v_mfma_f32_16x16x32_bf16 v[60:63], v[238:241], v[108:111], v[60:63]\n"
    "v_mfma_f32_16x16x32_bf16 v[68:71], v[238:241], v[176:179], v[68:71]\n"
    "v_cvt_pk_bf16_f32 v224, v224, v225\n"
    "s_waitcnt lgkmcnt(1)\n"
    "v_mfma_f32_16x16x32_bf16 v[76:79], v[238:241], v[180:183], v[76:79]\n"
    "v_cvt_pk_bf16_f32 v225, v226, v227\n"
    "v_mfma_f32_16x16x32_bf16 v[24:27], v[242:245], v[184:187], v[24:27]\n"
    "v_cvt_pk_bf16_f32 v226, v228, v229\n"
    "v_mfma_f32_16x16x32_bf16 v[32:35], v[242:245], v[116:119], v[32:35]\n"
    "v_cvt_pk_bf16_f32 v227, v230, v231\n"
    "v_mfma_f32_16x16x32_bf16 v[40:43], v[242:245], v[120:123], v[40:43]\n"
    "v_mfma_f32_16x16x32_bf16 v[48:51], v[242:245], v[128:131], v[48:51]\n"
    "ds_read_b128 v[92:95], v5 offset:5440\n"
    "v_mfma_f32_16x16x32_bf16 v[56:59], v[242:245], v[136:139], v[56:59]\n"
    "ds_read_b64_tr_b16 v[248:249], v6 offset:12288\n"
    "ds_read_b64_tr_b16 v[250:251], v6 offset:12800\n"
    "v_mfma_f32_16x16x32_bf16 v[64:67], v[242:245], v[144:147], v[64:67]\n"
    "ds_read_b64_tr_b16 v[246:247], v6 offset:16384\n"
    "ds_read_b64_tr_b16 v[252:253], v6 offset:16896\n"
    "v_mfma_f32_16x16x32_bf16 v[72:75], v[242:245], v[152:155], v[72:75]\n"
    "ds_read_b64_tr_b16 v[254:255], v6 offset:16384\n"
    "s_waitcnt lgkmcnt(6)\n"
    "v_mfma_f32_16x16x32_bf16 v[80:83], v[242:245], v[88:91], v[80:83]\n"
    "v_mfma_f32_16x16x32_bf16 v[192:195], v[224:227], v[132:135], v[192:195]\n"
    "v_mfma_f32_16x16x32_bf16 v[196:199], v[224:227], v[140:143], v[196:199]\n"
    "v_mfma_f32_16x16x32_bf16 v[200:203], v[224:227], v[148:151], v[200:203]\n"
    "v_mfma_f32_16x16x32_bf16 v[204:207], v[224:227], v[156:159], v[204:207]\n"
    "v_mfma_f32_16x16x32_bf16 v[208:211], v[224:227], v[172:175], v[208:211]\n"
    "v_mfma_f32_16x16x32_bf16 v[212:215], v[224:227], v[188:191], v[212:215]\n"
    "ds_read_b128 v[84:87], v13 offset:2816\n"
    "v_mfma_f32_16x16x32_bf16 v[216:219], v[224:227], v[164:167], v[216:219]\n"
    "s_waitcnt lgkmcnt(1)\n"
    "v_mfma_f32_16x16x32_bf16 v[220:223], v[224:227], v[92:95], v[220:223]\n"
    "v_mfma_f32_16x16x32_bf16 v[224:227], v[232:235], v[246:249], 0\n"
    "v_mfma_f32_16x16x32_bf16 v[228:231], v[232:235], v[250:253], 0\n"
    "v_mfma_f32_16x16x32_bf16 v[20:23], v[248:251], v[100:103], v[20:23]\n"
    "v_mfma_f32_16x16x32_bf16 v[28:31], v[248:251], v[160:163], v[28:31]\n"
    "v_mfma_f32_16x16x32_bf16 v[36:39], v[248:251], v[104:107], v[36:39]\n"
    "v_mfma_f32_16x16x32_bf16 v[44:47], v[248:251], v[168:171], v[44:47]\n"
    "ds_read_b128 v[112:115], v13 offset:2880\n"
    "v_mfma_f32_16x16x32_bf16 v[52:55], v[248:251], v[108:111], v[52:55]\n"
    "v_mfma_f32_16x16x32_bf16 v[60:63], v[248:251], v[176:179], v[60:63]\n"
    "v_mfma_f32_16x16x32_bf16 v[68:71], v[248:251], v[180:183], v[68:71]\n"
    "v_cvt_pk_bf16_f32 v224, v224, v225\n"
    "s_waitcnt lgkmcnt(1)\n"
    "v_mfma_f32_16x16x32_bf16 v[76:79], v[248:251], v[84:87], v[76:79]\n"
    "v_cvt_pk_bf16_f32 v225, v226, v227\n"
    "v_mfma_f32_16x16x32_bf16 v[24:27], v[252:255], v[116:119], v[24:27]\n"
    "v_cvt_pk_bf16_f32 v226, v228, v229\n"
    "v_mfma_f32_16x16x32_bf16 v[32:35], v[252:255], v[120:123], v[32:35]\n"
    "v_cvt_pk_bf16_f32 v227, v230, v231\n"
    "v_mfma_f32_16x16x32_bf16 v[40:43], v[252:255], v[128:131], v[40:43]\n"
    "s_cmp_ge_u32 s39, 6\n"
    "s_cbranch_scc1 .Lt6_2%=\n"
    "s_cmp_ge_u32 s39, 4\n"
    "s_cbranch_scc1 .Lt4_2%=\n"
    "s_cmp_ge_u32 s39, 2\n"
    "s_cbranch_scc1 .Lt2_2%=\n"
    ".Lt0_2%=:\n"
    "v_mfma_f32_16x16x32_bf16 v[48:51], v[252:255], v[136:139], v[48:51]\n"
    "ds_read_b128 v[124:127], v5 offset:5696\n"
    "v_mfma_f32_16x16x32_bf16 v[56:59], v[252:255], v[144:147], v[56:59]\n"
    "s_barrier\n"
    "ds_read_b64_tr_b16 v[96:97], v6 offset:20480\n"
    "v_mfma_f32_16x16x32_bf16 v[64:67], v[252:255], v[152:155], v[64:67]\n"
    "ds_read_b64_tr_b16 v[98:99], v6 offset:20992\n"
    "v_mfma_f32_16x16x32_bf16 v[72:75], v[252:255], v[88:91], v[72:75]\n"
    "ds_read_b128 v[184:187], v5 offset:1600\n"
    "s_waitcnt lgkmcnt(4)\n"
    "v_mfma_f32_16x16x32_bf16 v[80:83], v[252:255], v[112:115], v[80:83]\n"
    "ds_read_b128 v[132:135], v5 offset:1728\n"
    "v_mfma_f32_16x16x32_bf16 v[192:195], v[224:227], v[140:143], v[192:195]\n"
    "ds_read_b128 v[100:103], v5 offset:1856\n"
    "v_mfma_f32_16x16x32_bf16 v[196:199], v[224:227], v[148:151], v[196:199]\n"
    "ds_read_b128 v[160:163], v5 offset:1984\n"
    "ds_read_b64_tr_b16 v[104:105], v6 offset:24576\n"
    "v_mfma_f32_16x16x32_bf16 v[200:203], v[224:227], v[156:159], v[200:203]\n"
    "ds_read_b64_tr_b16 v[106:107], v6 offset:25088\n"
    "v_mfma_f32_16x16x32_bf16 v[204:207], v[224:227], v[172:175], v[204:207]\n"
    "ds_read_b128 v[168:171], v5 offset:2144\n"
    "v_mfma_f32_16x16x32_bf16 v[208:211], v[224:227], v[188:191], v[208:211]\n"
    "ds_read_b64_tr_b16 v[108:109], v6 offset:28672\n"
    "ds_read_b64_tr_b16 v[110:111], v6 offset:29184\n"
    "v_mfma_f32_16x16x32_bf16 v[212:215], v[224:227], v[164:167], v[212:215]\n"
    "v_add_f32 v20, v20, v192\n"
    "v_mfma_f32_16x16x32_bf16 v[216:219], v[224:227], v[92:95], v[216:219]\n"
    "v_add_f32 v21, v21, v193\n"
    "s_waitcnt lgkmcnt(7)\n"
    "v_mfma_f32_16x16x32_bf16 v[220:223], v[224:227], v[124:127], v[220:223]\n"
    "ds_read_b128 v[176:179], v5 offset:2272\n"
    "v_mfma_f32_16x16x32_bf16 v[68:71], v[96:99], v[184:187], v[68:71]\n"
    "ds_read_b64_tr_b16 v[180:181], v6 offset:32768\n"
    "ds_read_b64_tr_b16 v[182:183], v6 offset:33280\n"
    "v_mfma_f32_16x16x32_bf16 v[72:75], v[96:99], v[132:135], v[72:75]\n"
    "v_add_f32 v22, v22, v194\n"
    "s_waitcnt lgkmcnt(6)\n"
    "v_mfma_f32_16x16x32_bf16 v[76:79], v[96:99], v[100:103], v[76:79]\n"
    "v_add_f32 v23, v23, v195\n"
    "v_mfma_f32_16x16x32_bf16 v[80:83], v[96:99], v[160:163], v[80:83]\n"
    "ds_read_b128 v[84:87], v5 offset:2400\n"
    "v_mfma_f32_16x16x32_bf16 v[68:71], v[104:107], v[132:135], v[68:71]\n"
    "ds_read_b64_tr_b16 v[116:117], v6 offset:36864\n"
    "ds_read_b64_tr_b16 v[118:119], v6 offset:37376\n"
    "v_mfma_f32_16x16x32_bf16 v[72:75], v[104:107], v[100:103], v[72:75]\n"
    "v_add_f32 v24, v24, v192\n"
    "v_mfma_f32_16x16x32_bf16 v[76:79], v[104:107], v[160:163], v[76:79]\n"
    "v_add_f32 v25, v25, v193\n"
    "s_waitcnt lgkmcnt(5)\n"
    "v_mfma_f32_16x16x32_bf16 v[80:83], v[104:107], v[168:171], v[80:83]\n"
    "ds_read_b128 v[120:123], v5 offset:2528\n"
    "v_mfma_f32_16x16x32_bf16 v[68:71], v[108:111], v[100:103], v[68:71]\n"
    "ds_read_b64_tr_b16 v[128:129], v6 offset:40960\n"
    "ds_read_b64_tr_b16 v[130:131], v6 offset:41472\n"
    "v_mfma_f32_16x16x32_bf16 v[72:75], v[108:111], v[160:163], v[72:75]\n"
    "v_add_f32 v26, v26, v194\n"
    "v_mfma_f32_16x16x32_bf16 v[76:79], v[108:111], v[168:171], v[76:79]\n"
    "v_add_f32 v27, v27, v195\n"
    "v_mfma_f32_16x16x32_bf16 v[80:83], v[108:111], v[176:179], v[80:83]\n"
    "ds_read_b128 v[136:139], v5 offset:2656\n"
    "s_waitcnt lgkmcnt(4)\n"
    "v_mfma_f32_16x16x32_bf16 v[68:71], v[180:183], v[160:163], v[68:71]\n"
    "ds_read_b64_tr_b16 v[144:145], v6 offset:45056\n"
    "ds_read_b64_tr_b16 v[146:147], v6 offset:45568\n"
    "v_mfma_f32_16x16x32_bf16 v[72:75], v[180:183], v[168:171], v[72:75]\n"
    "v_add_f32 v28, v28, v196\n"
    "v_mfma_f32_16x16x32_bf16 v[76:79], v[180:183], v[176:179], v[76:79]\n"
    "v_add_f32 v29, v29, v197\n"
    "v_mfma_f32_16x16x32_bf16 v[80:83], v[180:183], v[84:87], v[80:83]\n"
    "ds_read_b128 v[152:155], v5 offset:2784\n"
    "v_mfma_f32_16x16x32_bf16 v[68:71], v[116:119], v[168:171], v[68:71]\n"
    "ds_read_b64_tr_b16 v[88:89], v6 offset:49152\n"
    "ds_read_b64_tr_b16 v[90:91], v6 offset:49664\n"
    "v_mfma_f32_16x16x32_bf16 v[72:75], v[116:119], v[176:179], v[72:75]\n"
    "v_add_f32 v30, v30, v198\n"
    "v_mfma_f32_16x16x32_bf16 v[76:79], v[116:119], v[84:87], v[76:79]\n"
    "v_add_f32 v31, v31, v199\n"
    "s_waitcnt lgkmcnt(5)\n"
    "v_mfma_f32_16x16x32_bf16 v[80:83], v[116:119], v[120:123], v[80:83]\n"
    "ds_read_b128 v[112:115], v5 offset:2912\n"
    "v_mfma_f32_16x16x32_bf16 v[68:71], v[128:131], v[176:179], v[68:71]\n"
    "ds_read_b64_tr_b16 v[140:141], v6 offset:53248\n"
    "ds_read_b64_tr_b16 v[142:143], v6 offset:53760\n"
    "v_mfma_f32_16x16x32_bf16 v[72:75], v[128:131], v[84:87], v[72:75]\n"
    "v_add_f32 v32, v32, v196\n"
    "v_mfma_f32_16x16x32_bf16 v[76:79], v[128:131], v[120:123], v[76:79]\n"
    "v_add_f32 v33, v33, v197\n"
    "v_mfma_f32_16x16x32_bf16 v[80:83], v[128:131], v[136:139], v[80:83]\n"
    "ds_read_b128 v[148:151], v5 offset:3040\n"
    "s_waitcnt lgkmcnt(4)\n"
    "v_mfma_f32_16x16x32_bf16 v[68:71], v[144:147], v[84:87], v[68:71]\n"
    "s_barrier\n"
    "ds_read_b64_tr_b16 v[156:157], v6 offset:57344\n"
    "v_mfma_f32_16x16x32_bf16 v[72:75], v[144:147], v[120:123], v[72:75]\n"
    "ds_read_b64_tr_b16 v[158:159], v6 offset:57856\n"
    "v_add_f32 v34, v34, v198\n"
    "v_mfma_f32_16x16x32_bf16 v[76:79], v[144:147], v[136:139], v[76:79]\n"
    "v_add_f32 v35, v35, v199\n"
    "v_mfma_f32_16x16x32_bf16 v[80:83], v[144:147], v[152:155], v[80:83]\n"
    "ds_read_b128 v[172:175], v5 offset:3168\n"
    "v_mfma_f32_16x16x32_bf16 v[68:71], v[88:91], v[120:123], v[68:71]\n"
    "ds_read_b64_tr_b16 v[188:189], v6 offset:61440\n"
    "ds_read_b64_tr_b16 v[190:191], v6 offset:61952\n"
    "v_mfma_f32_16x16x32_bf16 v[72:75], v[88:91], v[136:139], v[72:75]\n"
    "v_add_f32 v36, v36, v200\n"
    "v_mfma_f32_16x16x32_bf16 v[76:79], v[88:91], v[152:155], v[76:79]\n"
    "v_add_f32 v37, v37, v201\n"
    "s_waitcnt lgkmcnt(5)\n"
    "v_mfma_f32_16x16x32_bf16 v[80:83], v[88:91], v[112:115], v[80:83]\n"
    "ds_read_b128 v[164:167], v5 offset:3296\n"
    "v_mfma_f32_16x16x32_bf16 v[68:71], v[140:143], v[136:139], v[68:71]\n"
    "ds_read_b64_tr_b16 v[92:93], v7 offset:0\n"
    "ds_read_b64_tr_b16 v[94:95], v7 offset:512\n"
    "v_mfma_f32_16x16x32_bf16 v[72:75], v[140:143], v[152:155], v[72:75]\n"
    "v_add_f32 v38, v38, v202\n"
    "v_mfma_f32_16x16x32_bf16 v[76:79], v[140:143], v[112:115], v[76:79]\n"
    "v_add_f32 v39, v39, v203\n"
    "v_mfma_f32_16x16x32_bf16 v[80:83], v[140:143], v[148:151], v[80:83]\n"
    "ds_read_b128 v[124:127], v5 offset:3424\n"
    "s_waitcnt lgkmcnt(4)\n"
    "v_mfma_f32_16x16x32_bf16 v[68:71], v[156:159], v[152:155], v[68:71]\n"
    "ds_read_b64_tr_b16 v[224:225], v7 offset:4096\n"
    "ds_read_b64_tr_b16 v[226:227], v7 offset:4608\n"
    "v_mfma_f32_16x16x32_bf16 v[72:75], v[156:159], v[112:115], v[72:75]\n"
    "v_add_f32 v40, v40, v200\n"
    "v_mfma_f32_16x16x32_bf16 v[76:79], v[156:159], v[148:151], v[76:79]\n"
    "v_add_f32 v41, v41, v201\n"
    "v_mfma_f32_16x16x32_bf16 v[80:83], v[156:159], v[172:175], v[80:83]\n"
    "ds_read_b128 v[228:231], v5 offset:3552\n"
    "v_mfma_f32_16x16x32_bf16 v[68:71], v[188:191], v[112:115], v[68:71]\n"
    "ds_read_b64_tr_b16 v[232:233], v7 offset:8192\n"
    "ds_read_b64_tr_b16 v[234:235], v7 offset:8704\n"
    "v_mfma_f32_16x16x32_bf16 v[72:75], v[188:191], v[148:151], v[72:75]\n"
    "v_add_f32 v42, v42, v202\n"
    "v_mfma_f32_16x16x32_bf16 v[76:79], v[188:191], v[172:175], v[76:79]\n"
    "v_add_f32 v43, v43, v203\n"
    "s_waitcnt lgkmcnt(5)\n"
    "v_mfma_f32_16x16x32_bf16 v[80:83], v[188:191], v[164:167], v[80:83]\n"
    "ds_read_b128 v[236:239], v5 offset:3680\n"
    "v_mfma_f32_16x16x32_bf16 v[68:71], v[92:95], v[148:151], v[68:71]\n"
    "ds_read_b64_tr_b16 v[240:241], v7 offset:12288\n"
    "ds_read_b64_tr_b16 v[242:243], v7 offset:12800\n"
    "v_mfma_f32_16x16x32_bf16 v[72:75], v[92:95], v[172:175], v[72:75]\n"
    "v_add_f32 v44, v44, v204\n"
    "v_mfma_f32_16x16x32_bf16 v[76:79], v[92:95], v[164:167], v[76:79]\n"
    "v_add_f32 v45, v45, v205\n"
    "v_mfma_f32_16x16x32_bf16 v[80:83], v[92:95], v[124:127], v[80:83]\n"
    "ds_read_b128 v[244:247], v5 offset:3808\n"
    "s_waitcnt lgkmcnt(4)\n"
    "v_mfma_f32_16x16x32_bf16 v[68:71], v[224:227], v[172:175], v[68:71]\n"
    "ds_read_b64_tr_b16 v[248:249], v7 offset:16384\n"
    "ds_read_b64_tr_b16 v[250:251], v7 offset:16896\n"
    "v_mfma_f32_16x16x32_bf16 v[72:75], v[224:227], v[164:167], v[72:75]\n"
    "v_add_f32 v46, v46, v206\n"
    "v_mfma_f32_16x16x32_bf16 v[76:79], v[224:227], v[124:127], v[76:79]\n"
    "v_add_f32 v47, v47, v207\n"
    "v_mfma_f32_16x16x32_bf16 v[80:83], v[224:227], v[228:231], v[80:83]\n"
    "ds_read_b128 v[252:255], v5 offset:3936\n"
    "v_mfma_f32_16x16x32_bf16 v[68:71], v[232:235], v[164:167], v[68:71]\n"
    "ds_read_b64_tr_b16 v[184:185], v7 offset:20480\n"
    "ds_read_b64_tr_b16 v[186:187], v7 offset:20992\n"
    "v_mfma_f32_16x16x32_bf16 v[72:75], v[232:235], v[124:127], v[72:75]\n"
    "v_add_f32 v48, v48, v204\n"
    "v_mfma_f32_16x16x32_bf16 v[76:79], v[232:235], v[228:231], v[76:79]\n"
    "v_add_f32 v49, v49, v205\n"
    "s_waitcnt lgkmcnt(5)\n"
    "v_mfma_f32_16x16x32_bf16 v[80:83], v[232:235], v[236:239], v[80:83]\n"
    "ds_read_b128 v[132:135], v5 offset:4064\n"
    "v_mfma_f32_16x16x32_bf16 v[68:71], v[240:243], v[124:127], v[68:71]\n"
    "ds_read_b64_tr_b16 v[100:101], v7 offset:24576\n"
    "ds_read_b64_tr_b16 v[102:103], v7 offset:25088\n"
    "v_mfma_f32_16x16x32_bf16 v[72:75], v[240:243], v[228:231], v[72:75]\n"
    "v_add_f32 v50, v50, v206\n"
    "v_mfma_f32_16x16x32_bf16 v[76:79], v[240:243], v[236:239], v[76:79]\n"
    "v_add_f32 v51, v51, v207\n"
    "v_mfma_f32_16x16x32_bf16 v[80:83], v[240:243], v[244:247], v[80:83]\n"
    "ds_read_b128 v[192:195], v5 offset:4224\n"
    "s_waitcnt lgkmcnt(4)\n"
    "v_mfma_f32_16x16x32_bf16 v[68:71], v[248:251], v[228:231], v[68:71]\n"
    "ds_read_b64_tr_b16 v[160:161], v7 offset:28672\n"
    "ds_read_b64_tr_b16 v[162:163], v7 offset:29184\n"
    "v_mfma_f32_16x16x32_bf16 v[72:75], v[248:251], v[236:239], v[72:75]\n"
    "v_add_f32 v52, v52, v208\n"
    "v_mfma_f32_16x16x32_bf16 v[76:79], v[248:251], v[244:247], v[76:79]\n"
    "v_add_f32 v53, v53, v209\n"
    "v_mfma_f32_16x16x32_bf16 v[80:83], v[248:251], v[252:255], v[80:83]\n"
    "ds_read_b128 v[168:171], v5 offset:4352\n"
    "v_mfma_f32_16x16x32_bf16 v[68:71], v[184:187], v[236:239], v[68:71]\n"
    "s_barrier\n"
    "ds_read_b64_tr_b16 v[176:177], v7 offset:32768\n"
    "v_mfma_f32_16x16x32_bf16 v[72:75], v[184:187], v[244:247], v[72:75]\n"
    "ds_read_b64_tr_b16 v[178:179], v7 offset:33280\n"
    "v_add_f32 v54, v54, v210\n"
    "v_mfma_f32_16x16x32_bf16 v[76:79], v[184:187], v[252:255], v[76:79]\n"
    "v_add_f32 v55, v55, v211\n"
    "s_waitcnt lgkmcnt(5)\n"
    "v_mfma_f32_16x16x32_bf16 v[80:83], v[184:187], v[132:135], v[80:83]\n"
    "ds_read_b128 v[84:87], v5 offset:4480\n"
    "v_mfma_f32_16x16x32_bf16 v[68:71], v[100:103], v[244:247], v[68:71]\n"
    "ds_read_b64_tr_b16 v[196:197], v7 offset:36864\n"
    "ds_read_b64_tr_b16 v[198:199], v7 offset:37376\n"
    "v_mfma_f32_16x16x32_bf16 v[72:75], v[100:103], v[252:255], v[72:75]\n"
    "v_add_f32 v56, v56, v208\n"
    "v_mfma_f32_16x16x32_bf16 v[76:79], v[100:103], v[132:135], v[76:79]\n"
    "v_add_f32 v57, v57, v209\n"
    "v_mfma_f32_16x16x32_bf16 v[80:83], v[100:103], v[192:195], v[80:83]\n"
    "ds_read_b128 v[120:123], v5 offset:4608\n"
    "s_waitcnt lgkmcnt(5)\n"
    "v_mfma_f32_16x16x32_bf16 v[68:71], v[160:163], v[252:255], v[68:71]\n"
    "ds_read_b64_tr_b16 v[136:137], v6 offset:0\n"
    "ds_read_b64_tr_b16 v[138:139], v6 offset:512\n"
    "v_mfma_f32_16x16x32_bf16 v[72:75], v[160:163], v[132:135], v[72:75]\n"
    "v_add_f32 v58, v58, v210\n"
    "v_mfma_f32_16x16x32_bf16 v[76:79], v[160:163], v[192:195], v[76:79]\n"
    "v_add_f32 v59, v59, v211\n"
    "v_mfma_f32_16x16x32_bf16 v[80:83], v[160:163], v[168:171], v[80:83]\n"
    "ds_read_b128 v[152:155], v5 offset:4736\n"
    "s_waitcnt lgkmcnt(4)\n"
    "v_mfma_f32_16x16x32_bf16 v[68:71], v[176:179], v[132:135], v[68:71]\n"
    "ds_read_b64_tr_b16 v[112:113], v6 offset:4096\n"
    "ds_read_b64_tr_b16 v[114:115], v6 offset:4608\n"
    "v_mfma_f32_16x16x32_bf16 v[72:75], v[176:179], v[192:195], v[72:75]\n"
    "v_add_f32 v60, v60, v212\n"
    "v_mfma_f32_16x16x32_bf16 v[76:79], v[176:179], v[168:171], v[76:79]\n"
    "v_add_f32 v61, v61, v213\n"
    "v_mfma_f32_16x16x32_bf16 v[80:83], v[176:179], v[84:87], v[80:83]\n"
    "ds_read_b128 v[200:203], v5 offset:4864\n"
    "v_mfma_f32_16x16x32_bf16 v[68:71], v[196:199], v[192:195], v[68:71]\n"
    "ds_read_b64_tr_b16 v[148:149], v6 offset:8192\n"
    "ds_read_b64_tr_b16 v[150:151], v6 offset:8704\n"
    "v_mfma_f32_16x16x32_bf16 v[72:75], v[196:199], v[168:171], v[72:75]\n"
    "v_add_f32 v62, v62, v214\n"
    "v_mfma_f32_16x16x32_bf16 v[76:79], v[196:199], v[84:87], v[76:79]\n"
    "v_add_f32 v63, v63, v215\n"
    "s_waitcnt lgkmcnt(5)\n"
    "v_mfma_f32_16x16x32_bf16 v[80:83], v[196:199], v[120:123], v[80:83]\n"
    "ds_read_b128 v[172:175], v5 offset:4992\n"
    "v_mfma_f32_16x16x32_bf16 v[68:71], v[136:139], v[168:171], v[68:71]\n"
    "ds_read_b64_tr_b16 v[164:165], v6 offset:12288\n"
    "ds_read_b64_tr_b16 v[166:167], v6 offset:12800\n"
    "v_mfma_f32_16x16x32_bf16 v[72:75], v[136:139], v[84:87], v[72:75]\n"
    "v_add_f32 v64, v64, v212\n"
    "v_mfma_f32_16x16x32_bf16 v[76:79], v[136:139], v[120:123], v[76:79]\n"
    "v_add_f32 v65, v65, v213\n"
    "v_mfma_f32_16x16x32_bf16 v[80:83], v[136:139], v[152:155], v[80:83]\n"
    "ds_read_b128 v[124:127], v5 offset:5120\n"
    "s_waitcnt lgkmcnt(4)\n"
    "v_mfma_f32_16x16x32_bf16 v[68:71], v[112:115], v[84:87], v[68:71]\n"
    "ds_read_b64_tr_b16 v[204:205], v6 offset:16384\n"
    "ds_read_b64_tr_b16 v[206:207], v6 offset:16896\n"
    "v_mfma_f32_16x16x32_bf16 v[72:75], v[112:115], v[120:123], v[72:75]\n"
    "v_add_f32 v66, v66, v214\n"
    "v_mfma_f32_16x16x32_bf16 v[76:79], v[112:115], v[152:155], v[76:79]\n"
    "v_add_f32 v67, v67, v215\n"
    "v_mfma_f32_16x16x32_bf16 v[80:83], v[112:115], v[200:203], v[80:83]\n"
    "ds_read_b128 v[228:231], v5 offset:5248\n"
    "v_mfma_f32_16x16x32_bf16 v[68:71], v[148:151], v[120:123], v[68:71]\n"
    "s_barrier\n"
    "ds_read_b64_tr_b16 v[236:237], v6 offset:20480\n"
    "v_mfma_f32_16x16x32_bf16 v[72:75], v[148:151], v[152:155], v[72:75]\n"
    "ds_read_b64_tr_b16 v[238:239], v6 offset:20992\n"
    "v_mfma_f32_16x16x32_bf16 v[76:79], v[148:151], v[200:203], v[76:79]\n"
    "s_waitcnt lgkmcnt(5)\n"
    "v_mfma_f32_16x16x32_bf16 v[80:83], v[148:151], v[172:175], v[80:83]\n"
    "ds_read_b128 v[244:247], v5 offset:5376\n"
    "v_mfma_f32_16x16x32_bf16 v[68:71], v[164:167], v[152:155], v[68:71]\n"
    "ds_read_b64_tr_b16 v[252:253], v6 offset:24576\n"
    "ds_read_b64_tr_b16 v[254:255], v6 offset:25088\n"
    "v_mfma_f32_16x16x32_bf16 v[72:75], v[164:167], v[200:203], v[72:75]\n"
    "v_mfma_f32_16x16x32_bf16 v[76:79], v[164:167], v[172:175], v[76:79]\n"
    "v_mfma_f32_16x16x32_bf16 v[80:83], v[164:167], v[124:127], v[80:83]\n"
    "ds_read_b128 v[208:211], v5 offset:5504\n"
    "s_waitcnt lgkmcnt(5)\n"
    "v_mfma_f32_16x16x32_bf16 v[68:71], v[204:207], v[200:203], v[68:71]\n"
    "ds_read_b64_tr_b16 v[132:133], v6 offset:28672\n"
    "ds_read_b64_tr_b16 v[134:135], v6 offset:29184\n"
    "v_mfma_f32_16x16x32_bf16 v[72:75], v[204:207], v[172:175], v[72:75]\n"
    "v_mfma_f32_16x16x32_bf16 v[76:79], v[204:207], v[124:127], v[76:79]\n"
    "v_mfma_f32_16x16x32_bf16 v[80:83], v[204:207], v[228:231], v[80:83]\n"
    "ds_read_b128 v[192:195], v5 offset:5632\n"
    "s_waitcnt lgkmcnt(4)\n"
    "v_mfma_f32_16x16x32_bf16 v[68:71], v[236:239], v[172:175], v[68:71]\n"
    "ds_read_b64_tr_b16 v[168:169], v6 offset:32768\n"
    "ds_read_b64_tr_b16 v[170:171], v6 offset:33280\n"
    "v_mfma_f32_16x16x32_bf16 v[72:75], v[236:239], v[124:127], v[72:75]\n"
    "v_mfma_f32_16x16x32_bf16 v[76:79], v[236:239], v[228:231], v[76:79]\n"
    "v_mfma_f32_16x16x32_bf16 v[80:83], v[236:239], v[244:247], v[80:83]\n"
    "ds_read_b128 v[84:87], v5 offset:5760\n"
    "v_mfma_f32_16x16x32_bf16 v[68:71], v[252:255], v[124:127], v[68:71]\n"
    "ds_read_b64_tr_b16 v[212:213], v6 offset:36864\n"
    "ds_read_b64_tr_b16 v[214:215], v6 offset:37376\n"
    "v_mfma_f32_16x16x32_bf16 v[72:75], v[252:255], v[228:231], v[72:75]\n"
    "v_mfma_f32_16x16x32_bf16 v[76:79], v[252:255], v[244:247], v[76:79]\n"
    "s_waitcnt lgkmcnt(5)\n"
    "v_mfma_f32_16x16x32_bf16 v[80:83], v[252:255], v[208:211], v[80:83]\n"
    "ds_read_b128 v[120:123], v5 offset:5888\n"
    "v_mfma_f32_16x16x32_bf16 v[68:71], v[132:135], v[228:231], v[68:71]\n"
    "ds_read_b128 v[152:155], v5 offset:1088\n"
    "v_mfma_f32_16x16x32_bf16 v[72:75], v[132:135], v[244:247], v[72:75]\n"
    "ds_read_b128 v[200:203], v5 offset:1216\n"
    "v_mfma_f32_16x16x32_bf16 v[76:79], v[132:135], v[208:211], v[76:79]\n"
    "ds_read_b128 v[172:175], v5 offset:1344\n"
    "v_mfma_f32_16x16x32_bf16 v[80:83], v[132:135], v[192:195], v[80:83]\n"
    "ds_read_b128 v[124:127], v5 offset:1472\n"
    "s_waitcnt lgkmcnt(5)\n"
    "v_mfma_f32_16x16x32_bf16 v[68:71], v[168:171], v[244:247], v[68:71]\n"
    "v_mfma_f32_16x16x32_bf16 v[72:75], v[168:171], v[208:211], v[72:75]\n"
    "v_mfma_f32_16x16x32_bf16 v[76:79], v[168:171], v[192:195], v[76:79]\n"
    "v_mfma_f32_16x16x32_bf16 v[80:83], v[168:171], v[84:87], v[80:83]\n"
    "ds_read_b128 v[228:231], v5 offset:1600\n"
    "v_mfma_f32_16x16x32_bf16 v[68:71], v[212:215], v[208:211], v[68:71]\n"
    "v_mfma_f32_16x16x32_bf16 v[72:75], v[212:215], v[192:195], v[72:75]\n"
    "v_mfma_f32_16x16x32_bf16 v[76:79], v[212:215], v[84:87], v[76:79]\n"
    "s_waitcnt lgkmcnt(1)\n"
    "v_mfma_f32_16x16x32_bf16 v[80:83], v[212:215], v[120:123], v[80:83]\n"
    "ds_read_b128 v[244:247], v5 offset:1728\n"
    "v_mfma_f32_16x16x32_bf16 v[52:55], v[96:99], v[152:155], v[52:55]\n"
    "v_mfma_f32_16x16x32_bf16 v[56:59], v[96:99], v[200:203], v[56:59]\n"
    "v_mfma_f32_16x16x32_bf16 v[60:63], v[96:99], v[172:175], v[60:63]\n"
    "v_add_f32 v68, v68, v216\n"
    "v_add_f32 v69, v69, v217\n"
    "v_mfma_f32_16x16x32_bf16 v[64:67], v[96:99], v[124:127], v[64:67]\n"
    "ds_read_b128 v[208:211], v5 offset:1856\n"
    "v_mfma_f32_16x16x32_bf16 v[52:55], v[104:107], v[200:203], v[52:55]\n"
    "v_add_f32 v70, v70, v218\n"
    "v_add_f32 v71, v71, v219\n"
    "v_mfma_f32_16x16x32_bf16 v[56:59], v[104:107], v[172:175], v[56:59]\n"
    "v_add_f32 v72, v72, v216\n"
    "v_add_f32 v73, v73, v217\n"
    "v_mfma_f32_16x16x32_bf16 v[60:63], v[104:107], v[124:127], v[60:63]\n"
    "v_add_f32 v74, v74, v218\n"
    "v_add_f32 v75, v75, v219\n"
    "s_waitcnt lgkmcnt(1)\n"
    "v_mfma_f32_16x16x32_bf16 v[64:67], v[104:107], v[228:231], v[64:67]\n"
    "ds_read_b128 v[192:195], v5 offset:1984\n"
    "v_mfma_f32_16x16x32_bf16 v[52:55], v[108:111], v[172:175], v[52:55]\n"
    "v_add_f32 v76, v76, v220\n"
    "v_add_f32 v77, v77, v221\n"
    "v_mfma_f32_16x16x32_bf16 v[56:59], v[108:111], v[124:127], v[56:59]\n"
    "v_add_f32 v78, v78, v222\n"
    "v_add_f32 v79, v79, v223\n"
    "v_mfma_f32_16x16x32_bf16 v[60:63], v[108:111], v[228:231], v[60:63]\n"
    "v_add_f32 v80, v80, v220\n"
    "v_add_f32 v81, v81, v221\n"
    "v_mfma_f32_16x16x32_bf16 v[64:67], v[108:111], v[244:247], v[64:67]\n"
    "ds_read_b128 v[84:87], v5 offset:2144\n"
    "v_mfma_f32_16x16x32_bf16 v[52:55], v[180:183], v[124:127], v[52:55]\n"
    "v_add_f32 v82, v82, v222\n"
    "v_add_f32 v83, v83, v223\n"
    "v_mfma_f32_16x16x32_bf16 v[56:59], v[180:183], v[228:231], v[56:59]\n"
    "v_cvt_pk_bf16_f32 v68, v68, v69\n"
    "v_cvt_pk_bf16_f32 v69, v70, v71\n"
    "v_mfma_f32_16x16x32_bf16 v[60:63], v[180:183], v[244:247], v[60:63]\n"
    "v_cvt_pk_bf16_f32 v70, v72, v73\n"
    "v_cvt_pk_bf16_f32 v71, v74, v75\n"
    "s_waitcnt lgkmcnt(1)\n"
    "v_mfma_f32_16x16x32_bf16 v[64:67], v[180:183], v[208:211], v[64:67]\n"
    "ds_read_b128 v[120:123], v5 offset:2272\n"
    "v_mfma_f32_16x16x32_bf16 v[52:55], v[116:119], v[228:231], v[52:55]\n"
    "s_xor_b32 s43, s41, 221184\n"
    "buffer_store_dwordx4 v[68:71], v8, s[24:27], s43 offen sc1\n"
    "v_mfma_f32_16x16x32_bf16 v[56:59], v[116:119], v[244:247], v[56:59]\n"
    "v_cvt_pk_bf16_f32 v76, v76, v77\n"
    "v_cvt_pk_bf16_f32 v77, v78, v79\n"
    ".Lpad1%=:\n"
    "s_cbranch_execz .Lpad1r%=\n"
    "v_mfma_f32_16x16x32_bf16 v[60:63], v[116:119], v[208:211], v[60:63]\n"
    "v_cvt_pk_bf16_f32 v78, v80, v81\n"
    "v_cvt_pk_bf16_f32 v79, v82, v83\n"
    "v_mfma_f32_16x16x32_bf16 v[64:67], v[116:119], v[192:195], v[64:67]\n"
    "ds_read_b128 v[152:155], v5 offset:2400\n"
    "v_mfma_f32_16x16x32_bf16 v[52:55], v[128:131], v[244:247], v[52:55]\n"
    "s_xor_b32 s43, s41, 258048\n"
    "buffer_store_dwordx4 v[76:79], v8, s[24:27], s43 offen sc1\n"
    "v_mfma_f32_16x16x32_bf16 v[56:59], v[128:131], v[208:211], v[56:59]\n"
    "v_mfma_f32_16x16x32_bf16 v[60:63], v[128:131], v[192:195], v[60:63]\n"
    "s_waitcnt lgkmcnt(1)\n"
    "v_mfma_f32_16x16x32_bf16 v[64:67], v[128:131], v[84:87], v[64:67]\n"
    "s_barrier\n"
    "ds_read_b128 v[200:203], v5 offset:2528\n"
    "v_mfma_f32_16x16x32_bf16 v[52:55], v[144:147], v[208:211], v[52:55]\n"
    "v_mfma_f32_16x16x32_bf16 v[56:59], v[144:147], v[192:195], v[56:59]\n"
    "v_mfma_f32_16x16x32_bf16 v[60:63], v[144:147], v[84:87], v[60:63]\n"
    "v_mfma_f32_16x16x32_bf16 v[64:67], v[144:147], v[120:123], v[64:67]\n"
    "ds_read_b128 v[216:219], v5 offset:2656\n"
    "v_mfma_f32_16x16x32_bf16 v[52:55], v[88:91], v[192:195], v[52:55]\n"
    "v_mfma_f32_16x16x32_bf16 v[56:59], v[88:91], v[84:87], v[56:59]\n"
    "v_mfma_f32_16x16x32_bf16 v[60:63], v[88:91], v[120:123], v[60:63]\n"
    "s_waitcnt lgkmcnt(1)\n"
    "v_mfma_f32_16x16x32_bf16 v[64:67], v[88:91], v[152:155], v[64:67]\n"
    "ds_read_b128 v[172:175], v5 offset:2784\n"
    "v_mfma_f32_16x16x32_bf16 v[52:55], v[140:143], v[84:87], v[52:55]\n"
    "v_mfma_f32_16x16x32_bf16 v[56:59], v[140:143], v[120:123], v[56:59]\n"
    "v_mfma_f32_16x16x32_bf16 v[60:63], v[140:143], v[152:155], v[60:63]\n"
    "v_mfma_f32_16x16x32_bf16 v[64:67], v[140:143], v[200:203], v[64:67]\n"
    "ds_read_b128 v[124:127], v5 offset:2912\n"
    "v_mfma_f32_16x16x32_bf16 v[52:55], v[156:159], v[120:123], v[52:55]\n"
    "v_mfma_f32_16x16x32_bf16 v[56:59], v[156:159], v[152:155], v[56:59]\n"
    "v_mfma_f32_16x16x32_bf16 v[60:63], v[156:159], v[200:203], v[60:63]\n"
    "s_waitcnt lgkmcnt(1)\n"
    "v_mfma_f32_16x16x32_bf16 v[64:67], v[156:159], v[216:219], v[64:67]\n"
    "ds_read_b128 v[220:223], v5 offset:3040\n"
    "v_mfma_f32_16x16x32_bf16 v[52:55], v[188:191], v[152:155], v[52:55]\n"
    "v_mfma_f32_16x16x32_bf16 v[56:59], v[188:191], v[200:203], v[56:59]\n"
    "v_mfma_f32_16x16x32_bf16 v[60:63], v[188:191], v[216:219], v[60:63]\n"
    "v_mfma_f32_16x16x32_bf16 v[64:67], v[188:191], v[172:175], v[64:67]\n"
    "ds_read_b128 v[228:231], v5 offset:3168\n"
    "v_mfma_f32_16x16x32_bf16 v[52:55], v[92:95], v[200:203], v[52:55]\n"
    "v_mfma_f32_16x16x32_bf16 v[56:59], v[92:95], v[216:219], v[56:59]\n"
    "v_mfma_f32_16x16x32_bf16 v[60:63], v[92:95], v[172:175], v[60:63]\n"
    "s_waitcnt lgkmcnt(1)\n"
    "v_mfma_f32_16x16x32_bf16 v[64:67], v[92:95], v[124:127], v[64:67]\n"
    "ds_read_b128 v[244:247], v5 offset:3296\n"
    "v_mfma_f32_16x16x32_bf16 v[52:55], v[224:227], v[216:219], v[52:55]\n"
    "v_mfma_f32_16x16x32_bf16 v[56:59], v[224:227], v[172:175], v[56:59]\n"
    "v_mfma_f32_16x16x32_bf16 v[60:63], v[224:227], v[124:127], v[60:63]\n"
    "v_mfma_f32_16x16x32_bf16 v[64:67], v[224:227], v[220:223], v[64:67]\n"
    "ds_read_b128 v[208:211], v5 offset:3424\n"
    "v_mfma_f32_16x16x32_bf16 v[52:55], v[232:235], v[172:175], v[52:55]\n"
    "v_mfma_f32_16x16x32_bf16 v[56:59], v[232:235], v[124:127], v[56:59]\n"
    "v_mfma_f32_16x16x32_bf16 v[60:63], v[232:235], v[220:223], v[60:63]\n"
    "s_waitcnt lgkmcnt(1)\n"
    "v_mfma_f32_16x16x32_bf16 v[64:67], v[232:235], v[228:231], v[64:67]\n"
    "ds_read_b128 v[192:195], v5 offset:3552\n"
    "v_mfma_f32_16x16x32_bf16 v[52:55], v[240:243], v[124:127], v[52:55]\n"
    "v_mfma_f32_16x16x32_bf16 v[56:59], v[240:243], v[220:223], v[56:59]\n"
    "v_mfma_f32_16x16x32_bf16 v[60:63], v[240:243], v[228:231], v[60:63]\n"
    "v_mfma_f32_16x16x32_bf16 v[64:67], v[240:243], v[244:247], v[64:67]\n"
    "ds_read_b128 v[84:87], v5 offset:3680\n"
    "v_mfma_f32_16x16x32_bf16 v[52:55], v[248:251], v[220:223], v[52:55]\n"
    "v_mfma_f32_16x16x32_bf16 v[56:59], v[248:251], v[228:231], v[56:59]\n"
    "v_mfma_f32_16x16x32_bf16 v[60:63], v[248:251], v[244:247], v[60:63]\n"
    "s_waitcnt lgkmcnt(1)\n"
    "v_mfma_f32_16x16x32_bf16 v[64:67], v[248:251], v[208:211], v[64:67]\n"
    "ds_read_b128 v[120:123], v5 offset:3808\n"
    "v_mfma_f32_16x16x32_bf16 v[52:55], v[184:187], v[228:231], v[52:55]\n"
    "v_mfma_f32_16x16x32_bf16 v[56:59], v[184:187], v[244:247], v[56:59]\n"
    "v_mfma_f32_16x16x32_bf16 v[60:63], v[184:187], v[208:211], v[60:63]\n"
    "v_mfma_f32_16x16x32_bf16 v[64:67], v[184:187], v[192:195], v[64:67]\n"
    "ds_read_b128 v[152:155], v5 offset:3936\n"
    "v_mfma_f32_16x16x32_bf16 v[52:55], v[100:103], v[244:247], v[52:55]\n"
    "v_mfma_f32_16x16x32_bf16 v[56:59], v[100:103], v[208:211], v[56:59]\n"
    "v_mfma_f32_16x16x32_bf16 v[60:63], v[100:103], v[192:195], v[60:63]\n"
    "s_waitcnt lgkmcnt(1)\n"
    "v_mfma_f32_16x16x32_bf16 v[64:67], v[100:103], v[84:87], v[64:67]\n"
    "ds_read_b128 v[200:203], v5 offset:4064\n"
    "v_mfma_f32_16x16x32_bf16 v[52:55], v[160:163], v[208:211], v[52:55]\n"
    "v_mfma_f32_16x16x32_bf16 v[56:59], v[160:163], v[192:195], v[56:59]\n"
    "v_mfma_f32_16x16x32_bf16 v[60:63], v[160:163], v[84:87], v[60:63]\n"
    "v_mfma_f32_16x16x32_bf16 v[64:67], v[160:163], v[120:123], v[64:67]\n"
    "ds_read_b128 v[216:219], v5 offset:4224\n"
    "v_mfma_f32_16x16x32_bf16 v[52:55], v[176:179], v[192:195], v[52:55]\n"
    "v_mfma_f32_16x16x32_bf16 v[56:59], v[176:179], v[84:87], v[56:59]\n"
    "v_mfma_f32_16x16x32_bf16 v[60:63], v[176:179], v[120:123], v[60:63]\n"
    "s_waitcnt lgkmcnt(1)\n"
    "v_mfma_f32_16x16x32_bf16 v[64:67], v[176:179], v[152:155], v[64:67]\n"
    "ds_read_b128 v[172:175], v5 offset:4352\n"
    "v_mfma_f32_16x16x32_bf16 v[52:55], v[196:199], v[84:87], v[52:55]\n"
    "v_mfma_f32_16x16x32_bf16 v[56:59], v[196:199], v[120:123], v[56:59]\n"
    "v_mfma_f32_16x16x32_bf16 v[60:63], v[196:199], v[152:155], v[60:63]\n"
    "v_mfma_f32_16x16x32_bf16 v[64:67], v[196:199], v[200:203], v[64:67]\n"
    "ds_read_b128 v[124:127], v5 offset:4480\n"
    "v_mfma_f32_16x16x32_bf16 v[52:55], v[136:139], v[120:123], v[52:55]\n"
    "v_mfma_f32_16x16x32_bf16 v[56:59], v[136:139], v[152:155], v[56:59]\n"
    "v_mfma_f32_16x16x32_bf16 v[60:63], v[136:139], v[200:203], v[60:63]\n"
    "s_waitcnt lgkmcnt(1)\n"
    "v_mfma_f32_16x16x32_bf16 v[64:67], v[136:139], v[216:219], v[64:67]\n"
    "ds_read_b128 v[220:223], v5 offset:4608\n"
    "v_mfma_f32_16x16x32_bf16 v[52:55], v[112:115], v[152:155], v[52:55]\n"
    "v_mfma_f32_16x16x32_bf16 v[56:59], v[112:115], v[200:203], v[56:59]\n"
    "v_mfma_f32_16x16x32_bf16 v[60:63], v[112:115], v[216:219], v[60:63]\n"
    "v_mfma_f32_16x16x32_bf16 v[64:67], v[112:115], v[172:175], v[64:67]\n"
    "ds_read_b128 v[228:231], v5 offset:4736\n"
    "v_mfma_f32_16x16x32_bf16 v[52:55], v[148:151], v[200:203], v[52:55]\n"
    "v_mfma_f32_16x16x32_bf16 v[56:59], v[148:151], v[216:219], v[56:59]\n"
    "v_mfma_f32_16x16x32_bf16 v[60:63], v[148:151], v[172:175], v[60:63]\n"
    "s_waitcnt lgkmcnt(1)\n"
    "v_mfma_f32_16x16x32_bf16 v[64:67], v[148:151], v[124:127], v[64:67]\n"
    "ds_read_b128 v[244:247], v5 offset:4864\n"
    "v_mfma_f32_16x16x32_bf16 v[52:55], v[164:167], v[216:219], v[52:55]\n"
    "v_mfma_f32_16x16x32_bf16 v[56:59], v[164:167], v[172:175], v[56:59]\n"
    "v_mfma_f32_16x16x32_bf16 v[60:63], v[164:167], v[124:127], v[60:63]\n"
    "v_mfma_f32_16x16x32_bf16 v[64:67], v[164:167], v[220:223], v[64:67]\n"
    "s_waitcnt vmcnt(0)\n"
    "v_add_u32 v18, 1, v18\n"
    "ds_read_b128 v[208:211], v5 offset:4992\n"
    "v_mfma_f32_16x16x32_bf16 v[52:55], v[204:207], v[172:175], v[52:55]\n"
    "buffer_store_dword v18, v19, s[24:27], 0 offen sc1\n"
    "v_mfma_f32_16x16x32_bf16 v[56:59], v[204:207], v[124:127], v[56:59]\n"
    "v_mfma_f32_16x16x32_bf16 v[60:63], v[204:207], v[220:223], v[60:63]\n"
    "s_waitcnt lgkmcnt(1)\n"
    "v_mfma_f32_16x16x32_bf16 v[64:67], v[204:207], v[228:231], v[64:67]\n"
    "ds_read_b128 v[192:195], v5 offset:5120\n"
    "v_mfma_f32_16x16x32_bf16 v[52:55], v[236:239], v[124:127], v[52:55]\n"
    "v_mfma_f32_16x16x32_bf16 v[56:59], v[236:239], v[220:223], v[56:59]\n"
    "v_mfma_f32_16x16x32_bf16 v[60:63], v[236:239], v[228:231], v[60:63]\n"
    "v_mfma_f32_16x16x32_bf16 v[64:67], v[236:239], v[244:247], v[64:67]\n"
    "ds_read_b128 v[84:87], v5 offset:5248\n"
    "v_mfma_f32_16x16x32_bf16 v[52:55], v[252:255], v[220:223], v[52:55]\n"
    "v_mfma_f32_16x16x32_bf16 v[56:59], v[252:255], v[228:231], v[56:59]\n"
    "v_mfma_f32_16x16x32_bf16 v[60:63], v[252:255], v[244:247], v[60:63]\n"
    "s_waitcnt lgkmcnt(1)\n"
    "v_mfma_f32_16x16x32_bf16 v[64:67], v[252:255], v[208:211], v[64:67]\n"
    "ds_read_b128 v[120:123], v5 offset:5376\n"
    "v_mfma_f32_16x16x32_bf16 v[52:55], v[132:135], v[228:231], v[52:55]\n"
    "ds_read_b128 v[152:155], v5 offset:576\n"
    "v_mfma_f32_16x16x32_bf16 v[56:59], v[132:135], v[244:247], v[56:59]\n"
    "ds_read_b128 v[200:203], v5 offset:704\n"
    "v_mfma_f32_16x16x32_bf16 v[60:63], v[132:135], v[208:211], v[60:63]\n"
    "ds_read_b128 v[216:219], v5 offset:832\n"
    "v_mfma_f32_16x16x32_bf16 v[64:67], v[132:135], v[192:195], v[64:67]\n"
    "ds_read_b128 v[172:175], v5 offset:960\n"
    "v_mfma_f32_16x16x32_bf16 v[52:55], v[168:171], v[244:247], v[52:55]\n"
    "v_mfma_f32_16x16x32_bf16 v[56:59], v[168:171], v[208:211], v[56:59]\n"
    "v_mfma_f32_16x16x32_bf16 v[60:63], v[168:171], v[192:195], v[60:63]\n"
    "s_waitcnt lgkmcnt(4)\n"
    "v_mfma_f32_16x16x32_bf16 v[64:67], v[168:171], v[84:87], v[64:67]\n"
    "ds_read_b128 v[124:127], v5 offset:1088\n"
    "v_mfma_f32_16x16x32_bf16 v[52:55], v[212:215], v[208:211], v[52:55]\n"
    "v_mfma_f32_16x16x32_bf16 v[56:59], v[212:215], v[192:195], v[56:59]\n"
    "v_mfma_f32_16x16x32_bf16 v[60:63], v[212:215], v[84:87], v[60:63]\n"
    "v_mfma_f32_16x16x32_bf16 v[64:67], v[212:215], v[120:123], v[64:67]\n"
    "ds_read_b128 v[220:223], v5 offset:1216\n"
    "s_waitcnt lgkmcnt(2)\n"
    "v_mfma_f32_16x16x32_bf16 v[36:39], v[96:99], v[152:155], v[36:39]\n"
    "v_mfma_f32_16x16x32_bf16 v[40:43], v[96:99], v[200:203], v[40:43]\n"
    "v_mfma_f32_16x16x32_bf16 v[44:47], v[96:99], v[216:219], v[44:47]\n"
    "v_cvt_pk_bf16_f32 v52, v52, v53\n"
    "v_cvt_pk_bf16_f32 v53, v54, v55\n"
    "v_mfma_f32_16x16x32_bf16 v[48:51], v[96:99], v[172:175], v[48:51]\n"
    "ds_read_b128 v[228:231], v5 offset:1344\n"
    "v_mfma_f32_16x16x32_bf16 v[36:39], v[104:107], v[200:203], v[36:39]\n"
    "v_cvt_pk_bf16_f32 v54, v56, v57\n"
    "v_cvt_pk_bf16_f32 v55, v58, v59\n"
    "v_mfma_f32_16x16x32_bf16 v[40:43], v[104:107], v[216:219], v[40:43]\n"
    "v_min_u32 v52, -2, v52\n"
    "v_min_u32 v53, -2, v53\n"
    "v_mfma_f32_16x16x32_bf16 v[44:47], v[104:107], v[172:175], v[44:47]\n"
    "v_min_u32 v54, -2, v54\n"
    "v_min_u32 v55, -2, v55\n"
    "s_waitcnt lgkmcnt(1)\n"
    "v_mfma_f32_16x16x32_bf16 v[48:51], v[104:107], v[124:127], v[48:51]\n"
    "ds_read_b128 v[244:247], v5 offset:1472\n"
    "v_mfma_f32_16x16x32_bf16 v[36:39], v[108:111], v[216:219], v[36:39]\n"
    "s_xor_b32 s43, s41, 147456\n"
    "buffer_store_dwordx4 v[52:55], v8, s[24:27], s43 offen sc1\n"
    "v_mfma_f32_16x16x32_bf16 v[40:43], v[108:111], v[172:175], v[40:43]\n"
    "v_cvt_pk_bf16_f32 v60, v60, v61\n"
    "v_cvt_pk_bf16_f32 v61, v62, v63\n"
    "v_mfma_f32_16x16x32_bf16 v[44:47], v[108:111], v[124:127], v[44:47]\n"
    "v_cvt_pk_bf16_f32 v62, v64, v65\n"
    "v_cvt_pk_bf16_f32 v63, v66, v67\n"
    "v_mfma_f32_16x16x32_bf16 v[48:51], v[108:111], v[220:223], v[48:51]\n"
    "ds_read_b128 v[208:211], v5 offset:1600\n"
    "v_mfma_f32_16x16x32_bf16 v[36:39], v[180:183], v[172:175], v[36:39]\n"
    "v_min_u32 v60, -2, v60\n"
    "v_min_u32 v61, -2, v61\n"
    "v_mfma_f32_16x16x32_bf16 v[40:43], v[180:183], v[124:127], v[40:43]\n"
    "v_min_u32 v62, -2, v62\n"
    "v_min_u32 v63, -2, v63\n"
    "v_mfma_f32_16x16x32_bf16 v[44:47], v[180:183], v[220:223], v[44:47]\n"
    "s_xor_b32 s43, s41, 184320\n"
    "buffer_store_dwordx4 v[60:63], v8, s[24:27], s43 offen sc1\n"
    "s_waitcnt lgkmcnt(1)\n"
    "v_mfma_f32_16x16x32_bf16 v[48:51], v[180:183], v[228:231], v[48:51]\n"
    "ds_read_b128 v[192:195], v5 offset:1728\n"
    "v_mfma_f32_16x16x32_bf16 v[36:39], v[116:119], v[124:127], v[36:39]\n"
    "v_mfma_f32_16x16x32_bf16 v[40:43], v[116:119], v[220:223], v[40:43]\n"
    "v_mfma_f32_16x16x32_bf16 v[44:47], v[116:119], v[228:231], v[44:47]\n"
    "v_mfma_f32_16x16x32_bf16 v[48:51], v[116:119], v[244:247], v[48:51]\n"
    "ds_read_b128 v[84:87], v5 offset:1856\n"
    "v_mfma_f32_16x16x32_bf16 v[36:39], v[128:131], v[220:223], v[36:39]\n"
    "v_mfma_f32_16x16x32_bf16 v[40:43], v[128:131], v[228:231], v[40:43]\n"
    "v_mfma_f32_16x16x32_bf16 v[44:47], v[128:131], v[244:247], v[44:47]\n"
    "s_waitcnt lgkmcnt(1)\n"
    "v_mfma_f32_16x16x32_bf16 v[48:51], v[128:131], v[208:211], v[48:51]\n"
    "ds_read_b128 v[120:123], v5 offset:1984\n"
    "v_mfma_f32_16x16x32_bf16 v[36:39], v[144:147], v[228:231], v[36:39]\n"
    "v_mfma_f32_16x16x32_bf16 v[40:43], v[144:147], v[244:247], v[40:43]\n"
    "v_mfma_f32_16x16x32_bf16 v[44:47], v[144:147], v[208:211], v[44:47]\n"
    "v_mfma_f32_16x16x32_bf16 v[48:51], v[144:147], v[192:195], v[48:51]\n"
    "ds_read_b128 v[152:155], v5 offset:2144\n"
    "v_mfma_f32_16x16x32_bf16 v[36:39], v[88:91], v[244:247], v[36:39]\n"
    "v_mfma_f32_16x16x32_bf16 v[40:43], v[88:91], v[208:211], v[40:43]\n"
    "v_mfma_f32_16x16x32_bf16 v[44:47], v[88:91], v[192:195], v[44:47]\n"
    "s_waitcnt lgkmcnt(1)\n"
    "v_mfma_f32_16x16x32_bf16 v[48:51], v[88:91], v[84:87], v[48:51]\n"
    "ds_read_b128 v[200:203], v5 offset:2272\n"
    "v_mfma_f32_16x16x32_bf16 v[36:39], v[140:143], v[208:211], v[36:39]\n"
    "v_mfma_f32_16x16x32_bf16 v[40:43], v[140:143], v[192:195], v[40:43]\n"
    "v_mfma_f32_16x16x32_bf16 v[44:47], v[140:143], v[84:87], v[44:47]\n"
    "v_mfma_f32_16x16x32_bf16 v[48:51], v[140:143], v[120:123], v[48:51]\n"
    "ds_read_b128 v[216:219], v5 offset:2400\n"
    "v_mfma_f32_16x16x32_bf16 v[36:39], v[156:159], v[192:195], v[36:39]\n"
    "v_mfma_f32_16x16x32_bf16 v[40:43], v[156:159], v[84:87], v[40:43]\n"
    "v_mfma_f32_16x16x32_bf16 v[44:47], v[156:159], v[120:123], v[44:47]\n"
    "s_waitcnt lgkmcnt(1)\n"
    "v_mfma_f32_16x16x32_bf16 v[48:51], v[156:159], v[152:155], v[48:51]\n"
    "ds_read_b128 v[172:175], v5 offset:2528\n"
    "v_mfma_f32_16x16x32_bf16 v[36:39], v[188:191], v[84:87], v[36:39]\n"
    "v_mfma_f32_16x16x32_bf16 v[40:43], v[188:191], v[120:123], v[40:43]\n"
    "v_mfma_f32_16x16x32_bf16 v[44:47], v[188:191], v[152:155], v[44:47]\n"
    "v_mfma_f32_16x16x32_bf16 v[48:51], v[188:191], v[200:203], v[48:51]\n"
    "ds_read_b128 v[124:127], v5 offset:2656\n"
    "v_mfma_f32_16x16x32_bf16 v[36:39], v[92:95], v[120:123], v[36:39]\n"
    "v_mfma_f32_16x16x32_bf16 v[40:43], v[92:95], v[152:155], v[40:43]\n"
    "v_mfma_f32_16x16x32_bf16 v[44:47], v[92:95], v[200:203], v[44:47]\n"
    "s_waitcnt lgkmcnt(1)\n"
    "v_mfma_f32_16x16x32_bf16 v[48:51], v[92:95], v[216:219], v[48:51]\n"
    "ds_read_b128 v[220:223], v5 offset:2784\n"
    "v_mfma_f32_16x16x32_bf16 v[36:39], v[224:227], v[152:155], v[36:39]\n"
    "v_mfma_f32_16x16x32_bf16 v[40:43], v[224:227], v[200:203], v[40:43]\n"
    "v_mfma_f32_16x16x32_bf16 v[44:47], v[224:227], v[216:219], v[44:47]\n"
    "v_mfma_f32_16x16x32_bf16 v[48:51], v[224:227], v[172:175], v[48:51]\n"
    "ds_read_b128 v[228:231], v5 offset:2912\n"
    "v_mfma_f32_16x16x32_bf16 v[36:39], v[232:235], v[200:203], v[36:39]\n"
    "v_mfma_f32_16x16x32_bf16 v[40:43], v[232:235], v[216:219], v[40:43]\n"
    "v_mfma_f32_16x16x32_bf16 v[44:47], v[232:235], v[172:175], v[44:47]\n"
    "s_waitcnt lgkmcnt(1)\n"
    "v_mfma_f32_16x16x32_bf16 v[48:51], v[232:235], v[124:127], v[48:51]\n"
    "ds_read_b128 v[244:247], v5 offset:3040\n"
    "v_mfma_f32_16x16x32_bf16 v[36:39], v[240:243], v[216:219], v[36:39]\n"
    "v_mfma_f32_16x16x32_bf16 v[40:43], v[240:243], v[172:175], v[40:43]\n"
    "v_mfma_f32_16x16x32_bf16 v[44:47], v[240:243], v[124:127], v[44:47]\n"
    "v_mfma_f32_16x16x32_bf16 v[48:51], v[240:243], v[220:223], v[48:51]\n"
    "ds_read_b128 v[208:211], v5 offset:3168\n"
    "v_mfma_f32_16x16x32_bf16 v[36:39], v[248:251], v[172:175], v[36:39]\n"
    "v_mfma_f32_16x16x32_bf16 v[40:43], v[248:251], v[124:127], v[40:43]\n"
    "v_mfma_f32_16x16x32_bf16 v[44:47], v[248:251], v[220:223], v[44:47]\n"
    "s_waitcnt lgkmcnt(1)\n"
    "v_mfma_f32_16x16x32_bf16 v[48:51], v[248:251], v[228:231], v[48:51]\n"
    "ds_read_b128 v[192:195], v5 offset:3296\n"
    "v_mfma_f32_16x16x32_bf16 v[36:39], v[184:187], v[124:127], v[36:39]\n"
    "v_mfma_f32_16x16x32_bf16 v[40:43], v[184:187], v[220:223], v[40:43]\n"
    "v_mfma_f32_16x16x32_bf16 v[44:47], v[184:187], v[228:231], v[44:47]\n"
    "v_mfma_f32_16x16x32_bf16 v[48:51], v[184:187], v[244:247], v[48:51]\n"
    "ds_read_b128 v[84:87], v5 offset:3424\n"
    "v_mfma_f32_16x16x32_bf16 v[36:39], v[100:103], v[220:223], v[36:39]\n"
    "v_mfma_f32_16x16x32_bf16 v[40:43], v[100:103], v[228:231], v[40:43]\n"
    "v_mfma_f32_16x16x32_bf16 v[44:47], v[100:103], v[244:247], v[44:47]\n"
    "s_waitcnt lgkmcnt(1)\n"
    "v_mfma_f32_16x16x32_bf16 v[48:51], v[100:103], v[208:211], v[48:51]\n"
    "ds_read_b128 v[120:123], v5 offset:3552\n"
    "v_mfma_f32_16x16x32_bf16 v[36:39], v[160:163], v[228:231], v[36:39]\n"
    "v_mfma_f32_16x16x32_bf16 v[40:43], v[160:163], v[244:247], v[40:43]\n"
    "v_mfma_f32_16x16x32_bf16 v[44:47], v[160:163], v[208:211], v[44:47]\n"
    "v_mfma_f32_16x16x32_bf16 v[48:51], v[160:163], v[192:195], v[48:51]\n"
    "ds_read_b128 v[152:155], v5 offset:3680\n"
    "v_mfma_f32_16x16x32_bf16 v[36:39], v[176:179], v[244:247], v[36:39]\n"
    "v_mfma_f32_16x16x32_bf16 v[40:43], v[176:179], v[208:211], v[40:43]\n"
    "v_mfma_f32_16x16x32_bf16 v[44:47], v[176:179], v[192:195], v[44:47]\n"
    "s_waitcnt lgkmcnt(1)\n"
    "v_mfma_f32_16x16x32_bf16 v[48:51], v[176:179], v[84:87], v[48:51]\n"
    "ds_read_b128 v[200:203], v5 offset:3808\n"
    "v_mfma_f32_16x16x32_bf16 v[36:39], v[196:199], v[208:211], v[36:39]\n"
    "v_mfma_f32_16x16x32_bf16 v[40:43], v[196:199], v[192:195], v[40:43]\n"
    "v_mfma_f32_16x16x32_bf16 v[44:47], v[196:199], v[84:87], v[44:47]\n"
    "v_mfma_f32_16x16x32_bf16 v[48:51], v[196:199], v[120:123], v[48:51]\n"
    "ds_read_b128 v[216:219], v5 offset:3936\n"
    "v_mfma_f32_16x16x32_bf16 v[36:39], v[136:139], v[192:195], v[36:39]\n"
    "v_mfma_f32_16x16x32_bf16 v[40:43], v[136:139], v[84:87], v[40:43]\n"
    "v_mfma_f32_16x16x32_bf16 v[44:47], v[136:139], v[120:123], v[44:47]\n"
    "s_waitcnt lgkmcnt(1)\n"
    "v_mfma_f32_16x16x32_bf16 v[48:51], v[136:139], v[152:155], v[48:51]\n"
    "ds_read_b128 v[172:175], v5 offset:4064\n"
    "v_mfma_f32_16x16x32_bf16 v[36:39], v[112:115], v[84:87], v[36:39]\n"
    "v_mfma_f32_16x16x32_bf16 v[40:43], v[112:115], v[120:123], v[40:43]\n"
    "v_mfma_f32_16x16x32_bf16 v[44:47], v[112:115], v[152:155], v[44:47]\n"
    "v_mfma_f32_16x16x32_bf16 v[48:51], v[112:115], v[200:203], v[48:51]\n"
    "ds_read_b128 v[124:127], v5 offset:4224\n"
    "v_mfma_f32_16x16x32_bf16 v[36:39], v[148:151], v[120:123], v[36:39]\n"
    "v_mfma_f32_16x16x32_bf16 v[40:43], v[148:151], v[152:155], v[40:43]\n"
    "v_mfma_f32_16x16x32_bf16 v[44:47], v[148:151], v[200:203], v[44:47]\n"
    "s_waitcnt lgkmcnt(1)\n"
    "v_mfma_f32_16x16x32_bf16 v[48:51], v[148:151], v[216:219], v[48:51]\n"
    "ds_read_b128 v[220:223], v5 offset:4352\n"
    "v_mfma_f32_16x16x32_bf16 v[36:39], v[164:167], v[152:155], v[36:39]\n"
    "v_mfma_f32_16x16x32_bf16 v[40:43], v[164:167], v[200:203], v[40:43]\n"
    "v_mfma_f32_16x16x32_bf16 v[44:47], v[164:167], v[216:219], v[44:47]\n"
    "v_mfma_f32_16x16x32_bf16 v[48:51], v[164:167], v[172:175], v[48:51]\n"
    "ds_read_b128 v[228:231], v5 offset:4480\n"
    "v_mfma_f32_16x16x32_bf16 v[36:39], v[204:207], v[200:203], v[36:39]\n"
    "v_mfma_f32_16x16x32_bf16 v[40:43], v[204:207], v[216:219], v[40:43]\n"
    "v_mfma_f32_16x16x32_bf16 v[44:47], v[204:207], v[172:175], v[44:47]\n"
    "s_waitcnt lgkmcnt(1)\n"
    "v_mfma_f32_16x16x32_bf16 v[48:51], v[204:207], v[124:127], v[48:51]\n"
    "ds_read_b128 v[244:247], v5 offset:4608\n"
    "v_mfma_f32_16x16x32_bf16 v[36:39], v[236:239], v[216:219], v[36:39]\n"
    "v_mfma_f32_16x16x32_bf16 v[40:43], v[236:239], v[172:175], v[40:43]\n"
    "v_mfma_f32_16x16x32_bf16 v[44:47], v[236:239], v[124:127], v[44:47]\n"
    "v_mfma_f32_16x16x32_bf16 v[48:51], v[236:239], v[220:223], v[48:51]\n"
    "ds_read_b128 v[208:211], v5 offset:4736\n"
    "v_mfma_f32_16x16x32_bf16 v[36:39], v[252:255], v[172:175], v[36:39]\n"
    "v_mfma_f32_16x16x32_bf16 v[40:43], v[252:255], v[124:127], v[40:43]\n"
    "v_mfma_f32_16x16x32_bf16 v[44:47], v[252:255], v[220:223], v[44:47]\n"
    "s_cmp_ge_u32 s39, 1\n"
    "s_cbranch_scc1 .Lt1_1%=\n"
    ".Lt0_1%=:\n"
    "s_waitcnt lgkmcnt(1)\n"
    "v_mfma_f32_16x16x32_bf16 v[48:51], v[252:255], v[228:231], v[48:51]\n"
    "ds_read_b128 v[192:195], v5 offset:4864\n"
    "v_mfma_f32_16x16x32_bf16 v[36:39], v[132:135], v[124:127], v[36:39]\n"
    "ds_read_b128 v[84:87], v5 offset:320\n"
    "v_mfma_f32_16x16x32_bf16 v[40:43], v[132:135], v[220:223], v[40:43]\n"
    "ds_read_b128 v[120:123], v5 offset:448\n"
    "v_mfma_f32_16x16x32_bf16 v[44:47], v[132:135], v[228:231], v[44:47]\n"
    "v_mfma_f32_16x16x32_bf16 v[48:51], v[132:135], v[244:247], v[48:51]\n"
    "ds_read_b128 v[152:155], v5 offset:576\n"
    "v_mfma_f32_16x16x32_bf16 v[36:39], v[168:171], v[220:223], v[36:39]\n"
    "v_mfma_f32_16x16x32_bf16 v[40:43], v[168:171], v[228:231], v[40:43]\n"
    "ds_read_b128 v[200:203], v5 offset:704\n"
    "v_mfma_f32_16x16x32_bf16 v[44:47], v[168:171], v[244:247], v[44:47]\n"
    "s_waitcnt lgkmcnt(4)\n"
    "v_mfma_f32_16x16x32_bf16 v[48:51], v[168:171], v[208:211], v[48:51]\n"
    "ds_read_b128 v[216:219], v5 offset:832\n"
    "v_mfma_f32_16x16x32_bf16 v[36:39], v[212:215], v[228:231], v[36:39]\n"
    "v_mfma_f32_16x16x32_bf16 v[40:43], v[212:215], v[244:247], v[40:43]\n"
    "ds_read_b128 v[172:175], v5 offset:960\n"
    "v_mfma_f32_16x16x32_bf16 v[44:47], v[212:215], v[208:211], v[44:47]\n"
    "v_mfma_f32_16x16x32_bf16 v[48:51], v[212:215], v[192:195], v[48:51]\n"
    "ds_read_b128 v[124:127], v5 offset:1088\n"
    "s_waitcnt lgkmcnt(4)\n"
    "v_mfma_f32_16x16x32_bf16 v[28:31], v[96:99], v[84:87], v[28:31]\n"
    "v_mfma_f32_16x16x32_bf16 v[32:35], v[96:99], v[120:123], v[32:35]\n"
    "ds_read_b128 v[220:223], v5 offset:1216\n"
    "v_mfma_f32_16x16x32_bf16 v[28:31], v[104:107], v[120:123], v[28:31]\n"
    "v_cvt_pk_bf16_f32 v36, v36, v37\n"
    "v_cvt_pk_bf16_f32 v37, v38, v39\n"
    "v_mfma_f32_16x16x32_bf16 v[32:35], v[104:107], v[152:155], v[32:35]\n"
    "ds_read_b128 v[228:231], v5 offset:1344\n"
    "v_mfma_f32_16x16x32_bf16 v[28:31], v[108:111], v[152:155], v[28:31]\n"
    "v_cvt_pk_bf16_f32 v38, v40, v41\n"
    "v_cvt_pk_bf16_f32 v39, v42, v43\n"
    "s_waitcnt lgkmcnt(3)\n"
    "v_mfma_f32_16x16x32_bf16 v[32:35], v[108:111], v[200:203], v[32:35]\n"
    "ds_read_b128 v[244:247], v5 offset:1472\n"
    "v_mfma_f32_16x16x32_bf16 v[28:31], v[180:183], v[200:203], v[28:31]\n"
    "v_min_u32 v36, -2, v36\n"
    "v_min_u32 v37, -2, v37\n"
    "v_mfma_f32_16x16x32_bf16 v[32:35], v[180:183], v[216:219], v[32:35]\n"
    "ds_read_b128 v[208:211], v5 offset:1600\n"
    "v_mfma_f32_16x16x32_bf16 v[28:31], v[116:119], v[216:219], v[28:31]\n"
    "v_min_u32 v38, -2, v38\n"
    "v_min_u32 v39, -2, v39\n"
    "v_mfma_f32_16x16x32_bf16 v[32:35], v[116:119], v[172:175], v[32:35]\n"
    "ds_read_b128 v[192:195], v5 offset:1728\n"
    "v_mfma_f32_16x16x32_bf16 v[28:31], v[128:131], v[172:175], v[28:31]\n"
    "s_xor_b32 s43, s41, 73728\n"
    "buffer_store_dwordx4 v[36:39], v8, s[24:27], s43 offen sc1\n"
    "s_waitcnt lgkmcnt(3)\n"
    "v_mfma_f32_16x16x32_bf16 v[32:35], v[128:131], v[124:127], v[32:35]\n"
    "ds_read_b128 v[84:87], v5 offset:1856\n"
    "v_mfma_f32_16x16x32_bf16 v[28:31], v[144:147], v[124:127], v[28:31]\n"
    "v_cvt_pk_bf16_f32 v44, v44, v45\n"
    "v_cvt_pk_bf16_f32 v45, v46, v47\n"
    "v_mfma_f32_16x16x32_bf16 v[32:35], v[144:147], v[220:223], v[32:35]\n"
    "ds_read_b128 v[120:123], v5 offset:1984\n"
    "v_mfma_f32_16x16x32_bf16 v[28:31], v[88:91], v[220:223], v[28:31]\n"
    "v_cvt_pk_bf16_f32 v46, v48, v49\n"
    "v_cvt_pk_bf16_f32 v47, v50, v51\n"
    "v_mfma_f32_16x16x32_bf16 v[32:35], v[88:91], v[228:231], v[32:35]\n"
    "ds_read_b128 v[152:155], v5 offset:2144\n"
    "v_mfma_f32_16x16x32_bf16 v[28:31], v[140:143], v[228:231], v[28:31]\n"
    "v_min_u32 v44, -2, v44\n"
    "v_min_u32 v45, -2, v45\n"
    "s_waitcnt lgkmcnt(3)\n"
    "v_mfma_f32_16x16x32_bf16 v[32:35], v[140:143], v[244:247], v[32:35]\n"
    "ds_read_b128 v[200:203], v5 offset:2272\n"
    "v_mfma_f32_16x16x32_bf16 v[28:31], v[156:159], v[244:247], v[28:31]\n"
    "v_min_u32 v46, -2, v46\n"
    "v_min_u32 v47, -2, v47\n"
    "v_mfma_f32_16x16x32_bf16 v[32:35], v[156:159], v[208:211], v[32:35]\n"
    "ds_read_b128 v[216:219], v5 offset:2400\n"
    "v_mfma_f32_16x16x32_bf16 v[28:31], v[188:191], v[208:211], v[28:31]\n"
    "s_xor_b32 s43, s41, 110592\n"
    "buffer_store_dwordx4 v[44:47], v8, s[24:27], s43 offen sc1\n"
    "v_mfma_f32_16x16x32_bf16 v[32:35], v[188:191], v[192:195], v[32:35]\n"
    "ds_read_b128 v[172:175], v5 offset:2528\n"
    "v_mfma_f32_16x16x32_bf16 v[28:31], v[92:95], v[192:195], v[28:31]\n"
    "s_waitcnt lgkmcnt(3)\n"
    "v_mfma_f32_16x16x32_bf16 v[32:35], v[92:95], v[84:87], v[32:35]\n"
    "ds_read_b128 v[124:127], v5 offset:2656\n"
    "v_mfma_f32_16x16x32_bf16 v[28:31], v[224:227], v[84:87], v[28:31]\n"
    "v_mfma_f32_16x16x32_bf16 v[32:35], v[224:227], v[120:123], v[32:35]\n"
    "ds_read_b128 v[220:223], v5 offset:2784\n"
    "v_mfma_f32_16x16x32_bf16 v[28:31], v[232:235], v[120:123], v[28:31]\n"
    "v_mfma_f32_16x16x32_bf16 v[32:35], v[232:235], v[152:155], v[32:35]\n"
    "ds_read_b128 v[228:231], v5 offset:2912\n"
    "v_mfma_f32_16x16x32_bf16 v[28:31], v[240:243], v[152:155], v[28:31]\n"
    "s_waitcnt lgkmcnt(3)\n"
    "v_mfma_f32_16x16x32_bf16 v[32:35], v[240:243], v[200:203], v[32:35]\n"
    "ds_read_b128 v[244:247], v5 offset:3040\n"
    "v_mfma_f32_16x16x32_bf16 v[28:31], v[248:251], v[200:203], v[28:31]\n"
    "v_mfma_f32_16x16x32_bf16 v[32:35], v[248:251], v[216:219], v[32:35]\n"
    "ds_read_b128 v[208:211], v5 offset:3168\n"
    "v_mfma_f32_16x16x32_bf16 v[28:31], v[184:187], v[216:219], v[28:31]\n"
    "v_mfma_f32_16x16x32_bf16 v[32:35], v[184:187], v[172:175], v[32:35]\n"
    "ds_read_b128 v[192:195], v5 offset:3296\n"
    "v_mfma_f32_16x16x32_bf16 v[28:31], v[100:103], v[172:175], v[28:31]\n"
    "s_waitcnt lgkmcnt(3)\n"
    "v_mfma_f32_16x16x32_bf16 v[32:35], v[100:103], v[124:127], v[32:35]\n"
    "ds_read_b128 v[84:87], v5 offset:3424\n"
    "v_mfma_f32_16x16x32_bf16 v[28:31], v[160:163], v[124:127], v[28:31]\n"
    "v_mfma_f32_16x16x32_bf16 v[32:35], v[160:163], v[220:223], v[32:35]\n"
    "ds_read_b128 v[120:123], v5 offset:3552\n"
    "v_mfma_f32_16x16x32_bf16 v[28:31], v[176:179], v[220:223], v[28:31]\n"
    "v_mfma_f32_16x16x32_bf16 v[32:35], v[176:179], v[228:231], v[32:35]\n"
    "ds_read_b128 v[152:155], v5 offset:3680\n"
    "v_mfma_f32_16x16x32_bf16 v[28:31], v[196:199], v[228:231], v[28:31]\n"
    "s_waitcnt lgkmcnt(3)\n"
    "v_mfma_f32_16x16x32_bf16 v[32:35], v[196:199], v[244:247], v[32:35]\n"
    "ds_read_b128 v[200:203], v5 offset:3808\n"
    "v_mfma_f32_16x16x32_bf16 v[28:31], v[136:139], v[244:247], v[28:31]\n"
    "v_mfma_f32_16x16x32_bf16 v[32:35], v[136:139], v[208:211], v[32:35]\n"
    "ds_read_b128 v[216:219], v5 offset:3936\n"
    "v_mfma_f32_16x16x32_bf16 v[28:31], v[112:115], v[208:211], v[28:31]\n"
    "v_mfma_f32_16x16x32_bf16 v[32:35], v[112:115], v[192:195], v[32:35]\n"
    "ds_read_b128 v[172:175], v5 offset:4064\n"
    "v_mfma_f32_16x16x32_bf16 v[28:31], v[148:151], v[192:195], v[28:31]\n"
    "s_waitcnt lgkmcnt(3)\n"
    "v_mfma_f32_16x16x32_bf16 v[32:35], v[148:151], v[84:87], v[32:35]\n"
    "ds_read_b128 v[124:127], v5 offset:4224\n"
    "v_mfma_f32_16x16x32_bf16 v[28:31], v[164:167], v[84:87], v[28:31]\n"
    "v_mfma_f32_16x16x32_bf16 v[32:35], v[164:167], v[120:123], v[32:35]\n"
    "ds_read_b128 v[220:223], v5 offset:4352\n"
    "v_mfma_f32_16x16x32_bf16 v[28:31], v[204:207], v[120:123], v[28:31]\n"
    "ds_read_b128 v[228:231], v5 offset:64\n"
    "v_mfma_f32_16x16x32_bf16 v[32:35], v[204:207], v[152:155], v[32:35]\n"
    "ds_read_b128 v[244:247], v5 offset:192\n"
    "v_mfma_f32_16x16x32_bf16 v[28:31], v[236:239], v[152:155], v[28:31]\n"
    "s_waitcnt lgkmcnt(4)\n"
    "v_mfma_f32_16x16x32_bf16 v[32:35], v[236:239], v[200:203], v[32:35]\n"
    "ds_read_b128 v[208:211], v5 offset:320\n"
    "v_mfma_f32_16x16x32_bf16 v[28:31], v[252:255], v[200:203], v[28:31]\n"
    "v_mfma_f32_16x16x32_bf16 v[32:35], v[252:255], v[216:219], v[32:35]\n"
    "ds_read_b128 v[192:195], v5 offset:448\n"
    "v_mfma_f32_16x16x32_bf16 v[28:31], v[132:135], v[216:219], v[28:31]\n"
    "v_mfma_f32_16x16x32_bf16 v[32:35], v[132:135], v[172:175], v[32:35]\n"
    "ds_read_b128 v[84:87], v5 offset:576\n"
    "v_mfma_f32_16x16x32_bf16 v[28:31], v[168:171], v[172:175], v[28:31]\n"
    "s_waitcnt lgkmcnt(3)\n"
    "v_mfma_f32_16x16x32_bf16 v[32:35], v[168:171], v[124:127], v[32:35]\n"
    "ds_read_b128 v[120:123], v5 offset:704\n"
    "v_mfma_f32_16x16x32_bf16 v[28:31], v[212:215], v[124:127], v[28:31]\n"
    "v_mfma_f32_16x16x32_bf16 v[32:35], v[212:215], v[220:223], v[32:35]\n"
    "ds_read_b128 v[152:155], v5 offset:832\n"
    "v_mfma_f32_16x16x32_bf16 v[20:23], v[96:99], v[228:231], v[20:23]\n"
    "v_mfma_f32_16x16x32_bf16 v[24:27], v[96:99], v[244:247], v[24:27]\n"
    "ds_read_b128 v[200:203], v5 offset:960\n"
    "v_mfma_f32_16x16x32_bf16 v[20:23], v[104:107], v[244:247], v[20:23]\n"
    "s_nop 1\n"
    "v_cvt_pk_bf16_f32 v28, v28, v29\n"
    "v_cvt_pk_bf16_f32 v29, v30, v31\n"
    "v_cvt_pk_bf16_f32 v30, v32, v33\n"
    "v_cvt_pk_bf16_f32 v31, v34, v35\n"
    "s_waitcnt lgkmcnt(3)\n"
    "v_mfma_f32_16x16x32_bf16 v[24:27], v[104:107], v[208:211], v[24:27]\n"
    "ds_read_b128 v[216:219], v5 offset:1088\n"
    "v_min_u32 v28, -2, v28\n"
    "v_min_u32 v29, -2, v29\n"
    "v_mfma_f32_16x16x32_bf16 v[20:23], v[108:111], v[208:211], v[20:23]\n"
    "v_min_u32 v30, -2, v30\n"
    "v_min_u32 v31, -2, v31\n"
    "s_xor_b32 s43, s41, 36864\n"
    "buffer_store_dwordx4 v[28:31], v8, s[24:27], s43 offen sc1\n"
    "v_mfma_f32_16x16x32_bf16 v[24:27], v[108:111], v[192:195], v[24:27]\n"
    "ds_read_b128 v[172:175], v5 offset:1216\n"
    "v_mfma_f32_16x16x32_bf16 v[20:23], v[180:183], v[192:195], v[20:23]\n"
    "v_mfma_f32_16x16x32_bf16 v[24:27], v[180:183], v[84:87], v[24:27]\n"
    "ds_read_b128 v[124:127], v5 offset:1344\n"
    "v_mfma_f32_16x16x32_bf16 v[20:23], v[116:119], v[84:87], v[20:23]\n"
    "s_waitcnt lgkmcnt(3)\n"
    "v_mfma_f32_16x16x32_bf16 v[24:27], v[116:119], v[120:123], v[24:27]\n"
    "ds_read_b128 v[220:223], v5 offset:1472\n"
    "v_mfma_f32_16x16x32_bf16 v[20:23], v[128:131], v[120:123], v[20:23]\n"
    "v_mfma_f32_16x16x32_bf16 v[24:27], v[128:131], v[152:155], v[24:27]\n"
    "ds_read_b128 v[228:231], v5 offset:1600\n"
    "v_mfma_f32_16x16x32_bf16 v[20:23], v[144:147], v[152:155], v[20:23]\n"
    "v_mfma_f32_16x16x32_bf16 v[24:27], v[144:147], v[200:203], v[24:27]\n"
    "ds_read_b128 v[96:99], v5 offset:1728\n"
    "v_mfma_f32_16x16x32_bf16 v[20:23], v[88:91], v[200:203], v[20:23]\n"
    "s_waitcnt lgkmcnt(3)\n"
    "v_mfma_f32_16x16x32_bf16 v[24:27], v[88:91], v[216:219], v[24:27]\n"
    "ds_read_b128 v[244:247], v5 offset:1856\n"
    "v_mfma_f32_16x16x32_bf16 v[20:23], v[140:143], v[216:219], v[20:23]\n"
    "v_mfma_f32_16x16x32_bf16 v[24:27], v[140:143], v[172:175], v[24:27]\n"
    "ds_read_b128 v[104:107], v5 offset:1984\n"
    "v_mfma_f32_16x16x32_bf16 v[20:23], v[156:159], v[172:175], v[20:23]\n"
    "v_mfma_f32_16x16x32_bf16 v[24:27], v[156:159], v[124:127], v[24:27]\n"
    "ds_read_b128 v[208:211], v5 offset:2144\n"
    "v_mfma_f32_16x16x32_bf16 v[20:23], v[188:191], v[124:127], v[20:23]\n"
    "s_waitcnt lgkmcnt(3)\n"
    "v_mfma_f32_16x16x32_bf16 v[24:27], v[188:191], v[220:223], v[24:27]\n"
    "ds_read_b128 v[108:111], v5 offset:2272\n"
    "v_mfma_f32_16x16x32_bf16 v[20:23], v[92:95], v[220:223], v[20:23]\n"
    "v_mfma_f32_16x16x32_bf16 v[24:27], v[92:95], v[228:231], v[24:27]\n"
    "ds_read_b128 v[192:195], v5 offset:2400\n"
    "v_mfma_f32_16x16x32_bf16 v[20:23], v[224:227], v[228:231], v[20:23]\n"
    "v_mfma_f32_16x16x32_bf16 v[24:27], v[224:227], v[96:99], v[24:27]\n"
    "ds_read_b128 v[180:183], v5 offset:2528\n"
    "v_mfma_f32_16x16x32_bf16 v[20:23], v[232:235], v[96:99], v[20:23]\n"
    "s_waitcnt lgkmcnt(3)\n"
    "v_mfma_f32_16x16x32_bf16 v[24:27], v[232:235], v[244:247], v[24:27]\n"
    "ds_read_b128 v[84:87], v5 offset:2656\n"
    "v_mfma_f32_16x16x32_bf16 v[20:23], v[240:243], v[244:247], v[20:23]\n"
    "v_mfma_f32_16x16x32_bf16 v[24:27], v[240:243], v[104:107], v[24:27]\n"
    "ds_read_b128 v[116:119], v5 offset:2784\n"
    "v_mfma_f32_16x16x32_bf16 v[20:23], v[248:251], v[104:107], v[20:23]\n"
    "v_mfma_f32_16x16x32_bf16 v[24:27], v[248:251], v[208:211], v[24:27]\n"
    "ds_read_b128 v[120:123], v5 offset:2912\n"
    "v_mfma_f32_16x16x32_bf16 v[20:23], v[184:187], v[208:211], v[20:23]\n"
    "s_waitcnt lgkmcnt(3)\n"
    "v_mfma_f32_16x16x32_bf16 v[24:27], v[184:187], v[108:111], v[24:27]\n"
    "ds_read_b128 v[128:131], v5 offset:3040\n"
    ".Lpad2%=:\n"
    "s_cbranch_execz .Lpad2r%=\n"
    "v_mfma_f32_16x16x32_bf16 v[20:23], v[100:103], v[108:111], v[20:23]\n"
    "v_mfma_f32_16x16x32_bf16 v[24:27], v[100:103], v[192:195], v[24:27]\n"
    "ds_read_b128 v[152:155], v5 offset:3168\n"
    "v_mfma_f32_16x16x32_bf16 v[20:23], v[160:163], v[192:195], v[20:23]\n"
    "v_mfma_f32_16x16x32_bf16 v[24:27], v[160:163], v[180:183], v[24:27]\n"
    "ds_read_b128 v[144:147], v5 offset:3296\n"
    "v_mfma_f32_16x16x32_bf16 v[20:23], v[176:179], v[180:183], v[20:23]\n"
    "s_waitcnt lgkmcnt(3)\n"
    "v_mfma_f32_16x16x32_bf16 v[24:27], v[176:179], v[84:87], v[24:27]\n"
    "ds_read_b128 v[200:203], v5 offset:3424\n"
    "v_mfma_f32_16x16x32_bf16 v[20:23], v[196:199], v[84:87], v[20:23]\n"
    "v_mfma_f32_16x16x32_bf16 v[24:27], v[196:199], v[116:119], v[24:27]\n"
    "ds_read_b128 v[88:91], v5 offset:3552\n"
    "v_mfma_f32_16x16x32_bf16 v[20:23], v[136:139], v[116:119], v[20:23]\n"
    "v_mfma_f32_16x16x32_bf16 v[24:27], v[136:139], v[120:123], v[24:27]\n"
    "ds_read_b128 v[216:219], v5 offset:3680\n"
    "v_mfma_f32_16x16x32_bf16 v[20:23], v[112:115], v[120:123], v[20:23]\n"
    "s_waitcnt lgkmcnt(3)\n"
    "v_mfma_f32_16x16x32_bf16 v[24:27], v[112:115], v[128:131], v[24:27]\n"
    "ds_read_b128 v[140:143], v5 offset:3808\n"
    "v_mfma_f32_16x16x32_bf16 v[20:23], v[148:151], v[128:131], v[20:23]\n"
    "v_mfma_f32_16x16x32_bf16 v[24:27], v[148:151], v[152:155], v[24:27]\n"
    "ds_read_b128 v[172:175], v5 offset:3936\n"
    "v_mfma_f32_16x16x32_bf16 v[20:23], v[164:167], v[152:155], v[20:23]\n"
    "v_mfma_f32_16x16x32_bf16 v[24:27], v[164:167], v[144:147], v[24:27]\n"
    "ds_read_b128 v[156:159], v5 offset:4064\n"
    "v_mfma_f32_16x16x32_bf16 v[20:23], v[204:207], v[144:147], v[20:23]\n"
    "s_waitcnt lgkmcnt(3)\n"
    "v_mfma_f32_16x16x32_bf16 v[24:27], v[204:207], v[200:203], v[24:27]\n"
    "v_mfma_f32_16x16x32_bf16 v[20:23], v[236:239], v[200:203], v[20:23]\n"
    "v_mfma_f32_16x16x32_bf16 v[24:27], v[236:239], v[88:91], v[24:27]\n"
    "v_mfma_f32_16x16x32_bf16 v[20:23], v[252:255], v[88:91], v[20:23]\n"
    "v_mfma_f32_16x16x32_bf16 v[24:27], v[252:255], v[216:219], v[24:27]\n"
    "v_mfma_f32_16x16x32_bf16 v[20:23], v[132:135], v[216:219], v[20:23]\n"
    "s_waitcnt lgkmcnt(0)\n"
    "v_mfma_f32_16x16x32_bf16 v[24:27], v[132:135], v[140:143], v[24:27]\n"
    "v_mfma_f32_16x16x32_bf16 v[20:23], v[168:171], v[140:143], v[20:23]\n"
    "v_mfma_f32_16x16x32_bf16 v[24:27], v[168:171], v[172:175], v[24:27]\n"
    "v_mfma_f32_16x16x32_bf16 v[20:23], v[212:215], v[172:175], v[20:23]\n"
    "v_mfma_f32_16x16x32_bf16 v[24:27], v[212:215], v[156:159], v[24:27]\n"
    "s_nop 6\n"
    "ds_write_b128 v16, v[20:23]\n"
    "ds_write_b128 v16, v[24:27] offset:1088\n"
    "s_waitcnt lgkmcnt(0)\n"
    "s_barrier\n"
    "ds_read_b128 v[96:99], v14\n"
    "ds_read_b128 v[100:103], v14 offset:2176\n"
    "ds_read_b128 v[104:107], v14 offset:4352\n"
    "s_waitcnt lgkmcnt(1)\n"
    "v_fmac_f32 v100, s56, v96\n"
    "v_fmac_f32 v101, s56, v97\n"
    "v_fmac_f32 v102, s56, v98\n"
    "v_fmac_f32 v103, s56, v99\n"
    "v_exp_f32 v100, v100\n"
    "v_exp_f32 v101, v101\n"
    "v_exp_f32 v102, v102\n"
    "v_exp_f32 v103, v103\n"
    "v_add_f32 v100, 1.0, v100\n"
    "v_add_f32 v101, 1.0, v101\n"
    "v_add_f32 v102, 1.0, v102\n"
    "v_add_f32 v103, 1.0, v103\n"
    "v_rcp_f32 v100, v100\n"
    "v_rcp_f32 v101, v101\n"
    "v_rcp_f32 v102, v102\n"
    "v_rcp_f32 v103, v103\n"
    "v_fma_f32 v100, v100, s58, v17\n"
    "v_fma_f32 v101, v101, s58, v17\n"
    "v_fma_f32 v102, v102, s58, v17\n"
    "v_fma_f32 v103, v103, s58, v17\n"
    "s_waitcnt lgkmcnt(0)\n"
    "v_mul_f32 v100, v100, v104\n"
    "v_mul_f32 v101, v101, v105\n"
    "v_mul_f32 v102, v102, v106\n"
    "v_mul_f32 v103, v103, v107\n"
    "v_cvt_pk_bf16_f32 v96, v100, v101\n"
    "v_cvt_pk_bf16_f32 v97, v102, v103\n"
    "buffer_store_dwordx2 v[96:97], v15, s[28:31], 0 offen\n"
    "s_endpgm\n"
    ".Lt1_1%=:\n"
    ".Lpad3%=:\n"
    "s_cbranch_execz .Lpad3r%=\n"
    "s_waitcnt lgkmcnt(1)\n"
    "v_mfma_f32_16x16x32_bf16 v[48:51], v[252:255], v[228:231], v[48:51]\n"
    "ds_read_b128 v[192:195], v5 offset:4864\n"
    "v_mfma_f32_16x16x32_bf16 v[36:39], v[132:135], v[124:127], v[36:39]\n"
    "ds_read_b128 v[84:87], v5 offset:64\n"
    "v_mfma_f32_16x16x32_bf16 v[40:43], v[132:135], v[220:223], v[40:43]\n"
    "ds_read_b128 v[120:123], v5 offset:192\n"
    "v_mfma_f32_16x16x32_bf16 v[44:47], v[132:135], v[228:231], v[44:47]\n"
    "v_mfma_f32_16x16x32_bf16 v[48:51], v[132:135], v[244:247], v[48:51]\n"
    "ds_read_b128 v[152:155], v5 offset:320\n"
    "v_mfma_f32_16x16x32_bf16 v[36:39], v[168:171], v[220:223], v[36:39]\n"
    "v_mfma_f32_16x16x32_bf16 v[40:43], v[168:171], v[228:231], v[40:43]\n"
    "ds_read_b128 v[200:203], v5 offset:448\n"
    "v_mfma_f32_16x16x32_bf16 v[44:47], v[168:171], v[244:247], v[44:47]\n"
    "s_waitcnt lgkmcnt(4)\n"
    "v_mfma_f32_16x16x32_bf16 v[48:51], v[168:171], v[208:211], v[48:51]\n"
    "ds_read_b128 v[216:219], v5 offset:576\n"
    "v_mfma_f32_16x16x32_bf16 v[36:39], v[212:215], v[228:231], v[36:39]\n"
    "v_mfma_f32_16x16x32_bf16 v[40:43], v[212:215], v[244:247], v[40:43]\n"
    "ds_read_b128 v[172:175], v5 offset:704\n"
    "v_mfma_f32_16x16x32_bf16 v[44:47], v[212:215], v[208:211], v[44:47]\n"
    "v_mfma_f32_16x16x32_bf16 v[48:51], v[212:215], v[192:195], v[48:51]\n"
    "ds_read_b128 v[124:127], v5 offset:832\n"
    "s_waitcnt lgkmcnt(4)\n"
    "v_mfma_f32_16x16x32_bf16 v[20:23], v[96:99], v[84:87], v[20:23]\n"
    "v_mfma_f32_16x16x32_bf16 v[24:27], v[96:99], v[120:123], v[24:27]\n"
    "ds_read_b128 v[220:223], v5 offset:960\n"
    "v_mfma_f32_16x16x32_bf16 v[20:23], v[104:107], v[120:123], v[20:23]\n"
    "v_cvt_pk_bf16_f32 v36, v36, v37\n"
    "v_cvt_pk_bf16_f32 v37, v38, v39\n"
    "v_mfma_f32_16x16x32_bf16 v[24:27], v[104:107], v[152:155], v[24:27]\n"
    "ds_read_b128 v[228:231], v5 offset:1088\n"
    "v_mfma_f32_16x16x32_bf16 v[20:23], v[108:111], v[152:155], v[20:23]\n"
    "v_cvt_pk_bf16_f32 v38, v40, v41\n"
    "v_cvt_pk_bf16_f32 v39, v42, v43\n"
    "s_waitcnt lgkmcnt(3)\n"
    "v_mfma_f32_16x16x32_bf16 v[24:27], v[108:111], v[200:203], v[24:27]\n"
    "ds_read_b128 v[244:247], v5 offset:1216\n"
    "v_mfma_f32_16x16x32_bf16 v[20:23], v[180:183], v[200:203], v[20:23]\n"
    "v_min_u32 v36, -2, v36\n"
    "v_min_u32 v37, -2, v37\n"
    "v_mfma_f32_16x16x32_bf16 v[24:27], v[180:183], v[216:219], v[24:27]\n"
    "ds_read_b128 v[208:211], v5 offset:1344\n"
    "v_mfma_f32_16x16x32_bf16 v[20:23], v[116:119], v[216:219], v[20:23]\n"
    "v_min_u32 v38, -2, v38\n"
    "v_min_u32 v39, -2, v39\n"
    "v_mfma_f32_16x16x32_bf16 v[24:27], v[116:119], v[172:175], v[24:27]\n"
    "ds_read_b128 v[192:195], v5 offset:1472\n"
    "v_mfma_f32_16x16x32_bf16 v[20:23], v[128:131], v[172:175], v[20:23]\n"
    "s_xor_b32 s43, s41, 73728\n"
    "buffer_store_dwordx4 v[36:39], v8, s[24:27], s43 offen sc1\n"
    "s_waitcnt lgkmcnt(3)\n"
    "v_mfma_f32_16x16x32_bf16 v[24:27], v[128:131], v[124:127], v[24:27]\n"
    "ds_read_b128 v[84:87], v5 offset:1600\n"
    "v_mfma_f32_16x16x32_bf16 v[20:23], v[144:147], v[124:127], v[20:23]\n"
    "v_cvt_pk_bf16_f32 v44, v44, v45\n"
    "v_cvt_pk_bf16_f32 v45, v46, v47\n"
    "v_mfma_f32_16x16x32_bf16 v[24:27], v[144:147], v[220:223], v[24:27]\n"
    "ds_read_b128 v[120:123], v5 offset:1728\n"
    "v_mfma_f32_16x16x32_bf16 v[20:23], v[88:91], v[220:223], v[20:23]\n"
    "v_cvt_pk_bf16_f32 v46, v48, v49\n"
    "v_cvt_pk_bf16_f32 v47, v50, v51\n"
    "v_mfma_f32_16x16x32_bf16 v[24:27], v[88:91], v[228:231], v[24:27]\n"
    "ds_read_b128 v[152:155], v5 offset:1856\n"
    "v_mfma_f32_16x16x32_bf16 v[20:23], v[140:143], v[228:231], v[20:23]\n"
    "v_min_u32 v44, -2, v44\n"
    "v_min_u32 v45, -2, v45\n"
    "s_waitcnt lgkmcnt(3)\n"
    "v_mfma_f32_16x16x32_bf16 v[24:27], v[140:143], v[244:247], v[24:27]\n"
    "ds_read_b128 v[200:203], v5 offset:1984\n"
    "v_mfma_f32_16x16x32_bf16 v[20:23], v[156:159], v[244:247], v[20:23]\n"
    "v_min_u32 v46, -2, v46\n"
    "v_min_u32 v47, -2, v47\n"
    "v_mfma_f32_16x16x32_bf16 v[24:27], v[156:159], v[208:211], v[24:27]\n"
    "ds_read_b128 v[216:219], v5 offset:2144\n"
    "v_mfma_f32_16x16x32_bf16 v[20:23], v[188:191], v[208:211], v[20:23]\n"
    "s_xor_b32 s43, s41, 110592\n"
    "buffer_store_dwordx4 v[44:47], v8, s[24:27], s43 offen sc1\n"
    "v_mfma_f32_16x16x32_bf16 v[24:27], v[188:191], v[192:195], v[24:27]\n"
    "ds_read_b128 v[172:175], v5 offset:2272\n"
    "v_mfma_f32_16x16x32_bf16 v[20:23], v[92:95], v[192:195], v[20:23]\n"
    "s_waitcnt lgkmcnt(3)\n"
    "v_mfma_f32_16x16x32_bf16 v[24:27], v[92:95], v[84:87], v[24:27]\n"
    "ds_read_b128 v[124:127], v5 offset:2400\n"
    "v_mfma_f32_16x16x32_bf16 v[20:23], v[224:227], v[84:87], v[20:23]\n"
    "v_mfma_f32_16x16x32_bf16 v[24:27], v[224:227], v[120:123], v[24:27]\n"
    "ds_read_b128 v[220:223], v5 offset:2528\n"
    "v_mfma_f32_16x16x32_bf16 v[20:23], v[232:235], v[120:123], v[20:23]\n"
    "v_mfma_f32_16x16x32_bf16 v[24:27], v[232:235], v[152:155], v[24:27]\n"
    "ds_read_b128 v[228:231], v5 offset:2656\n"
    "v_mfma_f32_16x16x32_bf16 v[20:23], v[240:243], v[152:155], v[20:23]\n"
    "s_waitcnt lgkmcnt(3)\n"
    "v_mfma_f32_16x16x32_bf16 v[24:27], v[240:243], v[200:203], v[24:27]\n"
    "ds_read_b128 v[244:247], v5 offset:2784\n"
    "v_mfma_f32_16x16x32_bf16 v[20:23], v[248:251], v[200:203], v[20:23]\n"
    "v_mfma_f32_16x16x32_bf16 v[24:27], v[248:251], v[216:219], v[24:27]\n"
    "ds_read_b128 v[208:211], v5 offset:2912\n"
    "v_mfma_f32_16x16x32_bf16 v[20:23], v[184:187], v[216:219], v[20:23]\n"
    "v_mfma_f32_16x16x32_bf16 v[24:27], v[184:187], v[172:175], v[24:27]\n"
    "ds_read_b128 v[192:195], v5 offset:3040\n"
    "v_mfma_f32_16x16x32_bf16 v[20:23], v[100:103], v[172:175], v[20:23]\n"
    "s_waitcnt lgkmcnt(3)\n"
    "v_mfma_f32_16x16x32_bf16 v[24:27], v[100:103], v[124:127], v[24:27]\n"
    "ds_read_b128 v[84:87], v5 offset:3168\n"
    "v_mfma_f32_16x16x32_bf16 v[20:23], v[160:163], v[124:127], v[20:23]\n"
    "v_mfma_f32_16x16x32_bf16 v[24:27], v[160:163], v[220:223], v[24:27]\n"
    "ds_read_b128 v[120:123], v5 offset:3296\n"
    "v_mfma_f32_16x16x32_bf16 v[20:23], v[176:179], v[220:223], v[20:23]\n"
    "v_mfma_f32_16x16x32_bf16 v[24:27], v[176:179], v[228:231], v[24:27]\n"
    "ds_read_b128 v[152:155], v5 offset:3424\n"
    "v_mfma_f32_16x16x32_bf16 v[20:23], v[196:199], v[228:231], v[20:23]\n"
    "s_waitcnt lgkmcnt(3)\n"
    "v_mfma_f32_16x16x32_bf16 v[24:27], v[196:199], v[244:247], v[24:27]\n"
    "ds_read_b128 v[200:203], v5 offset:3552\n"
    "v_mfma_f32_16x16x32_bf16 v[20:23], v[136:139], v[244:247], v[20:23]\n"
    "v_mfma_f32_16x16x32_bf16 v[24:27], v[136:139], v[208:211], v[24:27]\n"
    "ds_read_b128 v[216:219], v5 offset:3680\n"
    "v_mfma_f32_16x16x32_bf16 v[20:23], v[112:115], v[208:211], v[20:23]\n"
    "v_mfma_f32_16x16x32_bf16 v[24:27], v[112:115], v[192:195], v[24:27]\n"
    "ds_read_b128 v[172:175], v5 offset:3808\n"
    "v_mfma_f32_16x16x32_bf16 v[20:23], v[148:151], v[192:195], v[20:23]\n"
    "s_waitcnt lgkmcnt(3)\n"
    "v_mfma_f32_16x16x32_bf16 v[24:27], v[148:151], v[84:87], v[24:27]\n"
    "ds_read_b128 v[124:127], v5 offset:3936\n"
    "v_mfma_f32_16x16x32_bf16 v[20:23], v[164:167], v[84:87], v[20:23]\n"
    "v_mfma_f32_16x16x32_bf16 v[24:27], v[164:167], v[120:123], v[24:27]\n"
    "ds_read_b128 v[220:223], v5 offset:4064\n"
    "v_mfma_f32_16x16x32_bf16 v[20:23], v[204:207], v[120:123], v[20:23]\n"
    "ds_read_b128 v[228:231], v5 offset:320\n"
    "v_mfma_f32_16x16x32_bf16 v[24:27], v[204:207], v[152:155], v[24:27]\n"
    "ds_read_b128 v[244:247], v5 offset:448\n"
    "v_mfma_f32_16x16x32_bf16 v[20:23], v[236:239], v[152:155], v[20:23]\n"
    "s_waitcnt lgkmcnt(4)\n"
    "v_mfma_f32_16x16x32_bf16 v[24:27], v[236:239], v[200:203], v[24:27]\n"
    "ds_read_b128 v[208:211], v5 offset:576\n"
    "v_mfma_f32_16x16x32_bf16 v[20:23], v[252:255], v[200:203], v[20:23]\n"
    "v_mfma_f32_16x16x32_bf16 v[24:27], v[252:255], v[216:219], v[24:27]\n"
    "ds_read_b128 v[192:195], v5 offset:704\n"
    "v_mfma_f32_16x16x32_bf16 v[20:23], v[132:135], v[216:219], v[20:23]\n"
    "v_mfma_f32_16x16x32_bf16 v[24:27], v[132:135], v[172:175], v[24:27]\n"
    "ds_read_b128 v[84:87], v5 offset:832\n"
    "v_mfma_f32_16x16x32_bf16 v[20:23], v[168:171], v[172:175], v[20:23]\n"
    "s_waitcnt lgkmcnt(3)\n"
    "v_mfma_f32_16x16x32_bf16 v[24:27], v[168:171], v[124:127], v[24:27]\n"
    "ds_read_b128 v[120:123], v5 offset:960\n"
    "v_mfma_f32_16x16x32_bf16 v[20:23], v[212:215], v[124:127], v[20:23]\n"
    "v_mfma_f32_16x16x32_bf16 v[24:27], v[212:215], v[220:223], v[24:27]\n"
    "ds_read_b128 v[152:155], v5 offset:1088\n"
    "v_mfma_f32_16x16x32_bf16 v[28:31], v[96:99], v[228:231], v[28:31]\n"
    "v_mfma_f32_16x16x32_bf16 v[32:35], v[96:99], v[244:247], v[32:35]\n"
    "ds_read_b128 v[200:203], v5 offset:1216\n"
    "v_mfma_f32_16x16x32_bf16 v[28:31], v[104:107], v[244:247], v[28:31]\n"
    "s_nop 1\n"
    "v_cvt_pk_bf16_f32 v20, v20, v21\n"
    "v_cvt_pk_bf16_f32 v21, v22, v23\n"
    "v_cvt_pk_bf16_f32 v22, v24, v25\n"
    "v_cvt_pk_bf16_f32 v23, v26, v27\n"
    "s_waitcnt lgkmcnt(3)\n"
    "v_mfma_f32_16x16x32_bf16 v[32:35], v[104:107], v[208:211], v[32:35]\n"
    "ds_read_b128 v[216:219], v5 offset:1344\n"
    "v_min_u32 v20, -2, v20\n"
    "v_min_u32 v21, -2, v21\n"
    "v_mfma_f32_16x16x32_bf16 v[28:31], v[108:111], v[208:211], v[28:31]\n"
    "v_min_u32 v22, -2, v22\n"
    "v_min_u32 v23, -2, v23\n"
    "s_xor_b32 s43, s41, 0\n"
    "buffer_store_dwordx4 v[20:23], v8, s[24:27], s43 offen sc1\n"
    "v_mfma_f32_16x16x32_bf16 v[32:35], v[108:111], v[192:195], v[32:35]\n"
    "ds_read_b128 v[172:175], v5 offset:1472\n"
    "v_mfma_f32_16x16x32_bf16 v[28:31], v[180:183], v[192:195], v[28:31]\n"
    "v_mfma_f32_16x16x32_bf16 v[32:35], v[180:183], v[84:87], v[32:35]\n"
    "ds_read_b128 v[124:127], v5 offset:1600\n"
    "v_mfma_f32_16x16x32_bf16 v[28:31], v[116:119], v[84:87], v[28:31]\n"
    "s_waitcnt lgkmcnt(3)\n"
    "v_mfma_f32_16x16x32_bf16 v[32:35], v[116:119], v[120:123], v[32:35]\n"
    "ds_read_b128 v[220:223], v5 offset:1728\n"
    "v_mfma_f32_16x16x32_bf16 v[28:31], v[128:131], v[120:123], v[28:31]\n"
    "v_mfma_f32_16x16x32_bf16 v[32:35], v[128:131], v[152:155], v[32:35]\n"
    "ds_read_b128 v[228:231], v5 offset:1856\n"
    "v_mfma_f32_16x16x32_bf16 v[28:31], v[144:147], v[152:155], v[28:31]\n"
    "v_mfma_f32_16x16x32_bf16 v[32:35], v[144:147], v[200:203], v[32:35]\n"
    "ds_read_b128 v[96:99], v5 offset:1984\n"
    "v_mfma_f32_16x16x32_bf16 v[28:31], v[88:91], v[200:203], v[28:31]\n"
    "s_waitcnt lgkmcnt(3)\n"
    "v_mfma_f32_16x16x32_bf16 v[32:35], v[88:91], v[216:219], v[32:35]\n"
    "ds_read_b128 v[244:247], v5 offset:2144\n"
    "v_mfma_f32_16x16x32_bf16 v[28:31], v[140:143], v[216:219], v[28:31]\n"
    "v_mfma_f32_16x16x32_bf16 v[32:35], v[140:143], v[172:175], v[32:35]\n"
    "ds_read_b128 v[104:107], v5 offset:2272\n"
    "v_mfma_f32_16x16x32_bf16 v[28:31], v[156:159], v[172:175], v[28:31]\n"
    "v_mfma_f32_16x16x32_bf16 v[32:35], v[156:159], v[124:127], v[32:35]\n"
    "ds_read_b128 v[208:211], v5 offset:2400\n"
    "v_mfma_f32_16x16x32_bf16 v[28:31], v[188:191], v[124:127], v[28:31]\n"
    "s_waitcnt lgkmcnt(3)\n"
    "v_mfma_f32_16x16x32_bf16 v[32:35], v[188:191], v[220:223], v[32:35]\n"
    "ds_read_b128 v[108:111], v5 offset:2528\n"
    "v_mfma_f32_16x16x32_bf16 v[28:31], v[92:95], v[220:223], v[28:31]\n"
    "v_mfma_f32_16x16x32_bf16 v[32:35], v[92:95], v[228:231], v[32:35]\n"
    "ds_read_b128 v[192:195], v5 offset:2656\n"
    "v_mfma_f32_16x16x32_bf16 v[28:31], v[224:227], v[228:231], v[28:31]\n"
    "v_mfma_f32_16x16x32_bf16 v[32:35], v[224:227], v[96:99], v[32:35]\n"
    "ds_read_b128 v[180:183], v5 offset:2784\n"
    "v_mfma_f32_16x16x32_bf16 v[28:31], v[232:235], v[96:99], v[28:31]\n"
    "s_waitcnt lgkmcnt(3)\n"
    "v_mfma_f32_16x16x32_bf16 v[32:35], v[232:235], v[244:247], v[32:35]\n"
    "ds_read_b128 v[84:87], v5 offset:2912\n"
    "v_mfma_f32_16x16x32_bf16 v[28:31], v[240:243], v[244:247], v[28:31]\n"
    "v_mfma_f32_16x16x32_bf16 v[32:35], v[240:243], v[104:107], v[32:35]\n"
    "ds_read_b128 v[116:119], v5 offset:3040\n"
    "v_mfma_f32_16x16x32_bf16 v[28:31], v[248:251], v[104:107], v[28:31]\n"
    "v_mfma_f32_16x16x32_bf16 v[32:35], v[248:251], v[208:211], v[32:35]\n"
    "ds_read_b128 v[120:123], v5 offset:3168\n"
    "v_mfma_f32_16x16x32_bf16 v[28:31], v[184:187], v[208:211], v[28:31]\n"
    "s_waitcnt lgkmcnt(3)\n"
    "v_mfma_f32_16x16x32_bf16 v[32:35], v[184:187], v[108:111], v[32:35]\n"
    "ds_read_b128 v[128:131], v5 offset:3296\n"
    "v_mfma_f32_16x16x32_bf16 v[28:31], v[100:103], v[108:111], v[28:31]\n"
    "v_mfma_f32_16x16x32_bf16 v[32:35], v[100:103], v[192:195], v[32:35]\n"
    "ds_read_b128 v[152:155], v5 offset:3424\n"
    "v_mfma_f32_16x16x32_bf16 v[28:31], v[160:163], v[192:195], v[28:31]\n"
    "v_mfma_f32_16x16x32_bf16 v[32:35], v[160:163], v[180:183], v[32:35]\n"
    "ds_read_b128 v[144:147], v5 offset:3552\n"
    "v_mfma_f32_16x16x32_bf16 v[28:31], v[176:179], v[180:183], v[28:31]\n"
    "s_waitcnt lgkmcnt(3)\n"
    "v_mfma_f32_16x16x32_bf16 v[32:35], v[176:179], v[84:87], v[32:35]\n"
    "ds_read_b128 v[200:203], v5 offset:3680\n"
    "v_mfma_f32_16x16x32_bf16 v[28:31], v[196:199], v[84:87], v[28:31]\n"
    "v_mfma_f32_16x16x32_bf16 v[32:35], v[196:199], v[116:119], v[32:35]\n"
    "ds_read_b128 v[88:91], v5 offset:3808\n"
    "v_mfma_f32_16x16x32_bf16 v[28:31], v[136:139], v[116:119], v[28:31]\n"
    "v_mfma_f32_16x16x32_bf16 v[32:35], v[136:139], v[120:123], v[32:35]\n"
    "ds_read_b128 v[216:219], v5 offset:3936\n"
    "v_mfma_f32_16x16x32_bf16 v[28:31], v[112:115], v[120:123], v[28:31]\n"
    "s_waitcnt lgkmcnt(3)\n"
    "v_mfma_f32_16x16x32_bf16 v[32:35], v[112:115], v[128:131], v[32:35]\n"
    "ds_read_b128 v[140:143], v5 offset:4064\n"
    "v_mfma_f32_16x16x32_bf16 v[28:31], v[148:151], v[128:131], v[28:31]\n"
    "v_mfma_f32_16x16x32_bf16 v[32:35], v[148:151], v[152:155], v[32:35]\n"
    "ds_read_b128 v[172:175], v5 offset:4224\n"
    "v_mfma_f32_16x16x32_bf16 v[28:31], v[164:167], v[152:155], v[28:31]\n"
    "v_mfma_f32_16x16x32_bf16 v[32:35], v[164:167], v[144:147], v[32:35]\n"
    "ds_read_b128 v[156:159], v5 offset:4352\n"
    "v_mfma_f32_16x16x32_bf16 v[28:31], v[204:207], v[144:147], v[28:31]\n"
    "s_waitcnt lgkmcnt(3)\n"
    "v_mfma_f32_16x16x32_bf16 v[32:35], v[204:207], v[200:203], v[32:35]\n"
    "v_mfma_f32_16x16x32_bf16 v[28:31], v[236:239], v[200:203], v[28:31]\n"
    "v_mfma_f32_16x16x32_bf16 v[32:35], v[236:239], v[88:91], v[32:35]\n"
    "v_mfma_f32_16x16x32_bf16 v[28:31], v[252:255], v[88:91], v[28:31]\n"
    "v_mfma_f32_16x16x32_bf16 v[32:35], v[252:255], v[216:219], v[32:35]\n"
    "v_mfma_f32_16x16x32_bf16 v[28:31], v[132:135], v[216:219], v[28:31]\n"
    "s_waitcnt lgkmcnt(0)\n"
    "v_mfma_f32_16x16x32_bf16 v[32:35], v[132:135], v[140:143], v[32:35]\n"
    "v_mfma_f32_16x16x32_bf16 v[28:31], v[168:171], v[140:143], v[28:31]\n"
    "v_mfma_f32_16x16x32_bf16 v[32:35], v[168:171], v[172:175], v[32:35]\n"
    "v_mfma_f32_16x16x32_bf16 v[28:31], v[212:215], v[172:175], v[28:31]\n"
    "v_mfma_f32_16x16x32_bf16 v[32:35], v[212:215], v[156:159], v[32:35]\n"
    "s_nop 6\n"
    "ds_write_b128 v16, v[28:31]\n"
    "ds_write_b128 v16, v[32:35] offset:1088\n"
    "s_waitcnt lgkmcnt(0)\n"
    "s_barrier\n"
    "ds_read_b128 v[96:99], v14\n"
    "ds_read_b128 v[100:103], v14 offset:2176\n"
    "ds_read_b128 v[104:107], v14 offset:4352\n"
    "s_waitcnt lgkmcnt(1)\n"
    "v_fmac_f32 v100, s56, v96\n"
    "v_fmac_f32 v101, s56, v97\n"
    "v_fmac_f32 v102, s56, v98\n"
    "v_fmac_f32 v103, s56, v99\n"
    "v_exp_f32 v100, v100\n"
    "v_exp_f32 v101, v101\n"
    "v_exp_f32 v102, v102\n"
    "v_exp_f32 v103, v103\n"
    "v_add_f32 v100, 1.0, v100\n"
    "v_add_f32 v101, 1.0, v101\n"
    "v_add_f32 v102, 1.0, v102\n"
    "v_add_f32 v103, 1.0, v103\n"
    "v_rcp_f32 v100, v100\n"
    "v_rcp_f32 v101, v101\n"
    "v_rcp_f32 v102, v102\n"
    "v_rcp_f32 v103, v103\n"
    "v_fma_f32 v100, v100, s58, v17\n"
    "v_fma_f32 v101, v101, s58, v17\n"
    "v_fma_f32 v102, v102, s58, v17\n"
    "v_fma_f32 v103, v103, s58, v17\n"
    "s_waitcnt lgkmcnt(0)\n"
    "v_mul_f32 v100, v100, v104\n"
    "v_mul_f32 v101, v101, v105\n"
    "v_mul_f32 v102, v102, v106\n"
    "v_mul_f32 v103, v103, v107\n"
    "v_cvt_pk_bf16_f32 v96, v100, v101\n"
    "v_cvt_pk_bf16_f32 v97, v102, v103\n"
    "buffer_store_dwordx2 v[96:97], v15, s[28:31], 0 offen\n"
    "s_endpgm\n"
    ".Lt2_2%=:\n"
    ".Lpad4%=:\n"
    "s_cbranch_execz .Lpad4r%=\n"
    "v_mfma_f32_16x16x32_bf16 v[48:51], v[252:255], v[136:139], v[48:51]\n"
    "ds_read_b128 v[124:127], v5 offset:5696\n"
    "v_mfma_f32_16x16x32_bf16 v[56:59], v[252:255], v[144:147], v[56:59]\n"
    "s_barrier\n"
    "ds_read_b64_tr_b16 v[96:97], v6 offset:20480\n"
    "v_mfma_f32_16x16x32_bf16 v[64:67], v[252:255], v[152:155], v[64:67]\n"
    "ds_read_b64_tr_b16 v[98:99], v6 offset:20992\n"
    "v_mfma_f32_16x16x32_bf16 v[72:75], v[252:255], v[88:91], v[72:75]\n"
    "ds_read_b128 v[184:187], v5 offset:1088\n"
    "s_waitcnt lgkmcnt(4)\n"
    "v_mfma_f32_16x16x32_bf16 v[80:83], v[252:255], v[112:115], v[80:83]\n"
    "ds_read_b128 v[132:135], v5 offset:1216\n"
    "v_mfma_f32_16x16x32_bf16 v[192:195], v[224:227], v[140:143], v[192:195]\n"
    "ds_read_b128 v[100:103], v5 offset:1344\n"
    "v_mfma_f32_16x16x32_bf16 v[196:199], v[224:227], v[148:151], v[196:199]\n"
    "ds_read_b128 v[160:163], v5 offset:1472\n"
    "ds_read_b64_tr_b16 v[104:105], v6 offset:24576\n"
    "v_mfma_f32_16x16x32_bf16 v[200:203], v[224:227], v[156:159], v[200:203]\n"
    "ds_read_b64_tr_b16 v[106:107], v6 offset:25088\n"
    "v_mfma_f32_16x16x32_bf16 v[204:207], v[224:227], v[172:175], v[204:207]\n"
    "ds_read_b128 v[168:171], v5 offset:1600\n"
    "v_mfma_f32_16x16x32_bf16 v[208:211], v[224:227], v[188:191], v[208:211]\n"
    "ds_read_b64_tr_b16 v[108:109], v6 offset:28672\n"
    "ds_read_b64_tr_b16 v[110:111], v6 offset:29184\n"
    "v_mfma_f32_16x16x32_bf16 v[212:215], v[224:227], v[164:167], v[212:215]\n"
    "v_add_f32 v20, v20, v192\n"
    "v_mfma_f32_16x16x32_bf16 v[216:219], v[224:227], v[92:95], v[216:219]\n"
    "v_add_f32 v21, v21, v193\n"
    "s_waitcnt lgkmcnt(7)\n"
    "v_mfma_f32_16x16x32_bf16 v[220:223], v[224:227], v[124:127], v[220:223]\n"
    "ds_read_b128 v[176:179], v5 offset:1728\n"
    "v_mfma_f32_16x16x32_bf16 v[52:55], v[96:99], v[184:187], v[52:55]\n"
    "ds_read_b64_tr_b16 v[180:181], v6 offset:32768\n"
    "ds_read_b64_tr_b16 v[182:183], v6 offset:33280\n"
    "v_mfma_f32_16x16x32_bf16 v[56:59], v[96:99], v[132:135], v[56:59]\n"
    "v_add_f32 v22, v22, v194\n"
    "s_waitcnt lgkmcnt(6)\n"
    "v_mfma_f32_16x16x32_bf16 v[60:63], v[96:99], v[100:103], v[60:63]\n"
    "v_add_f32 v23, v23, v195\n"
    "v_mfma_f32_16x16x32_bf16 v[64:67], v[96:99], v[160:163], v[64:67]\n"
    "ds_read_b128 v[84:87], v5 offset:1856\n"
    "v_mfma_f32_16x16x32_bf16 v[52:55], v[104:107], v[132:135], v[52:55]\n"
    "ds_read_b64_tr_b16 v[116:117], v6 offset:36864\n"
    "ds_read_b64_tr_b16 v[118:119], v6 offset:37376\n"
    "v_mfma_f32_16x16x32_bf16 v[56:59], v[104:107], v[100:103], v[56:59]\n"
    "v_add_f32 v24, v24, v192\n"
    "v_mfma_f32_16x16x32_bf16 v[60:63], v[104:107], v[160:163], v[60:63]\n"
    "v_add_f32 v25, v25, v193\n"
    "s_waitcnt lgkmcnt(5)\n"
    "v_mfma_f32_16x16x32_bf16 v[64:67], v[104:107], v[168:171], v[64:67]\n"
    "ds_read_b128 v[120:123], v5 offset:1984\n"
    "v_mfma_f32_16x16x32_bf16 v[52:55], v[108:111], v[100:103], v[52:55]\n"
    "ds_read_b64_tr_b16 v[128:129], v6 offset:40960\n"
    "ds_read_b64_tr_b16 v[130:131], v6 offset:41472\n"
    "v_mfma_f32_16x16x32_bf16 v[56:59], v[108:111], v[160:163], v[56:59]\n"
    "v_add_f32 v26, v26, v194\n"
    "v_mfma_f32_16x16x32_bf16 v[60:63], v[108:111], v[168:171], v[60:63]\n"
    "v_add_f32 v27, v27, v195\n"
    "v_mfma_f32_16x16x32_bf16 v[64:67], v[108:111], v[176:179], v[64:67]\n"
    "ds_read_b128 v[136:139], v5 offset:2144\n"
    "s_waitcnt lgkmcnt(4)\n"
    "v_mfma_f32_16x16x32_bf16 v[52:55], v[180:183], v[160:163], v[52:55]\n"
    "ds_read_b64_tr_b16 v[144:145], v6 offset:45056\n"
    "ds_read_b64_tr_b16 v[146:147], v6 offset:45568\n"
    "v_mfma_f32_16x16x32_bf16 v[56:59], v[180:183], v[168:171], v[56:59]\n"
    "v_add_f32 v28, v28, v196\n"
    "v_mfma_f32_16x16x32_bf16 v[60:63], v[180:183], v[176:179], v[60:63]\n"
    "v_add_f32 v29, v29, v197\n"
    "v_mfma_f32_16x16x32_bf16 v[64:67], v[180:183], v[84:87], v[64:67]\n"
    "ds_read_b128 v[152:155], v5 offset:2272\n"
    "v_mfma_f32_16x16x32_bf16 v[52:55], v[116:119], v[168:171], v[52:55]\n"
    "ds_read_b64_tr_b16 v[88:89], v6 offset:49152\n"
    "ds_read_b64_tr_b16 v[90:91], v6 offset:49664\n"
    "v_mfma_f32_16x16x32_bf16 v[56:59], v[116:119], v[176:179], v[56:59]\n"
    "v_add_f32 v30, v30, v198\n"
    "v_mfma_f32_16x16x32_bf16 v[60:63], v[116:119], v[84:87], v[60:63]\n"
    "v_add_f32 v31, v31, v199\n"
    "s_waitcnt lgkmcnt(5)\n"
    "v_mfma_f32_16x16x32_bf16 v[64:67], v[116:119], v[120:123], v[64:67]\n"
    "ds_read_b128 v[112:115], v5 offset:2400\n"
    "v_mfma_f32_16x16x32_bf16 v[52:55], v[128:131], v[176:179], v[52:55]\n"
    "ds_read_b64_tr_b16 v[140:141], v6 offset:53248\n"
    "ds_read_b64_tr_b16 v[142:143], v6 offset:53760\n"
    "v_mfma_f32_16x16x32_bf16 v[56:59], v[128:131], v[84:87], v[56:59]\n"
    "v_add_f32 v32, v32, v196\n"
    "v_mfma_f32_16x16x32_bf16 v[60:63], v[128:131], v[120:123], v[60:63]\n"
    "v_add_f32 v33, v33, v197\n"
    "v_mfma_f32_16x16x32_bf16 v[64:67], v[128:131], v[136:139], v[64:67]\n"
    "ds_read_b128 v[148:151], v5 offset:2528\n"
    "s_waitcnt lgkmcnt(4)\n"
    "v_mfma_f32_16x16x32_bf16 v[52:55], v[144:147], v[84:87], v[52:55]\n"
    "s_barrier\n"
    "ds_read_b64_tr_b16 v[156:157], v6 offset:57344\n"
    "v_mfma_f32_16x16x32_bf16 v[56:59], v[144:147], v[120:123], v[56:59]\n"
    "ds_read_b64_tr_b16 v[158:159], v6 offset:57856\n"
    "v_add_f32 v34, v34, v198\n"
    "v_mfma_f32_16x16x32_bf16 v[60:63], v[144:147], v[136:139], v[60:63]\n"
    "v_add_f32 v35, v35, v199\n"
    "v_mfma_f32_16x16x32_bf16 v[64:67], v[144:147], v[152:155], v[64:67]\n"
    "ds_read_b128 v[172:175], v5 offset:2656\n"
    "v_mfma_f32_16x16x32_bf16 v[52:55], v[88:91], v[120:123], v[52:55]\n"
    "ds_read_b64_tr_b16 v[188:189], v6 offset:61440\n"
    "ds_read_b64_tr_b16 v[190:191], v6 offset:61952\n"
    "v_mfma_f32_16x16x32_bf16 v[56:59], v[88:91], v[136:139], v[56:59]\n"
    "v_add_f32 v36, v36, v200\n"
    "v_mfma_f32_16x16x32_bf16 v[60:63], v[88:91], v[152:155], v[60:63]\n"
    "v_add_f32 v37, v37, v201\n"
    "s_waitcnt lgkmcnt(5)\n"
    "v_mfma_f32_16x16x32_bf16 v[64:67], v[88:91], v[112:115], v[64:67]\n"
    "ds_read_b128 v[164:167], v5 offset:2784\n"
    "v_mfma_f32_16x16x32_bf16 v[52:55], v[140:143], v[136:139], v[52:55]\n"
    "ds_read_b64_tr_b16 v[92:93], v7 offset:0\n"
    "ds_read_b64_tr_b16 v[94:95], v7 offset:512\n"
    "v_mfma_f32_16x16x32_bf16 v[56:59], v[140:143], v[152:155], v[56:59]\n"
    "v_add_f32 v38, v38, v202\n"
    "v_mfma_f32_16x16x32_bf16 v[60:63], v[140:143], v[112:115], v[60:63]\n"
    "v_add_f32 v39, v39, v203\n"
    "v_mfma_f32_16x16x32_bf16 v[64:67], v[140:143], v[148:151], v[64:67]\n"
    "ds_read_b128 v[124:127], v5 offset:2912\n"
    "s_waitcnt lgkmcnt(4)\n"
    "v_mfma_f32_16x16x32_bf16 v[52:55], v[156:159], v[152:155], v[52:55]\n"
    "ds_read_b64_tr_b16 v[224:225], v7 offset:4096\n"
    "ds_read_b64_tr_b16 v[226:227], v7 offset:4608\n"
    "v_mfma_f32_16x16x32_bf16 v[56:59], v[156:159], v[112:115], v[56:59]\n"
    "v_add_f32 v40, v40, v200\n"
    "v_mfma_f32_16x16x32_bf16 v[60:63], v[156:159], v[148:151], v[60:63]\n"
    "v_add_f32 v41, v41, v201\n"
    "v_mfma_f32_16x16x32_bf16 v[64:67], v[156:159], v[172:175], v[64:67]\n"
    "ds_read_b128 v[228:231], v5 offset:3040\n"
    "v_mfma_f32_16x16x32_bf16 v[52:55], v[188:191], v[112:115], v[52:55]\n"
    "ds_read_b64_tr_b16 v[232:233], v7 offset:8192\n"
    "ds_read_b64_tr_b16 v[234:235], v7 offset:8704\n"
    "v_mfma_f32_16x16x32_bf16 v[56:59], v[188:191], v[148:151], v[56:59]\n"
    "v_add_f32 v42, v42, v202\n"
    "v_mfma_f32_16x16x32_bf16 v[60:63], v[188:191], v[172:175], v[60:63]\n"
    "v_add_f32 v43, v43, v203\n"
    "s_waitcnt lgkmcnt(5)\n"
    "v_mfma_f32_16x16x32_bf16 v[64:67], v[188:191], v[164:167], v[64:67]\n"
    "ds_read_b128 v[236:239], v5 offset:3168\n"
    "v_mfma_f32_16x16x32_bf16 v[52:55], v[92:95], v[148:151], v[52:55]\n"
    "ds_read_b64_tr_b16 v[240:241], v7 offset:12288\n"
    "ds_read_b64_tr_b16 v[242:243], v7 offset:12800\n"
    "v_mfma_f32_16x16x32_bf16 v[56:59], v[92:95], v[172:175], v[56:59]\n"
    "v_add_f32 v44, v44, v204\n"
    "v_mfma_f32_16x16x32_bf16 v[60:63], v[92:95], v[164:167], v[60:63]\n"
    "v_add_f32 v45, v45, v205\n"
    "v_mfma_f32_16x16x32_bf16 v[64:67], v[92:95], v[124:127], v[64:67]\n"
    "ds_read_b128 v[244:247], v5 offset:3296\n"
    "s_waitcnt lgkmcnt(4)\n"
    "v_mfma_f32_16x16x32_bf16 v[52:55], v[224:227], v[172:175], v[52:55]\n"
    "ds_read_b64_tr_b16 v[248:249], v7 offset:16384\n"
    "ds_read_b64_tr_b16 v[250:251], v7 offset:16896\n"
    "v_mfma_f32_16x16x32_bf16 v[56:59], v[224:227], v[164:167], v[56:59]\n"
    "v_add_f32 v46, v46, v206\n"
    "v_mfma_f32_16x16x32_bf16 v[60:63], v[224:227], v[124:127], v[60:63]\n"
    "v_add_f32 v47, v47, v207\n"
    "v_mfma_f32_16x16x32_bf16 v[64:67], v[224:227], v[228:231], v[64:67]\n"
    "ds_read_b128 v[252:255], v5 offset:3424\n"
    "v_mfma_f32_16x16x32_bf16 v[52:55], v[232:235], v[164:167], v[52:55]\n"
    "ds_read_b64_tr_b16 v[184:185], v7 offset:20480\n"
    "ds_read_b64_tr_b16 v[186:187], v7 offset:20992\n"
    "v_mfma_f32_16x16x32_bf16 v[56:59], v[232:235], v[124:127], v[56:59]\n"
    "v_add_f32 v48, v48, v204\n"
    "v_mfma_f32_16x16x32_bf16 v[60:63], v[232:235], v[228:231], v[60:63]\n"
    "v_add_f32 v49, v49, v205\n"
    "s_waitcnt lgkmcnt(5)\n"
    "v_mfma_f32_16x16x32_bf16 v[64:67], v[232:235], v[236:239], v[64:67]\n"
    "ds_read_b128 v[132:135], v5 offset:3552\n"
    "v_mfma_f32_16x16x32_bf16 v[52:55], v[240:243], v[124:127], v[52:55]\n"
    "ds_read_b64_tr_b16 v[100:101], v7 offset:24576\n"
    "ds_read_b64_tr_b16 v[102:103], v7 offset:25088\n"
    "v_mfma_f32_16x16x32_bf16 v[56:59], v[240:243], v[228:231], v[56:59]\n"
    "v_add_f32 v50, v50, v206\n"
    "v_mfma_f32_16x16x32_bf16 v[60:63], v[240:243], v[236:239], v[60:63]\n"
    "v_add_f32 v51, v51, v207\n"
    "v_mfma_f32_16x16x32_bf16 v[64:67], v[240:243], v[244:247], v[64:67]\n"
    "ds_read_b128 v[192:195], v5 offset:3680\n"
    "s_waitcnt lgkmcnt(4)\n"
    "v_mfma_f32_16x16x32_bf16 v[52:55], v[248:251], v[228:231], v[52:55]\n"
    "ds_read_b64_tr_b16 v[160:161], v7 offset:28672\n"
    "ds_read_b64_tr_b16 v[162:163], v7 offset:29184\n"
    "v_mfma_f32_16x16x32_bf16 v[56:59], v[248:251], v[236:239], v[56:59]\n"
    "v_add_f32 v68, v68, v216\n"
    "v_mfma_f32_16x16x32_bf16 v[60:63], v[248:251], v[244:247], v[60:63]\n"
    "v_add_f32 v69, v69, v217\n"
    "v_mfma_f32_16x16x32_bf16 v[64:67], v[248:251], v[252:255], v[64:67]\n"
    "ds_read_b128 v[168:171], v5 offset:3808\n"
    "v_mfma_f32_16x16x32_bf16 v[52:55], v[184:187], v[236:239], v[52:55]\n"
    "s_barrier\n"
    "ds_read_b64_tr_b16 v[176:177], v7 offset:32768\n"
    "v_mfma_f32_16x16x32_bf16 v[56:59], v[184:187], v[244:247], v[56:59]\n"
    "ds_read_b64_tr_b16 v[178:179], v7 offset:33280\n"
    "v_add_f32 v70, v70, v218\n"
    "v_mfma_f32_16x16x32_bf16 v[60:63], v[184:187], v[252:255], v[60:63]\n"
    "v_add_f32 v71, v71, v219\n"
    "s_waitcnt lgkmcnt(5)\n"
    "v_mfma_f32_16x16x32_bf16 v[64:67], v[184:187], v[132:135], v[64:67]\n"
    "ds_read_b128 v[84:87], v5 offset:3936\n"
    "v_mfma_f32_16x16x32_bf16 v[52:55], v[100:103], v[244:247], v[52:55]\n"
    "ds_read_b64_tr_b16 v[196:197], v7 offset:36864\n"
    "ds_read_b64_tr_b16 v[198:199], v7 offset:37376\n"
    "v_mfma_f32_16x16x32_bf16 v[56:59], v[100:103], v[252:255], v[56:59]\n"
    "v_add_f32 v72, v72, v216\n"
    "v_mfma_f32_16x16x32_bf16 v[60:63], v[100:103], v[132:135], v[60:63]\n"
    "v_add_f32 v73, v73, v217\n"
    "v_mfma_f32_16x16x32_bf16 v[64:67], v[100:103], v[192:195], v[64:67]\n"
    "ds_read_b128 v[120:123], v5 offset:4064\n"
    "s_waitcnt lgkmcnt(5)\n"
    "v_mfma_f32_16x16x32_bf16 v[52:55], v[160:163], v[252:255], v[52:55]\n"
    "ds_read_b64_tr_b16 v[136:137], v6 offset:0\n"
    "ds_read_b64_tr_b16 v[138:139], v6 offset:512\n"
    "v_mfma_f32_16x16x32_bf16 v[56:59], v[160:163], v[132:135], v[56:59]\n"
    "v_add_f32 v74, v74, v218\n"
    "v_mfma_f32_16x16x32_bf16 v[60:63], v[160:163], v[192:195], v[60:63]\n"
    ".Lpad5%=:\n"
    "s_cbranch_execz .Lpad5r%=\n"
    "v_add_f32 v75, v75, v219\n"
    "v_mfma_f32_16x16x32_bf16 v[64:67], v[160:163], v[168:171], v[64:67]\n"
    "ds_read_b128 v[152:155], v5 offset:4224\n"
    "s_waitcnt lgkmcnt(4)\n"
    "v_mfma_f32_16x16x32_bf16 v[52:55], v[176:179], v[132:135], v[52:55]\n"
    "ds_read_b64_tr_b16 v[112:113], v6 offset:4096\n"
    "ds_read_b64_tr_b16 v[114:115], v6 offset:4608\n"
    "v_mfma_f32_16x16x32_bf16 v[56:59], v[176:179], v[192:195], v[56:59]\n"
    "v_add_f32 v76, v76, v220\n"
    "v_mfma_f32_16x16x32_bf16 v[60:63], v[176:179], v[168:171], v[60:63]\n"
    "v_add_f32 v77, v77, v221\n"
    "v_mfma_f32_16x16x32_bf16 v[64:67], v[176:179], v[84:87], v[64:67]\n"
    "ds_read_b128 v[200:203], v5 offset:4352\n"
    "v_mfma_f32_16x16x32_bf16 v[52:55], v[196:199], v[192:195], v[52:55]\n"
    "ds_read_b64_tr_b16 v[148:149], v6 offset:8192\n"
    "ds_read_b64_tr_b16 v[150:151], v6 offset:8704\n"
    "v_mfma_f32_16x16x32_bf16 v[56:59], v[196:199], v[168:171], v[56:59]\n"
    "v_add_f32 v78, v78, v222\n"
    "v_mfma_f32_16x16x32_bf16 v[60:63], v[196:199], v[84:87], v[60:63]\n"
    "v_add_f32 v79, v79, v223\n"
    "s_waitcnt lgkmcnt(5)\n"
    "v_mfma_f32_16x16x32_bf16 v[64:67], v[196:199], v[120:123], v[64:67]\n"
    "ds_read_b128 v[172:175], v5 offset:4480\n"
    "v_mfma_f32_16x16x32_bf16 v[52:55], v[136:139], v[168:171], v[52:55]\n"
    "ds_read_b64_tr_b16 v[164:165], v6 offset:12288\n"
    "ds_read_b64_tr_b16 v[166:167], v6 offset:12800\n"
    "v_mfma_f32_16x16x32_bf16 v[56:59], v[136:139], v[84:87], v[56:59]\n"
    "v_add_f32 v80, v80, v220\n"
    "v_mfma_f32_16x16x32_bf16 v[60:63], v[136:139], v[120:123], v[60:63]\n"
    "v_add_f32 v81, v81, v221\n"
    "v_mfma_f32_16x16x32_bf16 v[64:67], v[136:139], v[152:155], v[64:67]\n"
    "ds_read_b128 v[124:127], v5 offset:4608\n"
    "s_waitcnt lgkmcnt(4)\n"
    "v_mfma_f32_16x16x32_bf16 v[52:55], v[112:115], v[84:87], v[52:55]\n"
    "ds_read_b64_tr_b16 v[204:205], v6 offset:16384\n"
    "ds_read_b64_tr_b16 v[206:207], v6 offset:16896\n"
    "v_mfma_f32_16x16x32_bf16 v[56:59], v[112:115], v[120:123], v[56:59]\n"
    "v_add_f32 v82, v82, v222\n"
    "v_mfma_f32_16x16x32_bf16 v[60:63], v[112:115], v[152:155], v[60:63]\n"
    "v_add_f32 v83, v83, v223\n"
    "v_mfma_f32_16x16x32_bf16 v[64:67], v[112:115], v[200:203], v[64:67]\n"
    "ds_read_b128 v[228:231], v5 offset:4736\n"
    "v_mfma_f32_16x16x32_bf16 v[52:55], v[148:151], v[120:123], v[52:55]\n"
    "s_barrier\n"
    "ds_read_b64_tr_b16 v[236:237], v6 offset:20480\n"
    "v_mfma_f32_16x16x32_bf16 v[56:59], v[148:151], v[152:155], v[56:59]\n"
    "ds_read_b64_tr_b16 v[238:239], v6 offset:20992\n"
    "v_mfma_f32_16x16x32_bf16 v[60:63], v[148:151], v[200:203], v[60:63]\n"
    "s_waitcnt lgkmcnt(5)\n"
    "v_mfma_f32_16x16x32_bf16 v[64:67], v[148:151], v[172:175], v[64:67]\n"
    "ds_read_b128 v[244:247], v5 offset:4864\n"
    "v_mfma_f32_16x16x32_bf16 v[52:55], v[164:167], v[152:155], v[52:55]\n"
    "ds_read_b64_tr_b16 v[252:253], v6 offset:24576\n"
    "ds_read_b64_tr_b16 v[254:255], v6 offset:25088\n"
    "v_mfma_f32_16x16x32_bf16 v[56:59], v[164:167], v[200:203], v[56:59]\n"
    "v_mfma_f32_16x16x32_bf16 v[60:63], v[164:167], v[172:175], v[60:63]\n"
    "v_mfma_f32_16x16x32_bf16 v[64:67], v[164:167], v[124:127], v[64:67]\n"
    "ds_read_b128 v[216:219], v5 offset:4992\n"
    "s_waitcnt lgkmcnt(5)\n"
    "v_mfma_f32_16x16x32_bf16 v[52:55], v[204:207], v[200:203], v[52:55]\n"
    "ds_read_b64_tr_b16 v[132:133], v6 offset:28672\n"
    "ds_read_b64_tr_b16 v[134:135], v6 offset:29184\n"
    "v_mfma_f32_16x16x32_bf16 v[56:59], v[204:207], v[172:175], v[56:59]\n"
    "v_mfma_f32_16x16x32_bf16 v[60:63], v[204:207], v[124:127], v[60:63]\n"
    "v_mfma_f32_16x16x32_bf16 v[64:67], v[204:207], v[228:231], v[64:67]\n"
    "ds_read_b128 v[192:195], v5 offset:5120\n"
    "s_waitcnt lgkmcnt(4)\n"
    "v_mfma_f32_16x16x32_bf16 v[52:55], v[236:239], v[172:175], v[52:55]\n"
    "ds_read_b64_tr_b16 v[168:169], v6 offset:32768\n"
    "ds_read_b64_tr_b16 v[170:171], v6 offset:33280\n"
    "v_mfma_f32_16x16x32_bf16 v[56:59], v[236:239], v[124:127], v[56:59]\n"
    "v_mfma_f32_16x16x32_bf16 v[60:63], v[236:239], v[228:231], v[60:63]\n"
    "v_mfma_f32_16x16x32_bf16 v[64:67], v[236:239], v[244:247], v[64:67]\n"
    "ds_read_b128 v[84:87], v5 offset:5248\n"
    "v_mfma_f32_16x16x32_bf16 v[52:55], v[252:255], v[124:127], v[52:55]\n"
    "ds_read_b64_tr_b16 v[220:221], v6 offset:36864\n"
    "ds_read_b64_tr_b16 v[222:223], v6 offset:37376\n"
    "v_mfma_f32_16x16x32_bf16 v[56:59], v[252:255], v[228:231], v[56:59]\n"
    "v_mfma_f32_16x16x32_bf16 v[60:63], v[252:255], v[244:247], v[60:63]\n"
    "s_waitcnt lgkmcnt(5)\n"
    "v_mfma_f32_16x16x32_bf16 v[64:67], v[252:255], v[216:219], v[64:67]\n"
    "ds_read_b128 v[120:123], v5 offset:5376\n"
    "v_mfma_f32_16x16x32_bf16 v[52:55], v[132:135], v[228:231], v[52:55]\n"
    "ds_read_b128 v[152:155], v5 offset:1600\n"
    "v_mfma_f32_16x16x32_bf16 v[56:59], v[132:135], v[244:247], v[56:59]\n"
    "ds_read_b128 v[200:203], v5 offset:1728\n"
    "v_mfma_f32_16x16x32_bf16 v[60:63], v[132:135], v[216:219], v[60:63]\n"
    "ds_read_b128 v[172:175], v5 offset:1856\n"
    "v_mfma_f32_16x16x32_bf16 v[64:67], v[132:135], v[192:195], v[64:67]\n"
    "ds_read_b128 v[124:127], v5 offset:1984\n"
    "s_waitcnt lgkmcnt(5)\n"
    "v_mfma_f32_16x16x32_bf16 v[52:55], v[168:171], v[244:247], v[52:55]\n"
    "v_mfma_f32_16x16x32_bf16 v[56:59], v[168:171], v[216:219], v[56:59]\n"
    "v_mfma_f32_16x16x32_bf16 v[60:63], v[168:171], v[192:195], v[60:63]\n"
    "v_mfma_f32_16x16x32_bf16 v[64:67], v[168:171], v[84:87], v[64:67]\n"
    "ds_read_b128 v[228:231], v5 offset:2144\n"
    "v_mfma_f32_16x16x32_bf16 v[52:55], v[220:223], v[216:219], v[52:55]\n"
    "v_mfma_f32_16x16x32_bf16 v[56:59], v[220:223], v[192:195], v[56:59]\n"
    "v_mfma_f32_16x16x32_bf16 v[60:63], v[220:223], v[84:87], v[60:63]\n"
    "s_waitcnt lgkmcnt(1)\n"
    "v_mfma_f32_16x16x32_bf16 v[64:67], v[220:223], v[120:123], v[64:67]\n"
    "ds_read_b128 v[244:247], v5 offset:2272\n"
    "v_mfma_f32_16x16x32_bf16 v[68:71], v[96:99], v[152:155], v[68:71]\n"
    "v_mfma_f32_16x16x32_bf16 v[72:75], v[96:99], v[200:203], v[72:75]\n"
    "v_mfma_f32_16x16x32_bf16 v[76:79], v[96:99], v[172:175], v[76:79]\n"
    "v_add_f32 v52, v52, v208\n"
    "v_add_f32 v53, v53, v209\n"
    "v_mfma_f32_16x16x32_bf16 v[80:83], v[96:99], v[124:127], v[80:83]\n"
    "ds_read_b128 v[216:219], v5 offset:2400\n"
    "v_mfma_f32_16x16x32_bf16 v[68:71], v[104:107], v[200:203], v[68:71]\n"
    "v_add_f32 v54, v54, v210\n"
    "v_add_f32 v55, v55, v211\n"
    "v_mfma_f32_16x16x32_bf16 v[72:75], v[104:107], v[172:175], v[72:75]\n"
    "v_add_f32 v56, v56, v208\n"
    "v_add_f32 v57, v57, v209\n"
    "v_mfma_f32_16x16x32_bf16 v[76:79], v[104:107], v[124:127], v[76:79]\n"
    "v_add_f32 v58, v58, v210\n"
    "v_add_f32 v59, v59, v211\n"
    "s_waitcnt lgkmcnt(1)\n"
    "v_mfma_f32_16x16x32_bf16 v[80:83], v[104:107], v[228:231], v[80:83]\n"
    "ds_read_b128 v[192:195], v5 offset:2528\n"
    "v_mfma_f32_16x16x32_bf16 v[68:71], v[108:111], v[172:175], v[68:71]\n"
    "v_add_f32 v60, v60, v212\n"
    "v_add_f32 v61, v61, v213\n"
    "v_mfma_f32_16x16x32_bf16 v[72:75], v[108:111], v[124:127], v[72:75]\n"
    "v_add_f32 v62, v62, v214\n"
    "v_add_f32 v63, v63, v215\n"
    "v_mfma_f32_16x16x32_bf16 v[76:79], v[108:111], v[228:231], v[76:79]\n"
    "v_add_f32 v64, v64, v212\n"
    "v_add_f32 v65, v65, v213\n"
    "v_mfma_f32_16x16x32_bf16 v[80:83], v[108:111], v[244:247], v[80:83]\n"
    "ds_read_b128 v[84:87], v5 offset:2656\n"
    "v_mfma_f32_16x16x32_bf16 v[68:71], v[180:183], v[124:127], v[68:71]\n"
    "v_add_f32 v66, v66, v214\n"
    "v_add_f32 v67, v67, v215\n"
    "v_mfma_f32_16x16x32_bf16 v[72:75], v[180:183], v[228:231], v[72:75]\n"
    "v_cvt_pk_bf16_f32 v52, v52, v53\n"
    "v_cvt_pk_bf16_f32 v53, v54, v55\n"
    "v_mfma_f32_16x16x32_bf16 v[76:79], v[180:183], v[244:247], v[76:79]\n"
    "v_cvt_pk_bf16_f32 v54, v56, v57\n"
    "v_cvt_pk_bf16_f32 v55, v58, v59\n"
    "s_waitcnt lgkmcnt(1)\n"
    "v_mfma_f32_16x16x32_bf16 v[80:83], v[180:183], v[216:219], v[80:83]\n"
    "ds_read_b128 v[120:123], v5 offset:2784\n"
    "v_mfma_f32_16x16x32_bf16 v[68:71], v[116:119], v[228:231], v[68:71]\n"
    "s_xor_b32 s43, s41, 147456\n"
    "buffer_store_dwordx4 v[52:55], v8, s[24:27], s43 offen sc1\n"
    "v_mfma_f32_16x16x32_bf16 v[72:75], v[116:119], v[244:247], v[72:75]\n"
    "v_cvt_pk_bf16_f32 v60, v60, v61\n"
    "v_cvt_pk_bf16_f32 v61, v62, v63\n"
    "v_mfma_f32_16x16x32_bf16 v[76:79], v[116:119], v[216:219], v[76:79]\n"
    "v_cvt_pk_bf16_f32 v62, v64, v65\n"
    "v_cvt_pk_bf16_f32 v63, v66, v67\n"
    "v_mfma_f32_16x16x32_bf16 v[80:83], v[116:119], v[192:195], v[80:83]\n"
    "ds_read_b128 v[152:155], v5 offset:2912\n"
    "v_mfma_f32_16x16x32_bf16 v[68:71], v[128:131], v[244:247], v[68:71]\n"
    "s_xor_b32 s43, s41, 184320\n"
    "buffer_store_dwordx4 v[60:63], v8, s[24:27], s43 offen sc1\n"
    "v_mfma_f32_16x16x32_bf16 v[72:75], v[128:131], v[216:219], v[72:75]\n"
    "v_mfma_f32_16x16x32_bf16 v[76:79], v[128:131], v[192:195], v[76:79]\n"
    "s_waitcnt lgkmcnt(1)\n"
    "v_mfma_f32_16x16x32_bf16 v[80:83], v[128:131], v[84:87], v[80:83]\n"
    "s_barrier\n"
    "ds_read_b128 v[200:203], v5 offset:3040\n"
    "v_mfma_f32_16x16x32_bf16 v[68:71], v[144:147], v[216:219], v[68:71]\n"
    "v_mfma_f32_16x16x32_bf16 v[72:75], v[144:147], v[192:195], v[72:75]\n"
    "v_mfma_f32_16x16x32_bf16 v[76:79], v[144:147], v[84:87], v[76:79]\n"
    "v_mfma_f32_16x16x32_bf16 v[80:83], v[144:147], v[120:123], v[80:83]\n"
    "ds_read_b128 v[208:211], v5 offset:3168\n"
    "v_mfma_f32_16x16x32_bf16 v[68:71], v[88:91], v[192:195], v[68:71]\n"
    "v_mfma_f32_16x16x32_bf16 v[72:75], v[88:91], v[84:87], v[72:75]\n"
    "v_mfma_f32_16x16x32_bf16 v[76:79], v[88:91], v[120:123], v[76:79]\n"
    "s_waitcnt lgkmcnt(1)\n"
    "v_mfma_f32_16x16x32_bf16 v[80:83], v[88:91], v[152:155], v[80:83]\n"
    "ds_read_b128 v[172:175], v5 offset:3296\n"
    "v_mfma_f32_16x16x32_bf16 v[68:71], v[140:143], v[84:87], v[68:71]\n"
    "v_mfma_f32_16x16x32_bf16 v[72:75], v[140:143], v[120:123], v[72:75]\n"
    "v_mfma_f32_16x16x32_bf16 v[76:79], v[140:143], v[152:155], v[76:79]\n"
    "v_mfma_f32_16x16x32_bf16 v[80:83], v[140:143], v[200:203], v[80:83]\n"
    "ds_read_b128 v[124:127], v5 offset:3424\n"
    "v_mfma_f32_16x16x32_bf16 v[68:71], v[156:159], v[120:123], v[68:71]\n"
    "v_mfma_f32_16x16x32_bf16 v[72:75], v[156:159], v[152:155], v[72:75]\n"
    "v_mfma_f32_16x16x32_bf16 v[76:79], v[156:159], v[200:203], v[76:79]\n"
    "s_waitcnt lgkmcnt(1)\n"
    "v_mfma_f32_16x16x32_bf16 v[80:83], v[156:159], v[208:211], v[80:83]\n"
    "ds_read_b128 v[212:215], v5 offset:3552\n"
    "v_mfma_f32_16x16x32_bf16 v[68:71], v[188:191], v[152:155], v[68:71]\n"
    "v_mfma_f32_16x16x32_bf16 v[72:75], v[188:191], v[200:203], v[72:75]\n"
    "v_mfma_f32_16x16x32_bf16 v[76:79], v[188:191], v[208:211], v[76:79]\n"
    "v_mfma_f32_16x16x32_bf16 v[80:83], v[188:191], v[172:175], v[80:83]\n"
    "ds_read_b128 v[228:231], v5 offset:3680\n"
    "v_mfma_f32_16x16x32_bf16 v[68:71], v[92:95], v[200:203], v[68:71]\n"
    "v_mfma_f32_16x16x32_bf16 v[72:75], v[92:95], v[208:211], v[72:75]\n"
    "v_mfma_f32_16x16x32_bf16 v[76:79], v[92:95], v[172:175], v[76:79]\n"
    "s_waitcnt lgkmcnt(1)\n"
    "v_mfma_f32_16x16x32_bf16 v[80:83], v[92:95], v[124:127], v[80:83]\n"
    "ds_read_b128 v[244:247], v5 offset:3808\n"
    "v_mfma_f32_16x16x32_bf16 v[68:71], v[224:227], v[208:211], v[68:71]\n"
    "v_mfma_f32_16x16x32_bf16 v[72:75], v[224:227], v[172:175], v[72:75]\n"
    "v_mfma_f32_16x16x32_bf16 v[76:79], v[224:227], v[124:127], v[76:79]\n"
    "v_mfma_f32_16x16x32_bf16 v[80:83], v[224:227], v[212:215], v[80:83]\n"
    "ds_read_b128 v[216:219], v5 offset:3936\n"
    "v_mfma_f32_16x16x32_bf16 v[68:71], v[232:235], v[172:175], v[68:71]\n"
    "v_mfma_f32_16x16x32_bf16 v[72:75], v[232:235], v[124:127], v[72:75]\n"
    "v_mfma_f32_16x16x32_bf16 v[76:79], v[232:235], v[212:215], v[76:79]\n"
    "s_waitcnt lgkmcnt(1)\n"
    "v_mfma_f32_16x16x32_bf16 v[80:83], v[232:235], v[228:231], v[80:83]\n"
    "ds_read_b128 v[192:195], v5 offset:4064\n"
    "v_mfma_f32_16x16x32_bf16 v[68:71], v[240:243], v[124:127], v[68:71]\n"
    "v_mfma_f32_16x16x32_bf16 v[72:75], v[240:243], v[212:215], v[72:75]\n"
    "v_mfma_f32_16x16x32_bf16 v[76:79], v[240:243], v[228:231], v[76:79]\n"
    "v_mfma_f32_16x16x32_bf16 v[80:83], v[240:243], v[244:247], v[80:83]\n"
    "ds_read_b128 v[84:87], v5 offset:4224\n"
    "v_mfma_f32_16x16x32_bf16 v[68:71], v[248:251], v[212:215], v[68:71]\n"
    "v_mfma_f32_16x16x32_bf16 v[72:75], v[248:251], v[228:231], v[72:75]\n"
    "v_mfma_f32_16x16x32_bf16 v[76:79], v[248:251], v[244:247], v[76:79]\n"
    "s_waitcnt lgkmcnt(1)\n"
    "v_mfma_f32_16x16x32_bf16 v[80:83], v[248:251], v[216:219], v[80:83]\n"
    "ds_read_b128 v[120:123], v5 offset:4352\n"
    "v_mfma_f32_16x16x32_bf16 v[68:71], v[184:187], v[228:231], v[68:71]\n"
    "v_mfma_f32_16x16x32_bf16 v[72:75], v[184:187], v[244:247], v[72:75]\n"
    "v_mfma_f32_16x16x32_bf16 v[76:79], v[184:187], v[216:219], v[76:79]\n"
    "v_mfma_f32_16x16x32_bf16 v[80:83], v[184:187], v[192:195], v[80:83]\n"
    "ds_read_b128 v[152:155], v5 offset:4480\n"
    "v_mfma_f32_16x16x32_bf16 v[68:71], v[100:103], v[244:247], v[68:71]\n"
    "v_mfma_f32_16x16x32_bf16 v[72:75], v[100:103], v[216:219], v[72:75]\n"
    "v_mfma_f32_16x16x32_bf16 v[76:79], v[100:103], v[192:195], v[76:79]\n"
    "s_waitcnt lgkmcnt(1)\n"
    "v_mfma_f32_16x16x32_bf16 v[80:83], v[100:103], v[84:87], v[80:83]\n"
    "ds_read_b128 v[200:203], v5 offset:4608\n"
    "v_mfma_f32_16x16x32_bf16 v[68:71], v[160:163], v[216:219], v[68:71]\n"
    "v_mfma_f32_16x16x32_bf16 v[72:75], v[160:163], v[192:195], v[72:75]\n"
    "v_mfma_f32_16x16x32_bf16 v[76:79], v[160:163], v[84:87], v[76:79]\n"
    "v_mfma_f32_16x16x32_bf16 v[80:83], v[160:163], v[120:123], v[80:83]\n"
    "ds_read_b128 v[208:211], v5 offset:4736\n"
    "v_mfma_f32_16x16x32_bf16 v[68:71], v[176:179], v[192:195], v[68:71]\n"
    "v_mfma_f32_16x16x32_bf16 v[72:75], v[176:179], v[84:87], v[72:75]\n"
    "v_mfma_f32_16x16x32_bf16 v[76:79], v[176:179], v[120:123], v[76:79]\n"
    "s_waitcnt lgkmcnt(1)\n"
    "v_mfma_f32_16x16x32_bf16 v[80:83], v[176:179], v[152:155], v[80:83]\n"
    "ds_read_b128 v[172:175], v5 offset:4864\n"
    "v_mfma_f32_16x16x32_bf16 v[68:71], v[196:199], v[84:87], v[68:71]\n"
    "v_mfma_f32_16x16x32_bf16 v[72:75], v[196:199], v[120:123], v[72:75]\n"
    "v_mfma_f32_16x16x32_bf16 v[76:79], v[196:199], v[152:155], v[76:79]\n"
    "v_mfma_f32_16x16x32_bf16 v[80:83], v[196:199], v[200:203], v[80:83]\n"
    "ds_read_b128 v[124:127], v5 offset:4992\n"
    "v_mfma_f32_16x16x32_bf16 v[68:71], v[136:139], v[120:123], v[68:71]\n"
    "v_mfma_f32_16x16x32_bf16 v[72:75], v[136:139], v[152:155], v[72:75]\n"
    "v_mfma_f32_16x16x32_bf16 v[76:79], v[136:139], v[200:203], v[76:79]\n"
    "s_waitcnt lgkmcnt(1)\n"
    "v_mfma_f32_16x16x32_bf16 v[80:83], v[136:139], v[208:211], v[80:83]\n"
    "ds_read_b128 v[212:215], v5 offset:5120\n"
    "v_mfma_f32_16x16x32_bf16 v[68:71], v[112:115], v[152:155], v[68:71]\n"
    "v_mfma_f32_16x16x32_bf16 v[72:75], v[112:115], v[200:203], v[72:75]\n"
    "v_mfma_f32_16x16x32_bf16 v[76:79], v[112:115], v[208:211], v[76:79]\n"
    "v_mfma_f32_16x16x32_bf16 v[80:83], v[112:115], v[172:175], v[80:83]\n"
    "ds_read_b128 v[228:231], v5 offset:5248\n"
    "v_mfma_f32_16x16x32_bf16 v[68:71], v[148:151], v[200:203], v[68:71]\n"
    "v_mfma_f32_16x16x32_bf16 v[72:75], v[148:151], v[208:211], v[72:75]\n"
    "v_mfma_f32_16x16x32_bf16 v[76:79], v[148:151], v[172:175], v[76:79]\n"
    "s_waitcnt lgkmcnt(1)\n"
    "v_mfma_f32_16x16x32_bf16 v[80:83], v[148:151], v[124:127], v[80:83]\n"
    "ds_read_b128 v[244:247], v5 offset:5376\n"
    "v_mfma_f32_16x16x32_bf16 v[68:71], v[164:167], v[208:211], v[68:71]\n"
    "v_mfma_f32_16x16x32_bf16 v[72:75], v[164:167], v[172:175], v[72:75]\n"
    "v_mfma_f32_16x16x32_bf16 v[76:79], v[164:167], v[124:127], v[76:79]\n"
    "v_mfma_f32_16x16x32_bf16 v[80:83], v[164:167], v[212:215], v[80:83]\n"
    "s_waitcnt vmcnt(0)\n"
    "v_add_u32 v18, 1, v18\n"
    "ds_read_b128 v[216:219], v5 offset:5504\n"
    "v_mfma_f32_16x16x32_bf16 v[68:71], v[204:207], v[172:175], v[68:71]\n"
    "buffer_store_dword v18, v19, s[24:27], 0 offen sc1\n"
    "v_mfma_f32_16x16x32_bf16 v[72:75], v[204:207], v[124:127], v[72:75]\n"
    "v_mfma_f32_16x16x32_bf16 v[76:79], v[204:207], v[212:215], v[76:79]\n"
    "s_waitcnt lgkmcnt(1)\n"
    "v_mfma_f32_16x16x32_bf16 v[80:83], v[204:207], v[228:231], v[80:83]\n"
    "ds_read_b128 v[192:195], v5 offset:5632\n"
    "v_mfma_f32_16x16x32_bf16 v[68:71], v[236:239], v[124:127], v[68:71]\n"
    "v_mfma_f32_16x16x32_bf16 v[72:75], v[236:239], v[212:215], v[72:75]\n"
    "v_mfma_f32_16x16x32_bf16 v[76:79], v[236:239], v[228:231], v[76:79]\n"
    "v_mfma_f32_16x16x32_bf16 v[80:83], v[236:239], v[244:247], v[80:83]\n"
    "ds_read_b128 v[84:87], v5 offset:5760\n"
    "v_mfma_f32_16x16x32_bf16 v[68:71], v[252:255], v[212:215], v[68:71]\n"
    "v_mfma_f32_16x16x32_bf16 v[72:75], v[252:255], v[228:231], v[72:75]\n"
    "v_mfma_f32_16x16x32_bf16 v[76:79], v[252:255], v[244:247], v[76:79]\n"
    "s_waitcnt lgkmcnt(1)\n"
    "v_mfma_f32_16x16x32_bf16 v[80:83], v[252:255], v[216:219], v[80:83]\n"
    "ds_read_b128 v[120:123], v5 offset:5888\n"
    "v_mfma_f32_16x16x32_bf16 v[68:71], v[132:135], v[228:231], v[68:71]\n"
    "ds_read_b128 v[152:155], v5 offset:64\n"
    "v_mfma_f32_16x16x32_bf16 v[72:75], v[132:135], v[244:247], v[72:75]\n"
    "ds_read_b128 v[200:203], v5 offset:192\n"
    "v_mfma_f32_16x16x32_bf16 v[76:79], v[132:135], v[216:219], v[76:79]\n"
    "ds_read_b128 v[208:211], v5 offset:320\n"
    "v_mfma_f32_16x16x32_bf16 v[80:83], v[132:135], v[192:195], v[80:83]\n"
    "ds_read_b128 v[172:175], v5 offset:448\n"
    "v_mfma_f32_16x16x32_bf16 v[68:71], v[168:171], v[244:247], v[68:71]\n"
    "v_mfma_f32_16x16x32_bf16 v[72:75], v[168:171], v[216:219], v[72:75]\n"
    "v_mfma_f32_16x16x32_bf16 v[76:79], v[168:171], v[192:195], v[76:79]\n"
    "s_waitcnt lgkmcnt(4)\n"
    "v_mfma_f32_16x16x32_bf16 v[80:83], v[168:171], v[84:87], v[80:83]\n"
    "ds_read_b128 v[124:127], v5 offset:576\n"
    "v_mfma_f32_16x16x32_bf16 v[68:71], v[220:223], v[216:219], v[68:71]\n"
    "v_mfma_f32_16x16x32_bf16 v[72:75], v[220:223], v[192:195], v[72:75]\n"
    "v_mfma_f32_16x16x32_bf16 v[76:79], v[220:223], v[84:87], v[76:79]\n"
    "v_mfma_f32_16x16x32_bf16 v[80:83], v[220:223], v[120:123], v[80:83]\n"
    "ds_read_b128 v[212:215], v5 offset:704\n"
    "s_waitcnt lgkmcnt(2)\n"
    "v_mfma_f32_16x16x32_bf16 v[20:23], v[96:99], v[152:155], v[20:23]\n"
    "v_mfma_f32_16x16x32_bf16 v[24:27], v[96:99], v[200:203], v[24:27]\n"
    "v_mfma_f32_16x16x32_bf16 v[28:31], v[96:99], v[208:211], v[28:31]\n"
    "v_cvt_pk_bf16_f32 v68, v68, v69\n"
    "v_cvt_pk_bf16_f32 v69, v70, v71\n"
    "v_mfma_f32_16x16x32_bf16 v[32:35], v[96:99], v[172:175], v[32:35]\n"
    "ds_read_b128 v[228:231], v5 offset:832\n"
    "v_cvt_pk_bf16_f32 v70, v72, v73\n"
    "v_mfma_f32_16x16x32_bf16 v[20:23], v[104:107], v[200:203], v[20:23]\n"
    "v_cvt_pk_bf16_f32 v71, v74, v75\n"
    "v_min_u32 v68, -2, v68\n"
    "v_mfma_f32_16x16x32_bf16 v[24:27], v[104:107], v[208:211], v[24:27]\n"
    "v_min_u32 v69, -2, v69\n"
    "v_min_u32 v70, -2, v70\n"
    "v_mfma_f32_16x16x32_bf16 v[28:31], v[104:107], v[172:175], v[28:31]\n"
    "v_min_u32 v71, -2, v71\n"
    "s_xor_b32 s43, s41, 221184\n"
    "s_waitcnt lgkmcnt(1)\n"
    "v_mfma_f32_16x16x32_bf16 v[32:35], v[104:107], v[124:127], v[32:35]\n"
    "ds_read_b128 v[244:247], v5 offset:960\n"
    "buffer_store_dwordx4 v[68:71], v8, s[24:27], s43 offen sc1\n"
    "v_mfma_f32_16x16x32_bf16 v[20:23], v[108:111], v[208:211], v[20:23]\n"
    "v_cvt_pk_bf16_f32 v76, v76, v77\n"
    "v_cvt_pk_bf16_f32 v77, v78, v79\n"
    "v_mfma_f32_16x16x32_bf16 v[24:27], v[108:111], v[172:175], v[24:27]\n"
    "v_cvt_pk_bf16_f32 v78, v80, v81\n"
    "v_cvt_pk_bf16_f32 v79, v82, v83\n"
    "v_mfma_f32_16x16x32_bf16 v[28:31], v[108:111], v[124:127], v[28:31]\n"
    "v_min_u32 v76, -2, v76\n"
    "v_min_u32 v77, -2, v77\n"
    "v_mfma_f32_16x16x32_bf16 v[32:35], v[108:111], v[212:215], v[32:35]\n"
    "ds_read_b128 v[216:219], v5 offset:1088\n"
    "v_mfma_f32_16x16x32_bf16 v[20:23], v[180:183], v[172:175], v[20:23]\n"
    "v_min_u32 v78, -2, v78\n"
    "v_min_u32 v79, -2, v79\n"
    "v_mfma_f32_16x16x32_bf16 v[24:27], v[180:183], v[124:127], v[24:27]\n"
    "s_xor_b32 s43, s41, 258048\n"
    "buffer_store_dwordx4 v[76:79], v8, s[24:27], s43 offen sc1\n"
    "v_mfma_f32_16x16x32_bf16 v[28:31], v[180:183], v[212:215], v[28:31]\n"
    "s_waitcnt lgkmcnt(1)\n"
    "v_mfma_f32_16x16x32_bf16 v[32:35], v[180:183], v[228:231], v[32:35]\n"
    "ds_read_b128 v[192:195], v5 offset:1216\n"
    "v_mfma_f32_16x16x32_bf16 v[20:23], v[116:119], v[124:127], v[20:23]\n"
    "v_mfma_f32_16x16x32_bf16 v[24:27], v[116:119], v[212:215], v[24:27]\n"
    "v_mfma_f32_16x16x32_bf16 v[28:31], v[116:119], v[228:231], v[28:31]\n"
    "v_mfma_f32_16x16x32_bf16 v[32:35], v[116:119], v[244:247], v[32:35]\n"
    "ds_read_b128 v[84:87], v5 offset:1344\n"
    "v_mfma_f32_16x16x32_bf16 v[20:23], v[128:131], v[212:215], v[20:23]\n"
    "v_mfma_f32_16x16x32_bf16 v[24:27], v[128:131], v[228:231], v[24:27]\n"
    "v_mfma_f32_16x16x32_bf16 v[28:31], v[128:131], v[244:247], v[28:31]\n"
    "s_waitcnt lgkmcnt(1)\n"
    "v_mfma_f32_16x16x32_bf16 v[32:35], v[128:131], v[216:219], v[32:35]\n"
    "ds_read_b128 v[120:123], v5 offset:1472\n"
    "v_mfma_f32_16x16x32_bf16 v[20:23], v[144:147], v[228:231], v[20:23]\n"
    "v_mfma_f32_16x16x32_bf16 v[24:27], v[144:147], v[244:247], v[24:27]\n"
    "v_mfma_f32_16x16x32_bf16 v[28:31], v[144:147], v[216:219], v[28:31]\n"
    "v_mfma_f32_16x16x32_bf16 v[32:35], v[144:147], v[192:195], v[32:35]\n"
    "ds_read_b128 v[152:155], v5 offset:1600\n"
    "v_mfma_f32_16x16x32_bf16 v[20:23], v[88:91], v[244:247], v[20:23]\n"
    "v_mfma_f32_16x16x32_bf16 v[24:27], v[88:91], v[216:219], v[24:27]\n"
    "v_mfma_f32_16x16x32_bf16 v[28:31], v[88:91], v[192:195], v[28:31]\n"
    "s_waitcnt lgkmcnt(1)\n"
    "v_mfma_f32_16x16x32_bf16 v[32:35], v[88:91], v[84:87], v[32:35]\n"
    "ds_read_b128 v[200:203], v5 offset:1728\n"
    "v_mfma_f32_16x16x32_bf16 v[20:23], v[140:143], v[216:219], v[20:23]\n"
    "v_mfma_f32_16x16x32_bf16 v[24:27], v[140:143], v[192:195], v[24:27]\n"
    "v_mfma_f32_16x16x32_bf16 v[28:31], v[140:143], v[84:87], v[28:31]\n"
    "v_mfma_f32_16x16x32_bf16 v[32:35], v[140:143], v[120:123], v[32:35]\n"
    "ds_read_b128 v[208:211], v5 offset:1856\n"
    "v_mfma_f32_16x16x32_bf16 v[20:23], v[156:159], v[192:195], v[20:23]\n"
    "v_mfma_f32_16x16x32_bf16 v[24:27], v[156:159], v[84:87], v[24:27]\n"
    "v_mfma_f32_16x16x32_bf16 v[28:31], v[156:159], v[120:123], v[28:31]\n"
    "s_waitcnt lgkmcnt(1)\n"
    "v_mfma_f32_16x16x32_bf16 v[32:35], v[156:159], v[152:155], v[32:35]\n"
    "ds_read_b128 v[172:175], v5 offset:1984\n"
    "v_mfma_f32_16x16x32_bf16 v[20:23], v[188:191], v[84:87], v[20:23]\n"
    "v_mfma_f32_16x16x32_bf16 v[24:27], v[188:191], v[120:123], v[24:27]\n"
    "v_mfma_f32_16x16x32_bf16 v[28:31], v[188:191], v[152:155], v[28:31]\n"
    "v_mfma_f32_16x16x32_bf16 v[32:35], v[188:191], v[200:203], v[32:35]\n"
    "ds_read_b128 v[124:127], v5 offset:2144\n"
    "v_mfma_f32_16x16x32_bf16 v[20:23], v[92:95], v[120:123], v[20:23]\n"
    "v_mfma_f32_16x16x32_bf16 v[24:27], v[92:95], v[152:155], v[24:27]\n"
    "v_mfma_f32_16x16x32_bf16 v[28:31], v[92:95], v[200:203], v[28:31]\n"
    "s_waitcnt lgkmcnt(1)\n"
    "v_mfma_f32_16x16x32_bf16 v[32:35], v[92:95], v[208:211], v[32:35]\n"
    "ds_read_b128 v[212:215], v5 offset:2272\n"
    "v_mfma_f32_16x16x32_bf16 v[20:23], v[224:227], v[152:155], v[20:23]\n"
    "v_mfma_f32_16x16x32_bf16 v[24:27], v[224:227], v[200:203], v[24:27]\n"
    "v_mfma_f32_16x16x32_bf16 v[28:31], v[224:227], v[208:211], v[28:31]\n"
    "v_mfma_f32_16x16x32_bf16 v[32:35], v[224:227], v[172:175], v[32:35]\n"
    "ds_read_b128 v[228:231], v5 offset:2400\n"
    "v_mfma_f32_16x16x32_bf16 v[20:23], v[232:235], v[200:203], v[20:23]\n"
    "v_mfma_f32_16x16x32_bf16 v[24:27], v[232:235], v[208:211], v[24:27]\n"
    "v_mfma_f32_16x16x32_bf16 v[28:31], v[232:235], v[172:175], v[28:31]\n"
    "s_waitcnt lgkmcnt(1)\n"
    "v_mfma_f32_16x16x32_bf16 v[32:35], v[232:235], v[124:127], v[32:35]\n"
    "ds_read_b128 v[244:247], v5 offset:2528\n"
    "v_mfma_f32_16x16x32_bf16 v[20:23], v[240:243], v[208:211], v[20:23]\n"
    "v_mfma_f32_16x16x32_bf16 v[24:27], v[240:243], v[172:175], v[24:27]\n"
    "v_mfma_f32_16x16x32_bf16 v[28:31], v[240:243], v[124:127], v[28:31]\n"
    "v_mfma_f32_16x16x32_bf16 v[32:35], v[240:243], v[212:215], v[32:35]\n"
    "ds_read_b128 v[216:219], v5 offset:2656\n"
    "v_mfma_f32_16x16x32_bf16 v[20:23], v[248:251], v[172:175], v[20:23]\n"
    "v_mfma_f32_16x16x32_bf16 v[24:27], v[248:251], v[124:127], v[24:27]\n"
    "v_mfma_f32_16x16x32_bf16 v[28:31], v[248:251], v[212:215], v[28:31]\n"
    "s_waitcnt lgkmcnt(1)\n"
    "v_mfma_f32_16x16x32_bf16 v[32:35], v[248:251], v[228:231], v[32:35]\n"
    "ds_read_b128 v[192:195], v5 offset:2784\n"
    "v_mfma_f32_16x16x32_bf16 v[20:23], v[184:187], v[124:127], v[20:23]\n"
    "v_mfma_f32_16x16x32_bf16 v[24:27], v[184:187], v[212:215], v[24:27]\n"
    "v_mfma_f32_16x16x32_bf16 v[28:31], v[184:187], v[228:231], v[28:31]\n"
    "v_mfma_f32_16x16x32_bf16 v[32:35], v[184:187], v[244:247], v[32:35]\n"
    "ds_read_b128 v[84:87], v5 offset:2912\n"
    "v_mfma_f32_16x16x32_bf16 v[20:23], v[100:103], v[212:215], v[20:23]\n"
    "v_mfma_f32_16x16x32_bf16 v[24:27], v[100:103], v[228:231], v[24:27]\n"
    "v_mfma_f32_16x16x32_bf16 v[28:31], v[100:103], v[244:247], v[28:31]\n"
    "s_waitcnt lgkmcnt(1)\n"
    "v_mfma_f32_16x16x32_bf16 v[32:35], v[100:103], v[216:219], v[32:35]\n"
    "ds_read_b128 v[120:123], v5 offset:3040\n"
    "v_mfma_f32_16x16x32_bf16 v[20:23], v[160:163], v[228:231], v[20:23]\n"
    "v_mfma_f32_16x16x32_bf16 v[24:27], v[160:163], v[244:247], v[24:27]\n"
    "v_mfma_f32_16x16x32_bf16 v[28:31], v[160:163], v[216:219], v[28:31]\n"
    "v_mfma_f32_16x16x32_bf16 v[32:35], v[160:163], v[192:195], v[32:35]\n"
    "ds_read_b128 v[152:155], v5 offset:3168\n"
    "v_mfma_f32_16x16x32_bf16 v[20:23], v[176:179], v[244:247], v[20:23]\n"
    "v_mfma_f32_16x16x32_bf16 v[24:27], v[176:179], v[216:219], v[24:27]\n"
    "v_mfma_f32_16x16x32_bf16 v[28:31], v[176:179], v[192:195], v[28:31]\n"
    "s_waitcnt lgkmcnt(1)\n"
    "v_mfma_f32_16x16x32_bf16 v[32:35], v[176:179], v[84:87], v[32:35]\n"
    "ds_read_b128 v[200:203], v5 offset:3296\n"
    "v_mfma_f32_16x16x32_bf16 v[20:23], v[196:199], v[216:219], v[20:23]\n"
    "v_mfma_f32_16x16x32_bf16 v[24:27], v[196:199], v[192:195], v[24:27]\n"
    "v_mfma_f32_16x16x32_bf16 v[28:31], v[196:199], v[84:87], v[28:31]\n"
    "v_mfma_f32_16x16x32_bf16 v[32:35], v[196:199], v[120:123], v[32:35]\n"
    "ds_read_b128 v[208:211], v5 offset:3424\n"
    "v_mfma_f32_16x16x32_bf16 v[20:23], v[136:139], v[192:195], v[20:23]\n"
    "v_mfma_f32_16x16x32_bf16 v[24:27], v[136:139], v[84:87], v[24:27]\n"
    "v_mfma_f32_16x16x32_bf16 v[28:31], v[136:139], v[120:123], v[28:31]\n"
    "s_waitcnt lgkmcnt(1)\n"
    "v_mfma_f32_16x16x32_bf16 v[32:35], v[136:139], v[152:155], v[32:35]\n"
    "ds_read_b128 v[172:175], v5 offset:3552\n"
    "v_mfma_f32_16x16x32_bf16 v[20:23], v[112:115], v[84:87], v[20:23]\n"
    "v_mfma_f32_16x16x32_bf16 v[24:27], v[112:115], v[120:123], v[24:27]\n"
    "v_mfma_f32_16x16x32_bf16 v[28:31], v[112:115], v[152:155], v[28:31]\n"
    "v_mfma_f32_16x16x32_bf16 v[32:35], v[112:115], v[200:203], v[32:35]\n"
    "ds_read_b128 v[124:127], v5 offset:3680\n"
    "v_mfma_f32_16x16x32_bf16 v[20:23], v[148:151], v[120:123], v[20:23]\n"
    "v_mfma_f32_16x16x32_bf16 v[24:27], v[148:151], v[152:155], v[24:27]\n"
    "v_mfma_f32_16x16x32_bf16 v[28:31], v[148:151], v[200:203], v[28:31]\n"
    "s_waitcnt lgkmcnt(1)\n"
    "v_mfma_f32_16x16x32_bf16 v[32:35], v[148:151], v[208:211], v[32:35]\n"
    "ds_read_b128 v[212:215], v5 offset:3808\n"
    "v_mfma_f32_16x16x32_bf16 v[20:23], v[164:167], v[152:155], v[20:23]\n"
    "v_mfma_f32_16x16x32_bf16 v[24:27], v[164:167], v[200:203], v[24:27]\n"
    "v_mfma_f32_16x16x32_bf16 v[28:31], v[164:167], v[208:211], v[28:31]\n"
    "v_mfma_f32_16x16x32_bf16 v[32:35], v[164:167], v[172:175], v[32:35]\n"
    "ds_read_b128 v[228:231], v5 offset:3936\n"
    "v_mfma_f32_16x16x32_bf16 v[20:23], v[204:207], v[200:203], v[20:23]\n"
    "v_mfma_f32_16x16x32_bf16 v[24:27], v[204:207], v[208:211], v[24:27]\n"
    "v_mfma_f32_16x16x32_bf16 v[28:31], v[204:207], v[172:175], v[28:31]\n"
    "s_waitcnt lgkmcnt(1)\n"
    "v_mfma_f32_16x16x32_bf16 v[32:35], v[204:207], v[124:127], v[32:35]\n"
    "ds_read_b128 v[244:247], v5 offset:4064\n"
    "v_mfma_f32_16x16x32_bf16 v[20:23], v[236:239], v[208:211], v[20:23]\n"
    "v_mfma_f32_16x16x32_bf16 v[24:27], v[236:239], v[172:175], v[24:27]\n"
    "v_mfma_f32_16x16x32_bf16 v[28:31], v[236:239], v[124:127], v[28:31]\n"
    "v_mfma_f32_16x16x32_bf16 v[32:35], v[236:239], v[212:215], v[32:35]\n"
    "ds_read_b128 v[216:219], v5 offset:4224\n"
    "v_mfma_f32_16x16x32_bf16 v[20:23], v[252:255], v[172:175], v[20:23]\n"
    "v_mfma_f32_16x16x32_bf16 v[24:27], v[252:255], v[124:127], v[24:27]\n"
    "v_mfma_f32_16x16x32_bf16 v[28:31], v[252:255], v[212:215], v[28:31]\n"
    "s_cmp_ge_u32 s39, 3\n"
    "s_cbranch_scc1 .Lt3_1%=\n"
    ".Lt2_1%=:\n"
    "s_waitcnt lgkmcnt(1)\n"
    "v_mfma_f32_16x16x32_bf16 v[32:35], v[252:255], v[228:231], v[32:35]\n"
    "ds_read_b128 v[192:195], v5 offset:4352\n"
    "v_mfma_f32_16x16x32_bf16 v[20:23], v[132:135], v[124:127], v[20:23]\n"
    "ds_read_b128 v[84:87], v5 offset:832\n"
    "v_mfma_f32_16x16x32_bf16 v[24:27], v[132:135], v[212:215], v[24:27]\n"
    "ds_read_b128 v[120:123], v5 offset:960\n"
    "v_mfma_f32_16x16x32_bf16 v[28:31], v[132:135], v[228:231], v[28:31]\n"
    "v_mfma_f32_16x16x32_bf16 v[32:35], v[132:135], v[244:247], v[32:35]\n"
    "ds_read_b128 v[152:155], v5 offset:1088\n"
    "v_mfma_f32_16x16x32_bf16 v[20:23], v[168:171], v[212:215], v[20:23]\n"
    "v_mfma_f32_16x16x32_bf16 v[24:27], v[168:171], v[228:231], v[24:27]\n"
    "ds_read_b128 v[200:203], v5 offset:1216\n"
    "v_mfma_f32_16x16x32_bf16 v[28:31], v[168:171], v[244:247], v[28:31]\n"
    "s_waitcnt lgkmcnt(4)\n"
    "v_mfma_f32_16x16x32_bf16 v[32:35], v[168:171], v[216:219], v[32:35]\n"
    "ds_read_b128 v[208:211], v5 offset:1344\n"
    "v_mfma_f32_16x16x32_bf16 v[20:23], v[220:223], v[228:231], v[20:23]\n"
    "v_mfma_f32_16x16x32_bf16 v[24:27], v[220:223], v[244:247], v[24:27]\n"
    "ds_read_b128 v[172:175], v5 offset:1472\n"
    "v_mfma_f32_16x16x32_bf16 v[28:31], v[220:223], v[216:219], v[28:31]\n"
    "v_mfma_f32_16x16x32_bf16 v[32:35], v[220:223], v[192:195], v[32:35]\n"
    "ds_read_b128 v[124:127], v5 offset:1600\n"
    "s_waitcnt lgkmcnt(4)\n"
    "v_mfma_f32_16x16x32_bf16 v[44:47], v[96:99], v[84:87], v[44:47]\n"
    "v_mfma_f32_16x16x32_bf16 v[48:51], v[96:99], v[120:123], v[48:51]\n"
    "ds_read_b128 v[212:215], v5 offset:1728\n"
    "v_mfma_f32_16x16x32_bf16 v[44:47], v[104:107], v[120:123], v[44:47]\n"
    "v_cvt_pk_bf16_f32 v20, v20, v21\n"
    "v_cvt_pk_bf16_f32 v21, v22, v23\n"
    "v_mfma_f32_16x16x32_bf16 v[48:51], v[104:107], v[152:155], v[48:51]\n"
    "ds_read_b128 v[228:231], v5 offset:1856\n"
    "v_mfma_f32_16x16x32_bf16 v[44:47], v[108:111], v[152:155], v[44:47]\n"
    "v_cvt_pk_bf16_f32 v22, v24, v25\n"
    "v_cvt_pk_bf16_f32 v23, v26, v27\n"
    "s_waitcnt lgkmcnt(3)\n"
    "v_mfma_f32_16x16x32_bf16 v[48:51], v[108:111], v[200:203], v[48:51]\n"
    "ds_read_b128 v[244:247], v5 offset:1984\n"
    "v_mfma_f32_16x16x32_bf16 v[44:47], v[180:183], v[200:203], v[44:47]\n"
    "v_min_u32 v20, -2, v20\n"
    "v_min_u32 v21, -2, v21\n"
    "v_mfma_f32_16x16x32_bf16 v[48:51], v[180:183], v[208:211], v[48:51]\n"
    "ds_read_b128 v[216:219], v5 offset:2144\n"
    "v_mfma_f32_16x16x32_bf16 v[44:47], v[116:119], v[208:211], v[44:47]\n"
    "v_min_u32 v22, -2, v22\n"
    "v_min_u32 v23, -2, v23\n"
    "v_mfma_f32_16x16x32_bf16 v[48:51], v[116:119], v[172:175], v[48:51]\n"
    "ds_read_b128 v[192:195], v5 offset:2272\n"
    "v_mfma_f32_16x16x32_bf16 v[44:47], v[128:131], v[172:175], v[44:47]\n"
    "s_xor_b32 s43, s41, 0\n"
    "buffer_store_dwordx4 v[20:23], v8, s[24:27], s43 offen sc1\n"
    "s_waitcnt lgkmcnt(3)\n"
    "v_mfma_f32_16x16x32_bf16 v[48:51], v[128:131], v[124:127], v[48:51]\n"
    "ds_read_b128 v[84:87], v5 offset:2400\n"
    "v_mfma_f32_16x16x32_bf16 v[44:47], v[144:147], v[124:127], v[44:47]\n"
    "v_cvt_pk_bf16_f32 v28, v28, v29\n"
    "v_cvt_pk_bf16_f32 v29, v30, v31\n"
    "v_mfma_f32_16x16x32_bf16 v[48:51], v[144:147], v[212:215], v[48:51]\n"
    "ds_read_b128 v[120:123], v5 offset:2528\n"
    "v_mfma_f32_16x16x32_bf16 v[44:47], v[88:91], v[212:215], v[44:47]\n"
    "v_cvt_pk_bf16_f32 v30, v32, v33\n"
    "v_cvt_pk_bf16_f32 v31, v34, v35\n"
    "v_mfma_f32_16x16x32_bf16 v[48:51], v[88:91], v[228:231], v[48:51]\n"
    "ds_read_b128 v[152:155], v5 offset:2656\n"
    "v_mfma_f32_16x16x32_bf16 v[44:47], v[140:143], v[228:231], v[44:47]\n"
    "v_min_u32 v28, -2, v28\n"
    "v_min_u32 v29, -2, v29\n"
    "s_waitcnt lgkmcnt(3)\n"
    "v_mfma_f32_16x16x32_bf16 v[48:51], v[140:143], v[244:247], v[48:51]\n"
    "ds_read_b128 v[200:203], v5 offset:2784\n"
    "v_mfma_f32_16x16x32_bf16 v[44:47], v[156:159], v[244:247], v[44:47]\n"
    "v_min_u32 v30, -2, v30\n"
    "v_min_u32 v31, -2, v31\n"
    "v_mfma_f32_16x16x32_bf16 v[48:51], v[156:159], v[216:219], v[48:51]\n"
    "ds_read_b128 v[208:211], v5 offset:2912\n"
    "v_mfma_f32_16x16x32_bf16 v[44:47], v[188:191], v[216:219], v[44:47]\n"
    ".Lpad6%=:\n"
    "s_cbranch_execz .Lpad6r%=\n"
    "s_xor_b32 s43, s41, 36864\n"
    "buffer_store_dwordx4 v[28:31], v8, s[24:27], s43 offen sc1\n"
    "v_mfma_f32_16x16x32_bf16 v[48:51], v[188:191], v[192:195], v[48:51]\n"
    "ds_read_b128 v[172:175], v5 offset:3040\n"
    "v_mfma_f32_16x16x32_bf16 v[44:47], v[92:95], v[192:195], v[44:47]\n"
    "s_waitcnt lgkmcnt(3)\n"
    "v_mfma_f32_16x16x32_bf16 v[48:51], v[92:95], v[84:87], v[48:51]\n"
    "ds_read_b128 v[124:127], v5 offset:3168\n"
    "v_mfma_f32_16x16x32_bf16 v[44:47], v[224:227], v[84:87], v[44:47]\n"
    "v_mfma_f32_16x16x32_bf16 v[48:51], v[224:227], v[120:123], v[48:51]\n"
    "ds_read_b128 v[212:215], v5 offset:3296\n"
    "v_mfma_f32_16x16x32_bf16 v[44:47], v[232:235], v[120:123], v[44:47]\n"
    "v_mfma_f32_16x16x32_bf16 v[48:51], v[232:235], v[152:155], v[48:51]\n"
    "ds_read_b128 v[228:231], v5 offset:3424\n"
    "v_mfma_f32_16x16x32_bf16 v[44:47], v[240:243], v[152:155], v[44:47]\n"
    "s_waitcnt lgkmcnt(3)\n"
    "v_mfma_f32_16x16x32_bf16 v[48:51], v[240:243], v[200:203], v[48:51]\n"
    "ds_read_b128 v[244:247], v5 offset:3552\n"
    "v_mfma_f32_16x16x32_bf16 v[44:47], v[248:251], v[200:203], v[44:47]\n"
    "v_mfma_f32_16x16x32_bf16 v[48:51], v[248:251], v[208:211], v[48:51]\n"
    "ds_read_b128 v[216:219], v5 offset:3680\n"
    "v_mfma_f32_16x16x32_bf16 v[44:47], v[184:187], v[208:211], v[44:47]\n"
    "v_mfma_f32_16x16x32_bf16 v[48:51], v[184:187], v[172:175], v[48:51]\n"
    "ds_read_b128 v[192:195], v5 offset:3808\n"
    "v_mfma_f32_16x16x32_bf16 v[44:47], v[100:103], v[172:175], v[44:47]\n"
    "s_waitcnt lgkmcnt(3)\n"
    "v_mfma_f32_16x16x32_bf16 v[48:51], v[100:103], v[124:127], v[48:51]\n"
    "ds_read_b128 v[84:87], v5 offset:3936\n"
    "v_mfma_f32_16x16x32_bf16 v[44:47], v[160:163], v[124:127], v[44:47]\n"
    "v_mfma_f32_16x16x32_bf16 v[48:51], v[160:163], v[212:215], v[48:51]\n"
    "ds_read_b128 v[120:123], v5 offset:4064\n"
    "v_mfma_f32_16x16x32_bf16 v[44:47], v[176:179], v[212:215], v[44:47]\n"
    "v_mfma_f32_16x16x32_bf16 v[48:51], v[176:179], v[228:231], v[48:51]\n"
    "ds_read_b128 v[152:155], v5 offset:4224\n"
    "v_mfma_f32_16x16x32_bf16 v[44:47], v[196:199], v[228:231], v[44:47]\n"
    "s_waitcnt lgkmcnt(3)\n"
    "v_mfma_f32_16x16x32_bf16 v[48:51], v[196:199], v[244:247], v[48:51]\n"
    "ds_read_b128 v[200:203], v5 offset:4352\n"
    "v_mfma_f32_16x16x32_bf16 v[44:47], v[136:139], v[244:247], v[44:47]\n"
    "v_mfma_f32_16x16x32_bf16 v[48:51], v[136:139], v[216:219], v[48:51]\n"
    "ds_read_b128 v[208:211], v5 offset:4480\n"
    "v_mfma_f32_16x16x32_bf16 v[44:47], v[112:115], v[216:219], v[44:47]\n"
    "v_mfma_f32_16x16x32_bf16 v[48:51], v[112:115], v[192:195], v[48:51]\n"
    "ds_read_b128 v[172:175], v5 offset:4608\n"
    "v_mfma_f32_16x16x32_bf16 v[44:47], v[148:151], v[192:195], v[44:47]\n"
    "s_waitcnt lgkmcnt(3)\n"
    "v_mfma_f32_16x16x32_bf16 v[48:51], v[148:151], v[84:87], v[48:51]\n"
    "ds_read_b128 v[124:127], v5 offset:4736\n"
    "v_mfma_f32_16x16x32_bf16 v[44:47], v[164:167], v[84:87], v[44:47]\n"
    "v_mfma_f32_16x16x32_bf16 v[48:51], v[164:167], v[120:123], v[48:51]\n"
    "ds_read_b128 v[212:215], v5 offset:4864\n"
    "v_mfma_f32_16x16x32_bf16 v[44:47], v[204:207], v[120:123], v[44:47]\n"
    "ds_read_b128 v[228:231], v5 offset:576\n"
    "v_mfma_f32_16x16x32_bf16 v[48:51], v[204:207], v[152:155], v[48:51]\n"
    "ds_read_b128 v[244:247], v5 offset:704\n"
    "v_mfma_f32_16x16x32_bf16 v[44:47], v[236:239], v[152:155], v[44:47]\n"
    "s_waitcnt lgkmcnt(4)\n"
    "v_mfma_f32_16x16x32_bf16 v[48:51], v[236:239], v[200:203], v[48:51]\n"
    "ds_read_b128 v[216:219], v5 offset:832\n"
    "v_mfma_f32_16x16x32_bf16 v[44:47], v[252:255], v[200:203], v[44:47]\n"
    "v_mfma_f32_16x16x32_bf16 v[48:51], v[252:255], v[208:211], v[48:51]\n"
    "ds_read_b128 v[192:195], v5 offset:960\n"
    "v_mfma_f32_16x16x32_bf16 v[44:47], v[132:135], v[208:211], v[44:47]\n"
    "v_mfma_f32_16x16x32_bf16 v[48:51], v[132:135], v[172:175], v[48:51]\n"
    "ds_read_b128 v[84:87], v5 offset:1088\n"
    "v_mfma_f32_16x16x32_bf16 v[44:47], v[168:171], v[172:175], v[44:47]\n"
    "s_waitcnt lgkmcnt(3)\n"
    "v_mfma_f32_16x16x32_bf16 v[48:51], v[168:171], v[124:127], v[48:51]\n"
    "ds_read_b128 v[120:123], v5 offset:1216\n"
    "v_mfma_f32_16x16x32_bf16 v[44:47], v[220:223], v[124:127], v[44:47]\n"
    "v_mfma_f32_16x16x32_bf16 v[48:51], v[220:223], v[212:215], v[48:51]\n"
    "ds_read_b128 v[152:155], v5 offset:1344\n"
    "v_mfma_f32_16x16x32_bf16 v[36:39], v[96:99], v[228:231], v[36:39]\n"
    "v_mfma_f32_16x16x32_bf16 v[40:43], v[96:99], v[244:247], v[40:43]\n"
    "ds_read_b128 v[200:203], v5 offset:1472\n"
    "v_mfma_f32_16x16x32_bf16 v[36:39], v[104:107], v[244:247], v[36:39]\n"
    "s_nop 1\n"
    "v_cvt_pk_bf16_f32 v44, v44, v45\n"
    "v_cvt_pk_bf16_f32 v45, v46, v47\n"
    "v_cvt_pk_bf16_f32 v46, v48, v49\n"
    "v_cvt_pk_bf16_f32 v47, v50, v51\n"
    "s_waitcnt lgkmcnt(3)\n"
    "v_mfma_f32_16x16x32_bf16 v[40:43], v[104:107], v[216:219], v[40:43]\n"
    "ds_read_b128 v[208:211], v5 offset:1600\n"
    "v_min_u32 v44, -2, v44\n"
    "v_min_u32 v45, -2, v45\n"
    "v_mfma_f32_16x16x32_bf16 v[36:39], v[108:111], v[216:219], v[36:39]\n"
    "v_min_u32 v46, -2, v46\n"
    "v_min_u32 v47, -2, v47\n"
    "s_xor_b32 s43, s41, 110592\n"
    "buffer_store_dwordx4 v[44:47], v8, s[24:27], s43 offen sc1\n"
    "v_mfma_f32_16x16x32_bf16 v[40:43], v[108:111], v[192:195], v[40:43]\n"
    "ds_read_b128 v[172:175], v5 offset:1728\n"
    "v_mfma_f32_16x16x32_bf16 v[36:39], v[180:183], v[192:195], v[36:39]\n"
    "v_mfma_f32_16x16x32_bf16 v[40:43], v[180:183], v[84:87], v[40:43]\n"
    "ds_read_b128 v[124:127], v5 offset:1856\n"
    "v_mfma_f32_16x16x32_bf16 v[36:39], v[116:119], v[84:87], v[36:39]\n"
    "s_waitcnt lgkmcnt(3)\n"
    "v_mfma_f32_16x16x32_bf16 v[40:43], v[116:119], v[120:123], v[40:43]\n"
    "ds_read_b128 v[212:215], v5 offset:1984\n"
    "v_mfma_f32_16x16x32_bf16 v[36:39], v[128:131], v[120:123], v[36:39]\n"
    "v_mfma_f32_16x16x32_bf16 v[40:43], v[128:131], v[152:155], v[40:43]\n"
    "ds_read_b128 v[228:231], v5 offset:2144\n"
    "v_mfma_f32_16x16x32_bf16 v[36:39], v[144:147], v[152:155], v[36:39]\n"
    "v_mfma_f32_16x16x32_bf16 v[40:43], v[144:147], v[200:203], v[40:43]\n"
    "ds_read_b128 v[96:99], v5 offset:2272\n"
    "v_mfma_f32_16x16x32_bf16 v[36:39], v[88:91], v[200:203], v[36:39]\n"
    "s_waitcnt lgkmcnt(3)\n"
    "v_mfma_f32_16x16x32_bf16 v[40:43], v[88:91], v[208:211], v[40:43]\n"
    "ds_read_b128 v[244:247], v5 offset:2400\n"
    "v_mfma_f32_16x16x32_bf16 v[36:39], v[140:143], v[208:211], v[36:39]\n"
    "v_mfma_f32_16x16x32_bf16 v[40:43], v[140:143], v[172:175], v[40:43]\n"
    "ds_read_b128 v[104:107], v5 offset:2528\n"
    "v_mfma_f32_16x16x32_bf16 v[36:39], v[156:159], v[172:175], v[36:39]\n"
    "v_mfma_f32_16x16x32_bf16 v[40:43], v[156:159], v[124:127], v[40:43]\n"
    "ds_read_b128 v[216:219], v5 offset:2656\n"
    "v_mfma_f32_16x16x32_bf16 v[36:39], v[188:191], v[124:127], v[36:39]\n"
    "s_waitcnt lgkmcnt(3)\n"
    "v_mfma_f32_16x16x32_bf16 v[40:43], v[188:191], v[212:215], v[40:43]\n"
    "ds_read_b128 v[108:111], v5 offset:2784\n"
    "v_mfma_f32_16x16x32_bf16 v[36:39], v[92:95], v[212:215], v[36:39]\n"
    "v_mfma_f32_16x16x32_bf16 v[40:43], v[92:95], v[228:231], v[40:43]\n"
    "ds_read_b128 v[192:195], v5 offset:2912\n"
    "v_mfma_f32_16x16x32_bf16 v[36:39], v[224:227], v[228:231], v[36:39]\n"
    "v_mfma_f32_16x16x32_bf16 v[40:43], v[224:227], v[96:99], v[40:43]\n"
    "ds_read_b128 v[180:183], v5 offset:3040\n"
    "v_mfma_f32_16x16x32_bf16 v[36:39], v[232:235], v[96:99], v[36:39]\n"
    "s_waitcnt lgkmcnt(3)\n"
    "v_mfma_f32_16x16x32_bf16 v[40:43], v[232:235], v[244:247], v[40:43]\n"
    "ds_read_b128 v[84:87], v5 offset:3168\n"
    "v_mfma_f32_16x16x32_bf16 v[36:39], v[240:243], v[244:247], v[36:39]\n"
    "v_mfma_f32_16x16x32_bf16 v[40:43], v[240:243], v[104:107], v[40:43]\n"
    "ds_read_b128 v[116:119], v5 offset:3296\n"
    "v_mfma_f32_16x16x32_bf16 v[36:39], v[248:251], v[104:107], v[36:39]\n"
    "v_mfma_f32_16x16x32_bf16 v[40:43], v[248:251], v[216:219], v[40:43]\n"
    "ds_read_b128 v[120:123], v5 offset:3424\n"
    "v_mfma_f32_16x16x32_bf16 v[36:39], v[184:187], v[216:219], v[36:39]\n"
    "s_waitcnt lgkmcnt(3)\n"
    "v_mfma_f32_16x16x32_bf16 v[40:43], v[184:187], v[108:111], v[40:43]\n"
    "ds_read_b128 v[128:131], v5 offset:3552\n"
    "v_mfma_f32_16x16x32_bf16 v[36:39], v[100:103], v[108:111], v[36:39]\n"
    "v_mfma_f32_16x16x32_bf16 v[40:43], v[100:103], v[192:195], v[40:43]\n"
    "ds_read_b128 v[152:155], v5 offset:3680\n"
    "v_mfma_f32_16x16x32_bf16 v[36:39], v[160:163], v[192:195], v[36:39]\n"
    "v_mfma_f32_16x16x32_bf16 v[40:43], v[160:163], v[180:183], v[40:43]\n"
    "ds_read_b128 v[144:147], v5 offset:3808\n"
    "v_mfma_f32_16x16x32_bf16 v[36:39], v[176:179], v[180:183], v[36:39]\n"
    "s_waitcnt lgkmcnt(3)\n"
    "v_mfma_f32_16x16x32_bf16 v[40:43], v[176:179], v[84:87], v[40:43]\n"
    "ds_read_b128 v[200:203], v5 offset:3936\n"
    "v_mfma_f32_16x16x32_bf16 v[36:39], v[196:199], v[84:87], v[36:39]\n"
    "v_mfma_f32_16x16x32_bf16 v[40:43], v[196:199], v[116:119], v[40:43]\n"
    "ds_read_b128 v[88:91], v5 offset:4064\n"
    "v_mfma_f32_16x16x32_bf16 v[36:39], v[136:139], v[116:119], v[36:39]\n"
    "v_mfma_f32_16x16x32_bf16 v[40:43], v[136:139], v[120:123], v[40:43]\n"
    "ds_read_b128 v[208:211], v5 offset:4224\n"
    "v_mfma_f32_16x16x32_bf16 v[36:39], v[112:115], v[120:123], v[36:39]\n"
    "s_waitcnt lgkmcnt(3)\n"
    "v_mfma_f32_16x16x32_bf16 v[40:43], v[112:115], v[128:131], v[40:43]\n"
    "ds_read_b128 v[140:143], v5 offset:4352\n"
    "v_mfma_f32_16x16x32_bf16 v[36:39], v[148:151], v[128:131], v[36:39]\n"
    "v_mfma_f32_16x16x32_bf16 v[40:43], v[148:151], v[152:155], v[40:43]\n"
    "ds_read_b128 v[172:175], v5 offset:4480\n"
    "v_mfma_f32_16x16x32_bf16 v[36:39], v[164:167], v[152:155], v[36:39]\n"
    "v_mfma_f32_16x16x32_bf16 v[40:43], v[164:167], v[144:147], v[40:43]\n"
    "ds_read_b128 v[156:159], v5 offset:4608\n"
    "v_mfma_f32_16x16x32_bf16 v[36:39], v[204:207], v[144:147], v[36:39]\n"
    "s_waitcnt lgkmcnt(3)\n"
    "v_mfma_f32_16x16x32_bf16 v[40:43], v[204:207], v[200:203], v[40:43]\n"
    "v_mfma_f32_16x16x32_bf16 v[36:39], v[236:239], v[200:203], v[36:39]\n"
    "v_mfma_f32_16x16x32_bf16 v[40:43], v[236:239], v[88:91], v[40:43]\n"
    "v_mfma_f32_16x16x32_bf16 v[36:39], v[252:255], v[88:91], v[36:39]\n"
    "v_mfma_f32_16x16x32_bf16 v[40:43], v[252:255], v[208:211], v[40:43]\n"
    "v_mfma_f32_16x16x32_bf16 v[36:39], v[132:135], v[208:211], v[36:39]\n"
    "s_waitcnt lgkmcnt(0)\n"
    "v_mfma_f32_16x16x32_bf16 v[40:43], v[132:135], v[140:143], v[40:43]\n"
    "v_mfma_f32_16x16x32_bf16 v[36:39], v[168:171], v[140:143], v[36:39]\n"
    "v_mfma_f32_16x16x32_bf16 v[40:43], v[168:171], v[172:175], v[40:43]\n"
    "v_mfma_f32_16x16x32_bf16 v[36:39], v[220:223], v[172:175], v[36:39]\n"
    "v_mfma_f32_16x16x32_bf16 v[40:43], v[220:223], v[156:159], v[40:43]\n"
    "s_nop 6\n"
    "ds_write_b128 v16, v[36:39]\n"
    "ds_write_b128 v16, v[40:43] offset:1088\n"
    "s_waitcnt lgkmcnt(0)\n"
    "s_barrier\n"
    "ds_read_b128 v[96:99], v14\n"
    "ds_read_b128 v[100:103], v14 offset:2176\n"
    "ds_read_b128 v[104:107], v14 offset:4352\n"
    "s_waitcnt lgkmcnt(1)\n"
    "v_fmac_f32 v100, s56, v96\n"
    "v_fmac_f32 v101, s56, v97\n"
    "v_fmac_f32 v102, s56, v98\n"
    "v_fmac_f32 v103, s56, v99\n"
    "v_exp_f32 v100, v100\n"
    "v_exp_f32 v101, v101\n"
    "v_exp_f32 v102, v102\n"
    "v_exp_f32 v103, v103\n"
    "v_add_f32 v100, 1.0, v100\n"
    "v_add_f32 v101, 1.0, v101\n"
    "v_add_f32 v102, 1.0, v102\n"
    "v_add_f32 v103, 1.0, v103\n"
    "v_rcp_f32 v100, v100\n"
    "v_rcp_f32 v101, v101\n"
    "v_rcp_f32 v102, v102\n"
    "v_rcp_f32 v103, v103\n"
    "v_fma_f32 v100, v100, s58, v17\n"
    "v_fma_f32 v101, v101, s58, v17\n"
    "v_fma_f32 v102, v102, s58, v17\n"
    "v_fma_f32 v103, v103, s58, v17\n"
    "s_waitcnt lgkmcnt(0)\n"
    "v_mul_f32 v100, v100, v104\n"
    "v_mul_f32 v101, v101, v105\n"
    "v_mul_f32 v102, v102, v106\n"
    "v_mul_f32 v103, v103, v107\n"
    "v_cvt_pk_bf16_f32 v96, v100, v101\n"
    "v_cvt_pk_bf16_f32 v97, v102, v103\n"
    "buffer_store_dwordx2 v[96:97], v15, s[28:31], 0 offen\n"
    "s_endpgm\n"
    ".Lt3_1%=:\n"
    ".Lpad7%=:\n"
    "s_cbranch_execz .Lpad7r%=\n"
    "s_waitcnt lgkmcnt(1)\n"
    "v_mfma_f32_16x16x32_bf16 v[32:35], v[252:255], v[228:231], v[32:35]\n"
    "ds_read_b128 v[192:195], v5 offset:4352\n"
    "v_mfma_f32_16x16x32_bf16 v[20:23], v[132:135], v[124:127], v[20:23]\n"
    "ds_read_b128 v[84:87], v5 offset:576\n"
    "v_mfma_f32_16x16x32_bf16 v[24:27], v[132:135], v[212:215], v[24:27]\n"
    "ds_read_b128 v[120:123], v5 offset:704\n"
    "v_mfma_f32_16x16x32_bf16 v[28:31], v[132:135], v[228:231], v[28:31]\n"
    "v_mfma_f32_16x16x32_bf16 v[32:35], v[132:135], v[244:247], v[32:35]\n"
    "ds_read_b128 v[152:155], v5 offset:832\n"
    "v_mfma_f32_16x16x32_bf16 v[20:23], v[168:171], v[212:215], v[20:23]\n"
    "v_mfma_f32_16x16x32_bf16 v[24:27], v[168:171], v[228:231], v[24:27]\n"
    "ds_read_b128 v[200:203], v5 offset:960\n"
    "v_mfma_f32_16x16x32_bf16 v[28:31], v[168:171], v[244:247], v[28:31]\n"
    "s_waitcnt lgkmcnt(4)\n"
    "v_mfma_f32_16x16x32_bf16 v[32:35], v[168:171], v[216:219], v[32:35]\n"
    "ds_read_b128 v[208:211], v5 offset:1088\n"
    "v_mfma_f32_16x16x32_bf16 v[20:23], v[220:223], v[228:231], v[20:23]\n"
    "v_mfma_f32_16x16x32_bf16 v[24:27], v[220:223], v[244:247], v[24:27]\n"
    "ds_read_b128 v[172:175], v5 offset:1216\n"
    "v_mfma_f32_16x16x32_bf16 v[28:31], v[220:223], v[216:219], v[28:31]\n"
    "v_mfma_f32_16x16x32_bf16 v[32:35], v[220:223], v[192:195], v[32:35]\n"
    "ds_read_b128 v[124:127], v5 offset:1344\n"
    "s_waitcnt lgkmcnt(4)\n"
    "v_mfma_f32_16x16x32_bf16 v[36:39], v[96:99], v[84:87], v[36:39]\n"
    "v_mfma_f32_16x16x32_bf16 v[40:43], v[96:99], v[120:123], v[40:43]\n"
    "ds_read_b128 v[212:215], v5 offset:1472\n"
    "v_mfma_f32_16x16x32_bf16 v[36:39], v[104:107], v[120:123], v[36:39]\n"
    "v_cvt_pk_bf16_f32 v20, v20, v21\n"
    "v_cvt_pk_bf16_f32 v21, v22, v23\n"
    "v_mfma_f32_16x16x32_bf16 v[40:43], v[104:107], v[152:155], v[40:43]\n"
    "ds_read_b128 v[228:231], v5 offset:1600\n"
    "v_mfma_f32_16x16x32_bf16 v[36:39], v[108:111], v[152:155], v[36:39]\n"
    "v_cvt_pk_bf16_f32 v22, v24, v25\n"
    "v_cvt_pk_bf16_f32 v23, v26, v27\n"
    "s_waitcnt lgkmcnt(3)\n"
    "v_mfma_f32_16x16x32_bf16 v[40:43], v[108:111], v[200:203], v[40:43]\n"
    "ds_read_b128 v[244:247], v5 offset:1728\n"
    "v_mfma_f32_16x16x32_bf16 v[36:39], v[180:183], v[200:203], v[36:39]\n"
    "v_min_u32 v20, -2, v20\n"
    "v_min_u32 v21, -2, v21\n"
    "v_mfma_f32_16x16x32_bf16 v[40:43], v[180:183], v[208:211], v[40:43]\n"
    "ds_read_b128 v[216:219], v5 offset:1856\n"
    "v_mfma_f32_16x16x32_bf16 v[36:39], v[116:119], v[208:211], v[36:39]\n"
    "v_min_u32 v22, -2, v22\n"
    "v_min_u32 v23, -2, v23\n"
    "v_mfma_f32_16x16x32_bf16 v[40:43], v[116:119], v[172:175], v[40:43]\n"
    "ds_read_b128 v[192:195], v5 offset:1984\n"
    "v_mfma_f32_16x16x32_bf16 v[36:39], v[128:131], v[172:175], v[36:39]\n"
    "s_xor_b32 s43, s41, 0\n"
    "buffer_store_dwordx4 v[20:23], v8, s[24:27], s43 offen sc1\n"
    "s_waitcnt lgkmcnt(3)\n"
    "v_mfma_f32_16x16x32_bf16 v[40:43], v[128:131], v[124:127], v[40:43]\n"
    "ds_read_b128 v[84:87], v5 offset:2144\n"
    "v_mfma_f32_16x16x32_bf16 v[36:39], v[144:147], v[124:127], v[36:39]\n"
    "v_cvt_pk_bf16_f32 v28, v28, v29\n"
    "v_cvt_pk_bf16_f32 v29, v30, v31\n"
    "v_mfma_f32_16x16x32_bf16 v[40:43], v[144:147], v[212:215], v[40:43]\n"
    "ds_read_b128 v[120:123], v5 offset:2272\n"
    "v_mfma_f32_16x16x32_bf16 v[36:39], v[88:91], v[212:215], v[36:39]\n"
    "v_cvt_pk_bf16_f32 v30, v32, v33\n"
    "v_cvt_pk_bf16_f32 v31, v34, v35\n"
    "v_mfma_f32_16x16x32_bf16 v[40:43], v[88:91], v[228:231], v[40:43]\n"
    "ds_read_b128 v[152:155], v5 offset:2400\n"
    "v_mfma_f32_16x16x32_bf16 v[36:39], v[140:143], v[228:231], v[36:39]\n"
    "v_min_u32 v28, -2, v28\n"
    "v_min_u32 v29, -2, v29\n"
    "s_waitcnt lgkmcnt(3)\n"
    "v_mfma_f32_16x16x32_bf16 v[40:43], v[140:143], v[244:247], v[40:43]\n"
    "ds_read_b128 v[200:203], v5 offset:2528\n"
    "v_mfma_f32_16x16x32_bf16 v[36:39], v[156:159], v[244:247], v[36:39]\n"
    "v_min_u32 v30, -2, v30\n"
    "v_min_u32 v31, -2, v31\n"
    "v_mfma_f32_16x16x32_bf16 v[40:43], v[156:159], v[216:219], v[40:43]\n"
    "ds_read_b128 v[208:211], v5 offset:2656\n"
    "v_mfma_f32_16x16x32_bf16 v[36:39], v[188:191], v[216:219], v[36:39]\n"
    "s_xor_b32 s43, s41, 36864\n"
    "buffer_store_dwordx4 v[28:31], v8, s[24:27], s43 offen sc1\n"
    "v_mfma_f32_16x16x32_bf16 v[40:43], v[188:191], v[192:195], v[40:43]\n"
    "ds_read_b128 v[172:175], v5 offset:2784\n"
    "v_mfma_f32_16x16x32_bf16 v[36:39], v[92:95], v[192:195], v[36:39]\n"
    "s_waitcnt lgkmcnt(3)\n"
    "v_mfma_f32_16x16x32_bf16 v[40:43], v[92:95], v[84:87], v[40:43]\n"
    "ds_read_b128 v[124:127], v5 offset:2912\n"
    "v_mfma_f32_16x16x32_bf16 v[36:39], v[224:227], v[84:87], v[36:39]\n"
    "v_mfma_f32_16x16x32_bf16 v[40:43], v[224:227], v[120:123], v[40:43]\n"
    "ds_read_b128 v[212:215], v5 offset:3040\n"
    "v_mfma_f32_16x16x32_bf16 v[36:39], v[232:235], v[120:123], v[36:39]\n"
    "v_mfma_f32_16x16x32_bf16 v[40:43], v[232:235], v[152:155], v[40:43]\n"
    "ds_read_b128 v[228:231], v5 offset:3168\n"
    "v_mfma_f32_16x16x32_bf16 v[36:39], v[240:243], v[152:155], v[36:39]\n"
    "s_waitcnt lgkmcnt(3)\n"
    "v_mfma_f32_16x16x32_bf16 v[40:43], v[240:243], v[200:203], v[40:43]\n"
    "ds_read_b128 v[244:247], v5 offset:3296\n"
    "v_mfma_f32_16x16x32_bf16 v[36:39], v[248:251], v[200:203], v[36:39]\n"
    "v_mfma_f32_16x16x32_bf16 v[40:43], v[248:251], v[208:211], v[40:43]\n"
    "ds_read_b128 v[216:219], v5 offset:3424\n"
    "v_mfma_f32_16x16x32_bf16 v[36:39], v[184:187], v[208:211], v[36:39]\n"
    "v_mfma_f32_16x16x32_bf16 v[40:43], v[184:187], v[172:175], v[40:43]\n"
    "ds_read_b128 v[192:195], v5 offset:3552\n"
    "v_mfma_f32_16x16x32_bf16 v[36:39], v[100:103], v[172:175], v[36:39]\n"
    "s_waitcnt lgkmcnt(3)\n"
    "v_mfma_f32_16x16x32_bf16 v[40:43], v[100:103], v[124:127], v[40:43]\n"
    "ds_read_b128 v[84:87], v5 offset:3680\n"
    "v_mfma_f32_16x16x32_bf16 v[36:39], v[160:163], v[124:127], v[36:39]\n"
    "v_mfma_f32_16x16x32_bf16 v[40:43], v[160:163], v[212:215], v[40:43]\n"
    "ds_read_b128 v[120:123], v5 offset:3808\n"
    "v_mfma_f32_16x16x32_bf16 v[36:39], v[176:179], v[212:215], v[36:39]\n"
    "v_mfma_f32_16x16x32_bf16 v[40:43], v[176:179], v[228:231], v[40:43]\n"
    "ds_read_b128 v[152:155], v5 offset:3936\n"
    "v_mfma_f32_16x16x32_bf16 v[36:39], v[196:199], v[228:231], v[36:39]\n"
    "s_waitcnt lgkmcnt(3)\n"
    "v_mfma_f32_16x16x32_bf16 v[40:43], v[196:199], v[244:247], v[40:43]\n"
    "ds_read_b128 v[200:203], v5 offset:4064\n"
    "v_mfma_f32_16x16x32_bf16 v[36:39], v[136:139], v[244:247], v[36:39]\n"
    "v_mfma_f32_16x16x32_bf16 v[40:43], v[136:139], v[216:219], v[40:43]\n"
    "ds_read_b128 v[208:211], v5 offset:4224\n"
    "v_mfma_f32_16x16x32_bf16 v[36:39], v[112:115], v[216:219], v[36:39]\n"
    "v_mfma_f32_16x16x32_bf16 v[40:43], v[112:115], v[192:195], v[40:43]\n"
    "ds_read_b128 v[172:175], v5 offset:4352\n"
    "v_mfma_f32_16x16x32_bf16 v[36:39], v[148:151], v[192:195], v[36:39]\n"
    "s_waitcnt lgkmcnt(3)\n"
    "v_mfma_f32_16x16x32_bf16 v[40:43], v[148:151], v[84:87], v[40:43]\n"
    "ds_read_b128 v[124:127], v5 offset:4480\n"
    "v_mfma_f32_16x16x32_bf16 v[36:39], v[164:167], v[84:87], v[36:39]\n"
    "v_mfma_f32_16x16x32_bf16 v[40:43], v[164:167], v[120:123], v[40:43]\n"
    "ds_read_b128 v[212:215], v5 offset:4608\n"
    "v_mfma_f32_16x16x32_bf16 v[36:39], v[204:207], v[120:123], v[36:39]\n"
    "ds_read_b128 v[228:231], v5 offset:832\n"
    "v_mfma_f32_16x16x32_bf16 v[40:43], v[204:207], v[152:155], v[40:43]\n"
    "ds_read_b128 v[244:247], v5 offset:960\n"
    "v_mfma_f32_16x16x32_bf16 v[36:39], v[236:239], v[152:155], v[36:39]\n"
    "s_waitcnt lgkmcnt(4)\n"
    "v_mfma_f32_16x16x32_bf16 v[40:43], v[236:239], v[200:203], v[40:43]\n"
    "ds_read_b128 v[216:219], v5 offset:1088\n"
    "v_mfma_f32_16x16x32_bf16 v[36:39], v[252:255], v[200:203], v[36:39]\n"
    "v_mfma_f32_16x16x32_bf16 v[40:43], v[252:255], v[208:211], v[40:43]\n"
    "ds_read_b128 v[192:195], v5 offset:1216\n"
    "v_mfma_f32_16x16x32_bf16 v[36:39], v[132:135], v[208:211], v[36:39]\n"
    "v_mfma_f32_16x16x32_bf16 v[40:43], v[132:135], v[172:175], v[40:43]\n"
    "ds_read_b128 v[84:87], v5 offset:1344\n"
    "v_mfma_f32_16x16x32_bf16 v[36:39], v[168:171], v[172:175], v[36:39]\n"
    "s_waitcnt lgkmcnt(3)\n"
    "v_mfma_f32_16x16x32_bf16 v[40:43], v[168:171], v[124:127], v[40:43]\n"
    "ds_read_b128 v[120:123], v5 offset:1472\n"
    "v_mfma_f32_16x16x32_bf16 v[36:39], v[220:223], v[124:127], v[36:39]\n"
    "v_mfma_f32_16x16x32_bf16 v[40:43], v[220:223], v[212:215], v[40:43]\n"
    "ds_read_b128 v[152:155], v5 offset:1600\n"
    "v_mfma_f32_16x16x32_bf16 v[44:47], v[96:99], v[228:231], v[44:47]\n"
    "v_mfma_f32_16x16x32_bf16 v[48:51], v[96:99], v[244:247], v[48:51]\n"
    "ds_read_b128 v[200:203], v5 offset:1728\n"
    "v_mfma_f32_16x16x32_bf16 v[44:47], v[104:107], v[244:247], v[44:47]\n"
    "s_nop 1\n"
    "v_cvt_pk_bf16_f32 v36, v36, v37\n"
    "v_cvt_pk_bf16_f32 v37, v38, v39\n"
    "v_cvt_pk_bf16_f32 v38, v40, v41\n"
    "v_cvt_pk_bf16_f32 v39, v42, v43\n"
    "s_waitcnt lgkmcnt(3)\n"
    "v_mfma_f32_16x16x32_bf16 v[48:51], v[104:107], v[216:219], v[48:51]\n"
    "ds_read_b128 v[208:211], v5 offset:1856\n"
    "v_min_u32 v36, -2, v36\n"
    "v_min_u32 v37, -2, v37\n"
    "v_mfma_f32_16x16x32_bf16 v[44:47], v[108:111], v[216:219], v[44:47]\n"
    "v_min_u32 v38, -2, v38\n"
    "v_min_u32 v39, -2, v39\n"
    "s_xor_b32 s43, s41, 73728\n"
    "buffer_store_dwordx4 v[36:39], v8, s[24:27], s43 offen sc1\n"
    "v_mfma_f32_16x16x32_bf16 v[48:51], v[108:111], v[192:195], v[48:51]\n"
    "ds_read_b128 v[172:175], v5 offset:1984\n"
    "v_mfma_f32_16x16x32_bf16 v[44:47], v[180:183], v[192:195], v[44:47]\n"
    "v_mfma_f32_16x16x32_bf16 v[48:51], v[180:183], v[84:87], v[48:51]\n"
    "ds_read_b128 v[124:127], v5 offset:2144\n"
    "v_mfma_f32_16x16x32_bf16 v[44:47], v[116:119], v[84:87], v[44:47]\n"
    "s_waitcnt lgkmcnt(3)\n"
    "v_mfma_f32_16x16x32_bf16 v[48:51], v[116:119], v[120:123], v[48:51]\n"
    "ds_read_b128 v[212:215], v5 offset:2272\n"
    "v_mfma_f32_16x16x32_bf16 v[44:47], v[128:131], v[120:123], v[44:47]\n"
    "v_mfma_f32_16x16x32_bf16 v[48:51], v[128:131], v[152:155], v[48:51]\n"
    "ds_read_b128 v[228:231], v5 offset:2400\n"
    "v_mfma_f32_16x16x32_bf16 v[44:47], v[144:147], v[152:155], v[44:47]\n"
    "v_mfma_f32_16x16x32_bf16 v[48:51], v[144:147], v[200:203], v[48:51]\n"
    "ds_read_b128 v[96:99], v5 offset:2528\n"
    "v_mfma_f32_16x16x32_bf16 v[44:47], v[88:91], v[200:203], v[44:47]\n"
    "s_waitcnt lgkmcnt(3)\n"
    "v_mfma_f32_16x16x32_bf16 v[48:51], v[88:91], v[208:211], v[48:51]\n"
    "ds_read_b128 v[244:247], v5 offset:2656\n"
    "v_mfma_f32_16x16x32_bf16 v[44:47], v[140:143], v[208:211], v[44:47]\n"
    "v_mfma_f32_16x16x32_bf16 v[48:51], v[140:143], v[172:175], v[48:51]\n"
    "ds_read_b128 v[104:107], v5 offset:2784\n"
    "v_mfma_f32_16x16x32_bf16 v[44:47], v[156:159], v[172:175], v[44:47]\n"
    "v_mfma_f32_16x16x32_bf16 v[48:51], v[156:159], v[124:127], v[48:51]\n"
    "ds_read_b128 v[216:219], v5 offset:2912\n"
    "v_mfma_f32_16x16x32_bf16 v[44:47], v[188:191], v[124:127], v[44:47]\n"
    "s_waitcnt lgkmcnt(3)\n"
    "v_mfma_f32_16x16x32_bf16 v[48:51], v[188:191], v[212:215], v[48:51]\n"
    "ds_read_b128 v[108:111], v5 offset:3040\n"
    "v_mfma_f32_16x16x32_bf16 v[44:47], v[92:95], v[212:215], v[44:47]\n"
    "v_mfma_f32_16x16x32_bf16 v[48:51], v[92:95], v[228:231], v[48:51]\n"
    "ds_read_b128 v[192:195], v5 offset:3168\n"
    "v_mfma_f32_16x16x32_bf16 v[44:47], v[224:227], v[228:231], v[44:47]\n"
    "v_mfma_f32_16x16x32_bf16 v[48:51], v[224:227], v[96:99], v[48:51]\n"
    "ds_read_b128 v[180:183], v5 offset:3296\n"
    "v_mfma_f32_16x16x32_bf16 v[44:47], v[232:235], v[96:99], v[44:47]\n"
    "s_waitcnt lgkmcnt(3)\n"
    "v_mfma_f32_16x16x32_bf16 v[48:51], v[232:235], v[244:247], v[48:51]\n"
    "ds_read_b128 v[84:87], v5 offset:3424\n"
    "v_mfma_f32_16x16x32_bf16 v[44:47], v[240:243], v[244:247], v[44:47]\n"
    "v_mfma_f32_16x16x32_bf16 v[48:51], v[240:243], v[104:107], v[48:51]\n"
    "ds_read_b128 v[116:119], v5 offset:3552\n"
    "v_mfma_f32_16x16x32_bf16 v[44:47], v[248:251], v[104:107], v[44:47]\n"
    "v_mfma_f32_16x16x32_bf16 v[48:51], v[248:251], v[216:219], v[48:51]\n"
    "ds_read_b128 v[120:123], v5 offset:3680\n"
    "v_mfma_f32_16x16x32_bf16 v[44:47], v[184:187], v[216:219], v[44:47]\n"
    "s_waitcnt lgkmcnt(3)\n"
    "v_mfma_f32_16x16x32_bf16 v[48:51], v[184:187], v[108:111], v[48:51]\n"
    "ds_read_b128 v[128:131], v5 offset:3808\n"
    "v_mfma_f32_16x16x32_bf16 v[44:47], v[100:103], v[108:111], v[44:47]\n"
    "v_mfma_f32_16x16x32_bf16 v[48:51], v[100:103], v[192:195], v[48:51]\n"
    "ds_read_b128 v[152:155], v5 offset:3936\n"
    "v_mfma_f32_16x16x32_bf16 v[44:47], v[160:163], v[192:195], v[44:47]\n"
    "v_mfma_f32_16x16x32_bf16 v[48:51], v[160:163], v[180:183], v[48:51]\n"
    "ds_read_b128 v[144:147], v5 offset:4064\n"
    "v_mfma_f32_16x16x32_bf16 v[44:47], v[176:179], v[180:183], v[44:47]\n"
    "s_waitcnt lgkmcnt(3)\n"
    "v_mfma_f32_16x16x32_bf16 v[48:51], v[176:179], v[84:87], v[48:51]\n"
    "ds_read_b128 v[200:203], v5 offset:4224\n"
    "v_mfma_f32_16x16x32_bf16 v[44:47], v[196:199], v[84:87], v[44:47]\n"
    "v_mfma_f32_16x16x32_bf16 v[48:51], v[196:199], v[116:119], v[48:51]\n"
    "ds_read_b128 v[88:91], v5 offset:4352\n"
    "v_mfma_f32_16x16x32_bf16 v[44:47], v[136:139], v[116:119], v[44:47]\n"
    "v_mfma_f32_16x16x32_bf16 v[48:51], v[136:139], v[120:123], v[48:51]\n"
    "ds_read_b128 v[208:211], v5 offset:4480\n"
    "v_mfma_f32_16x16x32_bf16 v[44:47], v[112:115], v[120:123], v[44:47]\n"
    "s_waitcnt lgkmcnt(3)\n"
    "v_mfma_f32_16x16x32_bf16 v[48:51], v[112:115], v[128:131], v[48:51]\n"
    "ds_read_b128 v[140:143], v5 offset:4608\n"
    "v_mfma_f32_16x16x32_bf16 v[44:47], v[148:151], v[128:131], v[44:47]\n"
    "v_mfma_f32_16x16x32_bf16 v[48:51], v[148:151], v[152:155], v[48:51]\n"
    "ds_read_b128 v[172:175], v5 offset:4736\n"
    "v_mfma_f32_16x16x32_bf16 v[44:47], v[164:167], v[152:155], v[44:47]\n"
    "v_mfma_f32_16x16x32_bf16 v[48:51], v[164:167], v[144:147], v[48:51]\n"
    "ds_read_b128 v[156:159], v5 offset:4864\n"
    "v_mfma_f32_16x16x32_bf16 v[44:47], v[204:207], v[144:147], v[44:47]\n"
    "s_waitcnt lgkmcnt(3)\n"
    "v_mfma_f32_16x16x32_bf16 v[48:51], v[204:207], v[200:203], v[48:51]\n"
    "v_mfma_f32_16x16x32_bf16 v[44:47], v[236:239], v[200:203], v[44:47]\n"
    "v_mfma_f32_16x16x32_bf16 v[48:51], v[236:239], v[88:91], v[48:51]\n"
    "v_mfma_f32_16x16x32_bf16 v[44:47], v[252:255], v[88:91], v[44:47]\n"
    "v_mfma_f32_16x16x32_bf16 v[48:51], v[252:255], v[208:211], v[48:51]\n"
    "v_mfma_f32_16x16x32_bf16 v[44:47], v[132:135], v[208:211], v[44:47]\n"
    "s_waitcnt lgkmcnt(0)\n"
    "v_mfma_f32_16x16x32_bf16 v[48:51], v[132:135], v[140:143], v[48:51]\n"
    "v_mfma_f32_16x16x32_bf16 v[44:47], v[168:171], v[140:143], v[44:47]\n"
    "v_mfma_f32_16x16x32_bf16 v[48:51], v[168:171], v[172:175], v[48:51]\n"
    "v_mfma_f32_16x16x32_bf16 v[44:47], v[220:223], v[172:175], v[44:47]\n"
    "v_mfma_f32_16x16x32_bf16 v[48:51], v[220:223], v[156:159], v[48:51]\n"
    "s_nop 6\n"
    "ds_write_b128 v16, v[44:47]\n"
    "ds_write_b128 v16, v[48:51] offset:1088\n"
    "s_waitcnt lgkmcnt(0)\n"
    "s_barrier\n"
    "ds_read_b128 v[96:99], v14\n"
    "ds_read_b128 v[100:103], v14 offset:2176\n"
    "ds_read_b128 v[104:107], v14 offset:4352\n"
    "s_waitcnt lgkmcnt(1)\n"
    "v_fmac_f32 v100, s56, v96\n"
    "v_fmac_f32 v101, s56, v97\n"
    "v_fmac_f32 v102, s56, v98\n"
    "v_fmac_f32 v103, s56, v99\n"
    "v_exp_f32 v100, v100\n"
    "v_exp_f32 v101, v101\n"
    "v_exp_f32 v102, v102\n"
    "v_exp_f32 v103, v103\n"
    "v_add_f32 v100, 1.0, v100\n"
    "v_add_f32 v101, 1.0, v101\n"
    "v_add_f32 v102, 1.0, v102\n"
    "v_add_f32 v103, 1.0, v103\n"
    "v_rcp_f32 v100, v100\n"
    "v_rcp_f32 v101, v101\n"
    "v_rcp_f32 v102, v102\n"
    "v_rcp_f32 v103, v103\n"
    "v_fma_f32 v100, v100, s58, v17\n"
    "v_fma_f32 v101, v101, s58, v17\n"
    "v_fma_f32 v102, v102, s58, v17\n"
    "v_fma_f32 v103, v103, s58, v17\n"
    "s_waitcnt lgkmcnt(0)\n"
    "v_mul_f32 v100, v100, v104\n"
    "v_mul_f32 v101, v101, v105\n"
    "v_mul_f32 v102, v102, v106\n"
    "v_mul_f32 v103, v103, v107\n"
    "v_cvt_pk_bf16_f32 v96, v100, v101\n"
    "v_cvt_pk_bf16_f32 v97, v102, v103\n"
    "buffer_store_dwordx2 v[96:97], v15, s[28:31], 0 offen\n"
    "s_endpgm\n"
    ".Lt4_2%=:\n"
    ".Lpad8%=:\n"
    "s_cbranch_execz .Lpad8r%=\n"
    "v_mfma_f32_16x16x32_bf16 v[48:51], v[252:255], v[136:139], v[48:51]\n"
    "ds_read_b128 v[124:127], v5 offset:5696\n"
    "v_mfma_f32_16x16x32_bf16 v[56:59], v[252:255], v[144:147], v[56:59]\n"
    "s_barrier\n"
    "ds_read_b64_tr_b16 v[96:97], v6 offset:20480\n"
    "v_mfma_f32_16x16x32_bf16 v[64:67], v[252:255], v[152:155], v[64:67]\n"
    "ds_read_b64_tr_b16 v[98:99], v6 offset:20992\n"
    "ds_read_b128 v[184:187], v5 offset:576\n"
    "v_mfma_f32_16x16x32_bf16 v[72:75], v[252:255], v[88:91], v[72:75]\n"
    "ds_read_b128 v[132:135], v5 offset:704\n"
    "ds_read_b128 v[100:103], v5 offset:832\n"
    "s_waitcnt lgkmcnt(6)\n"
    "v_mfma_f32_16x16x32_bf16 v[80:83], v[252:255], v[112:115], v[80:83]\n"
    "ds_read_b128 v[160:163], v5 offset:960\n"
    "v_mfma_f32_16x16x32_bf16 v[192:195], v[224:227], v[140:143], v[192:195]\n"
    "ds_read_b64_tr_b16 v[104:105], v6 offset:24576\n"
    "ds_read_b64_tr_b16 v[106:107], v6 offset:25088\n"
    "v_mfma_f32_16x16x32_bf16 v[196:199], v[224:227], v[148:151], v[196:199]\n"
    "v_mfma_f32_16x16x32_bf16 v[200:203], v[224:227], v[156:159], v[200:203]\n"
    "v_mfma_f32_16x16x32_bf16 v[204:207], v[224:227], v[172:175], v[204:207]\n"
    "ds_read_b128 v[168:171], v5 offset:1088\n"
    "v_mfma_f32_16x16x32_bf16 v[208:211], v[224:227], v[188:191], v[208:211]\n"
    "ds_read_b64_tr_b16 v[108:109], v6 offset:28672\n"
    "ds_read_b64_tr_b16 v[110:111], v6 offset:29184\n"
    "v_mfma_f32_16x16x32_bf16 v[212:215], v[224:227], v[164:167], v[212:215]\n"
    "v_add_f32 v20, v20, v192\n"
    "v_mfma_f32_16x16x32_bf16 v[216:219], v[224:227], v[92:95], v[216:219]\n"
    "v_add_f32 v21, v21, v193\n"
    "s_waitcnt lgkmcnt(5)\n"
    "v_mfma_f32_16x16x32_bf16 v[220:223], v[224:227], v[124:127], v[220:223]\n"
    "ds_read_b128 v[176:179], v5 offset:1216\n"
    "v_mfma_f32_16x16x32_bf16 v[36:39], v[96:99], v[184:187], v[36:39]\n"
    "ds_read_b64_tr_b16 v[180:181], v6 offset:32768\n"
    "ds_read_b64_tr_b16 v[182:183], v6 offset:33280\n"
    "v_mfma_f32_16x16x32_bf16 v[40:43], v[96:99], v[132:135], v[40:43]\n"
    "v_add_f32 v22, v22, v194\n"
    "v_mfma_f32_16x16x32_bf16 v[44:47], v[96:99], v[100:103], v[44:47]\n"
    "v_add_f32 v23, v23, v195\n"
    "v_mfma_f32_16x16x32_bf16 v[48:51], v[96:99], v[160:163], v[48:51]\n"
    "ds_read_b128 v[84:87], v5 offset:1344\n"
    "s_waitcnt lgkmcnt(4)\n"
    "v_mfma_f32_16x16x32_bf16 v[36:39], v[104:107], v[132:135], v[36:39]\n"
    "ds_read_b64_tr_b16 v[116:117], v6 offset:36864\n"
    "ds_read_b64_tr_b16 v[118:119], v6 offset:37376\n"
    "v_mfma_f32_16x16x32_bf16 v[40:43], v[104:107], v[100:103], v[40:43]\n"
    "v_add_f32 v24, v24, v192\n"
    "v_mfma_f32_16x16x32_bf16 v[44:47], v[104:107], v[160:163], v[44:47]\n"
    "v_add_f32 v25, v25, v193\n"
    "v_mfma_f32_16x16x32_bf16 v[48:51], v[104:107], v[168:171], v[48:51]\n"
    "ds_read_b128 v[120:123], v5 offset:1472\n"
    "v_mfma_f32_16x16x32_bf16 v[36:39], v[108:111], v[100:103], v[36:39]\n"
    "ds_read_b64_tr_b16 v[128:129], v6 offset:40960\n"
    "ds_read_b64_tr_b16 v[130:131], v6 offset:41472\n"
    "v_mfma_f32_16x16x32_bf16 v[40:43], v[108:111], v[160:163], v[40:43]\n"
    "v_add_f32 v26, v26, v194\n"
    "v_mfma_f32_16x16x32_bf16 v[44:47], v[108:111], v[168:171], v[44:47]\n"
    "v_add_f32 v27, v27, v195\n"
    "s_waitcnt lgkmcnt(5)\n"
    "v_mfma_f32_16x16x32_bf16 v[48:51], v[108:111], v[176:179], v[48:51]\n"
    "ds_read_b128 v[136:139], v5 offset:1600\n"
    "v_mfma_f32_16x16x32_bf16 v[36:39], v[180:183], v[160:163], v[36:39]\n"
    ".Lpad9%=:\n"
    "s_cbranch_execz .Lpad9r%=\n"
    "ds_read_b64_tr_b16 v[144:145], v6 offset:45056\n"
    "ds_read_b64_tr_b16 v[146:147], v6 offset:45568\n"
    "v_mfma_f32_16x16x32_bf16 v[40:43], v[180:183], v[168:171], v[40:43]\n"
    "v_add_f32 v28, v28, v196\n"
    "v_mfma_f32_16x16x32_bf16 v[44:47], v[180:183], v[176:179], v[44:47]\n"
    "v_add_f32 v29, v29, v197\n"
    "v_mfma_f32_16x16x32_bf16 v[48:51], v[180:183], v[84:87], v[48:51]\n"
    "ds_read_b128 v[152:155], v5 offset:1728\n"
    "s_waitcnt lgkmcnt(4)\n"
    "v_mfma_f32_16x16x32_bf16 v[36:39], v[116:119], v[168:171], v[36:39]\n"
    "ds_read_b64_tr_b16 v[88:89], v6 offset:49152\n"
    "ds_read_b64_tr_b16 v[90:91], v6 offset:49664\n"
    "v_mfma_f32_16x16x32_bf16 v[40:43], v[116:119], v[176:179], v[40:43]\n"
    "v_add_f32 v30, v30, v198\n"
    "v_mfma_f32_16x16x32_bf16 v[44:47], v[116:119], v[84:87], v[44:47]\n"
    "v_add_f32 v31, v31, v199\n"
    "v_mfma_f32_16x16x32_bf16 v[48:51], v[116:119], v[120:123], v[48:51]\n"
    "ds_read_b128 v[112:115], v5 offset:1856\n"
    "v_mfma_f32_16x16x32_bf16 v[36:39], v[128:131], v[176:179], v[36:39]\n"
    "ds_read_b64_tr_b16 v[140:141], v6 offset:53248\n"
    "ds_read_b64_tr_b16 v[142:143], v6 offset:53760\n"
    "v_mfma_f32_16x16x32_bf16 v[40:43], v[128:131], v[84:87], v[40:43]\n"
    "v_add_f32 v32, v32, v196\n"
    "v_mfma_f32_16x16x32_bf16 v[44:47], v[128:131], v[120:123], v[44:47]\n"
    "v_add_f32 v33, v33, v197\n"
    "s_waitcnt lgkmcnt(5)\n"
    "v_mfma_f32_16x16x32_bf16 v[48:51], v[128:131], v[136:139], v[48:51]\n"
    "ds_read_b128 v[148:151], v5 offset:1984\n"
    "v_mfma_f32_16x16x32_bf16 v[36:39], v[144:147], v[84:87], v[36:39]\n"
    "s_barrier\n"
    "ds_read_b64_tr_b16 v[156:157], v6 offset:57344\n"
    "v_mfma_f32_16x16x32_bf16 v[40:43], v[144:147], v[120:123], v[40:43]\n"
    "ds_read_b64_tr_b16 v[158:159], v6 offset:57856\n"
    "v_add_f32 v34, v34, v198\n"
    "v_mfma_f32_16x16x32_bf16 v[44:47], v[144:147], v[136:139], v[44:47]\n"
    "v_add_f32 v35, v35, v199\n"
    "v_mfma_f32_16x16x32_bf16 v[48:51], v[144:147], v[152:155], v[48:51]\n"
    "ds_read_b128 v[172:175], v5 offset:2144\n"
    "s_waitcnt lgkmcnt(4)\n"
    "v_mfma_f32_16x16x32_bf16 v[36:39], v[88:91], v[120:123], v[36:39]\n"
    "ds_read_b64_tr_b16 v[188:189], v6 offset:61440\n"
    "ds_read_b64_tr_b16 v[190:191], v6 offset:61952\n"
    "v_mfma_f32_16x16x32_bf16 v[40:43], v[88:91], v[136:139], v[40:43]\n"
    "v_add_f32 v52, v52, v208\n"
    "v_mfma_f32_16x16x32_bf16 v[44:47], v[88:91], v[152:155], v[44:47]\n"
    "v_add_f32 v53, v53, v209\n"
    "v_mfma_f32_16x16x32_bf16 v[48:51], v[88:91], v[112:115], v[48:51]\n"
    "ds_read_b128 v[164:167], v5 offset:2272\n"
    "v_mfma_f32_16x16x32_bf16 v[36:39], v[140:143], v[136:139], v[36:39]\n"
    "ds_read_b64_tr_b16 v[92:93], v7 offset:0\n"
    "ds_read_b64_tr_b16 v[94:95], v7 offset:512\n"
    "v_mfma_f32_16x16x32_bf16 v[40:43], v[140:143], v[152:155], v[40:43]\n"
    "v_add_f32 v54, v54, v210\n"
    "v_mfma_f32_16x16x32_bf16 v[44:47], v[140:143], v[112:115], v[44:47]\n"
    "v_add_f32 v55, v55, v211\n"
    "s_waitcnt lgkmcnt(5)\n"
    "v_mfma_f32_16x16x32_bf16 v[48:51], v[140:143], v[148:151], v[48:51]\n"
    "ds_read_b128 v[124:127], v5 offset:2400\n"
    "v_mfma_f32_16x16x32_bf16 v[36:39], v[156:159], v[152:155], v[36:39]\n"
    "ds_read_b64_tr_b16 v[224:225], v7 offset:4096\n"
    "ds_read_b64_tr_b16 v[226:227], v7 offset:4608\n"
    "v_mfma_f32_16x16x32_bf16 v[40:43], v[156:159], v[112:115], v[40:43]\n"
    "v_add_f32 v56, v56, v208\n"
    "v_mfma_f32_16x16x32_bf16 v[44:47], v[156:159], v[148:151], v[44:47]\n"
    "v_add_f32 v57, v57, v209\n"
    "v_mfma_f32_16x16x32_bf16 v[48:51], v[156:159], v[172:175], v[48:51]\n"
    "ds_read_b128 v[228:231], v5 offset:2528\n"
    "s_waitcnt lgkmcnt(4)\n"
    "v_mfma_f32_16x16x32_bf16 v[36:39], v[188:191], v[112:115], v[36:39]\n"
    "ds_read_b64_tr_b16 v[232:233], v7 offset:8192\n"
    "ds_read_b64_tr_b16 v[234:235], v7 offset:8704\n"
    "v_mfma_f32_16x16x32_bf16 v[40:43], v[188:191], v[148:151], v[40:43]\n"
    "v_add_f32 v58, v58, v210\n"
    "v_mfma_f32_16x16x32_bf16 v[44:47], v[188:191], v[172:175], v[44:47]\n"
    "v_add_f32 v59, v59, v211\n"
    "v_mfma_f32_16x16x32_bf16 v[48:51], v[188:191], v[164:167], v[48:51]\n"
    "ds_read_b128 v[236:239], v5 offset:2656\n"
    "v_mfma_f32_16x16x32_bf16 v[36:39], v[92:95], v[148:151], v[36:39]\n"
    "ds_read_b64_tr_b16 v[240:241], v7 offset:12288\n"
    "ds_read_b64_tr_b16 v[242:243], v7 offset:12800\n"
    "v_mfma_f32_16x16x32_bf16 v[40:43], v[92:95], v[172:175], v[40:43]\n"
    "v_add_f32 v60, v60, v212\n"
    "v_mfma_f32_16x16x32_bf16 v[44:47], v[92:95], v[164:167], v[44:47]\n"
    "v_add_f32 v61, v61, v213\n"
    "s_waitcnt lgkmcnt(5)\n"
    "v_mfma_f32_16x16x32_bf16 v[48:51], v[92:95], v[124:127], v[48:51]\n"
    "ds_read_b128 v[244:247], v5 offset:2784\n"
    "v_mfma_f32_16x16x32_bf16 v[36:39], v[224:227], v[172:175], v[36:39]\n"
    "ds_read_b64_tr_b16 v[248:249], v7 offset:16384\n"
    "ds_read_b64_tr_b16 v[250:251], v7 offset:16896\n"
    "v_mfma_f32_16x16x32_bf16 v[40:43], v[224:227], v[164:167], v[40:43]\n"
    "v_add_f32 v62, v62, v214\n"
    "v_mfma_f32_16x16x32_bf16 v[44:47], v[224:227], v[124:127], v[44:47]\n"
    "v_add_f32 v63, v63, v215\n"
    "v_mfma_f32_16x16x32_bf16 v[48:51], v[224:227], v[228:231], v[48:51]\n"
    "ds_read_b128 v[252:255], v5 offset:2912\n"
    "s_waitcnt lgkmcnt(4)\n"
    "v_mfma_f32_16x16x32_bf16 v[36:39], v[232:235], v[164:167], v[36:39]\n"
    "ds_read_b64_tr_b16 v[184:185], v7 offset:20480\n"
    "ds_read_b64_tr_b16 v[186:187], v7 offset:20992\n"
    "v_mfma_f32_16x16x32_bf16 v[40:43], v[232:235], v[124:127], v[40:43]\n"
    "v_add_f32 v64, v64, v212\n"
    "v_mfma_f32_16x16x32_bf16 v[44:47], v[232:235], v[228:231], v[44:47]\n"
    "v_add_f32 v65, v65, v213\n"
    "v_mfma_f32_16x16x32_bf16 v[48:51], v[232:235], v[236:239], v[48:51]\n"
    "ds_read_b128 v[132:135], v5 offset:3040\n"
    "v_mfma_f32_16x16x32_bf16 v[36:39], v[240:243], v[124:127], v[36:39]\n"
    "ds_read_b64_tr_b16 v[100:101], v7 offset:24576\n"
    "ds_read_b64_tr_b16 v[102:103], v7 offset:25088\n"
    "v_mfma_f32_16x16x32_bf16 v[40:43], v[240:243], v[228:231], v[40:43]\n"
    "v_add_f32 v66, v66, v214\n"
    "v_mfma_f32_16x16x32_bf16 v[44:47], v[240:243], v[236:239], v[44:47]\n"
    "v_add_f32 v67, v67, v215\n"
    "s_waitcnt lgkmcnt(5)\n"
    "v_mfma_f32_16x16x32_bf16 v[48:51], v[240:243], v[244:247], v[48:51]\n"
    "ds_read_b128 v[192:195], v5 offset:3168\n"
    "v_mfma_f32_16x16x32_bf16 v[36:39], v[248:251], v[228:231], v[36:39]\n"
    "ds_read_b64_tr_b16 v[160:161], v7 offset:28672\n"
    "ds_read_b64_tr_b16 v[162:163], v7 offset:29184\n"
    "v_mfma_f32_16x16x32_bf16 v[40:43], v[248:251], v[236:239], v[40:43]\n"
    "v_add_f32 v68, v68, v216\n"
    "v_mfma_f32_16x16x32_bf16 v[44:47], v[248:251], v[244:247], v[44:47]\n"
    "v_add_f32 v69, v69, v217\n"
    "v_mfma_f32_16x16x32_bf16 v[48:51], v[248:251], v[252:255], v[48:51]\n"
    "ds_read_b128 v[168:171], v5 offset:3296\n"
    "s_waitcnt lgkmcnt(4)\n"
    "v_mfma_f32_16x16x32_bf16 v[36:39], v[184:187], v[236:239], v[36:39]\n"
    "s_barrier\n"
    "ds_read_b64_tr_b16 v[176:177], v7 offset:32768\n"
    "v_mfma_f32_16x16x32_bf16 v[40:43], v[184:187], v[244:247], v[40:43]\n"
    "ds_read_b64_tr_b16 v[178:179], v7 offset:33280\n"
    "v_add_f32 v70, v70, v218\n"
    "v_mfma_f32_16x16x32_bf16 v[44:47], v[184:187], v[252:255], v[44:47]\n"
    "v_add_f32 v71, v71, v219\n"
    "v_mfma_f32_16x16x32_bf16 v[48:51], v[184:187], v[132:135], v[48:51]\n"
    "ds_read_b128 v[84:87], v5 offset:3424\n"
    "v_mfma_f32_16x16x32_bf16 v[36:39], v[100:103], v[244:247], v[36:39]\n"
    "ds_read_b64_tr_b16 v[196:197], v7 offset:36864\n"
    "ds_read_b64_tr_b16 v[198:199], v7 offset:37376\n"
    "v_mfma_f32_16x16x32_bf16 v[40:43], v[100:103], v[252:255], v[40:43]\n"
    "v_add_f32 v72, v72, v216\n"
    "v_mfma_f32_16x16x32_bf16 v[44:47], v[100:103], v[132:135], v[44:47]\n"
    "v_add_f32 v73, v73, v217\n"
    "s_waitcnt lgkmcnt(5)\n"
    "v_mfma_f32_16x16x32_bf16 v[48:51], v[100:103], v[192:195], v[48:51]\n"
    "ds_read_b128 v[120:123], v5 offset:3552\n"
    "v_mfma_f32_16x16x32_bf16 v[36:39], v[160:163], v[252:255], v[36:39]\n"
    "ds_read_b64_tr_b16 v[136:137], v6 offset:0\n"
    "ds_read_b64_tr_b16 v[138:139], v6 offset:512\n"
    "v_mfma_f32_16x16x32_bf16 v[40:43], v[160:163], v[132:135], v[40:43]\n"
    "v_add_f32 v74, v74, v218\n"
    "v_mfma_f32_16x16x32_bf16 v[44:47], v[160:163], v[192:195], v[44:47]\n"
    "v_add_f32 v75, v75, v219\n"
    "v_mfma_f32_16x16x32_bf16 v[48:51], v[160:163], v[168:171], v[48:51]\n"
    "ds_read_b128 v[152:155], v5 offset:3680\n"
    "s_waitcnt lgkmcnt(4)\n"
    "v_mfma_f32_16x16x32_bf16 v[36:39], v[176:179], v[132:135], v[36:39]\n"
    "ds_read_b64_tr_b16 v[112:113], v6 offset:4096\n"
    "ds_read_b64_tr_b16 v[114:115], v6 offset:4608\n"
    "v_mfma_f32_16x16x32_bf16 v[40:43], v[176:179], v[192:195], v[40:43]\n"
    "v_add_f32 v76, v76, v220\n"
    "v_mfma_f32_16x16x32_bf16 v[44:47], v[176:179], v[168:171], v[44:47]\n"
    "v_add_f32 v77, v77, v221\n"
    "v_mfma_f32_16x16x32_bf16 v[48:51], v[176:179], v[84:87], v[48:51]\n"
    "ds_read_b128 v[208:211], v5 offset:3808\n"
    "v_mfma_f32_16x16x32_bf16 v[36:39], v[196:199], v[192:195], v[36:39]\n"
    "ds_read_b64_tr_b16 v[148:149], v6 offset:8192\n"
    "ds_read_b64_tr_b16 v[150:151], v6 offset:8704\n"
    "v_mfma_f32_16x16x32_bf16 v[40:43], v[196:199], v[168:171], v[40:43]\n"
    "v_add_f32 v78, v78, v222\n"
    "v_mfma_f32_16x16x32_bf16 v[44:47], v[196:199], v[84:87], v[44:47]\n"
    "v_add_f32 v79, v79, v223\n"
    "s_waitcnt lgkmcnt(5)\n"
    "v_mfma_f32_16x16x32_bf16 v[48:51], v[196:199], v[120:123], v[48:51]\n"
    "ds_read_b128 v[172:175], v5 offset:3936\n"
    "v_mfma_f32_16x16x32_bf16 v[36:39], v[136:139], v[168:171], v[36:39]\n"
    "ds_read_b64_tr_b16 v[164:165], v6 offset:12288\n"
    "ds_read_b64_tr_b16 v[166:167], v6 offset:12800\n"
    "v_mfma_f32_16x16x32_bf16 v[40:43], v[136:139], v[84:87], v[40:43]\n"
    "v_add_f32 v80, v80, v220\n"
    "v_mfma_f32_16x16x32_bf16 v[44:47], v[136:139], v[120:123], v[44:47]\n"
    "v_add_f32 v81, v81, v221\n"
    "v_mfma_f32_16x16x32_bf16 v[48:51], v[136:139], v[152:155], v[48:51]\n"
    "ds_read_b128 v[124:127], v5 offset:4064\n"
    "s_waitcnt lgkmcnt(4)\n"
    "v_mfma_f32_16x16x32_bf16 v[36:39], v[112:115], v[84:87], v[36:39]\n"
    "ds_read_b64_tr_b16 v[212:213], v6 offset:16384\n"
    "ds_read_b64_tr_b16 v[214:215], v6 offset:16896\n"
    "v_mfma_f32_16x16x32_bf16 v[40:43], v[112:115], v[120:123], v[40:43]\n"
    "v_add_f32 v82, v82, v222\n"
    "v_mfma_f32_16x16x32_bf16 v[44:47], v[112:115], v[152:155], v[44:47]\n"
    "v_add_f32 v83, v83, v223\n"
    "v_mfma_f32_16x16x32_bf16 v[48:51], v[112:115], v[208:211], v[48:51]\n"
    "ds_read_b128 v[228:231], v5 offset:4224\n"
    "v_mfma_f32_16x16x32_bf16 v[36:39], v[148:151], v[120:123], v[36:39]\n"
    "s_barrier\n"
    "ds_read_b64_tr_b16 v[236:237], v6 offset:20480\n"
    "v_mfma_f32_16x16x32_bf16 v[40:43], v[148:151], v[152:155], v[40:43]\n"
    "ds_read_b64_tr_b16 v[238:239], v6 offset:20992\n"
    "v_mfma_f32_16x16x32_bf16 v[44:47], v[148:151], v[208:211], v[44:47]\n"
    "s_waitcnt lgkmcnt(5)\n"
    "v_mfma_f32_16x16x32_bf16 v[48:51], v[148:151], v[172:175], v[48:51]\n"
    "ds_read_b128 v[244:247], v5 offset:4352\n"
    "v_mfma_f32_16x16x32_bf16 v[36:39], v[164:167], v[152:155], v[36:39]\n"
    "ds_read_b64_tr_b16 v[252:253], v6 offset:24576\n"
    "ds_read_b64_tr_b16 v[254:255], v6 offset:25088\n"
    "v_mfma_f32_16x16x32_bf16 v[40:43], v[164:167], v[208:211], v[40:43]\n"
    "v_mfma_f32_16x16x32_bf16 v[44:47], v[164:167], v[172:175], v[44:47]\n"
    "v_mfma_f32_16x16x32_bf16 v[48:51], v[164:167], v[124:127], v[48:51]\n"
    "ds_read_b128 v[216:219], v5 offset:4480\n"
    "s_waitcnt lgkmcnt(5)\n"
    "v_mfma_f32_16x16x32_bf16 v[36:39], v[212:215], v[208:211], v[36:39]\n"
    "ds_read_b64_tr_b16 v[132:133], v6 offset:28672\n"
    "ds_read_b64_tr_b16 v[134:135], v6 offset:29184\n"
    "v_mfma_f32_16x16x32_bf16 v[40:43], v[212:215], v[172:175], v[40:43]\n"
    "v_mfma_f32_16x16x32_bf16 v[44:47], v[212:215], v[124:127], v[44:47]\n"
    "v_mfma_f32_16x16x32_bf16 v[48:51], v[212:215], v[228:231], v[48:51]\n"
    "ds_read_b128 v[192:195], v5 offset:4608\n"
    "s_waitcnt lgkmcnt(4)\n"
    "v_mfma_f32_16x16x32_bf16 v[36:39], v[236:239], v[172:175], v[36:39]\n"
    "ds_read_b64_tr_b16 v[168:169], v6 offset:32768\n"
    "ds_read_b64_tr_b16 v[170:171], v6 offset:33280\n"
    "v_mfma_f32_16x16x32_bf16 v[40:43], v[236:239], v[124:127], v[40:43]\n"
    "v_mfma_f32_16x16x32_bf16 v[44:47], v[236:239], v[228:231], v[44:47]\n"
    "v_mfma_f32_16x16x32_bf16 v[48:51], v[236:239], v[244:247], v[48:51]\n"
    "ds_read_b128 v[84:87], v5 offset:4736\n"
    "v_mfma_f32_16x16x32_bf16 v[36:39], v[252:255], v[124:127], v[36:39]\n"
    "ds_read_b64_tr_b16 v[220:221], v6 offset:36864\n"
    "ds_read_b64_tr_b16 v[222:223], v6 offset:37376\n"
    "v_mfma_f32_16x16x32_bf16 v[40:43], v[252:255], v[228:231], v[40:43]\n"
    "v_mfma_f32_16x16x32_bf16 v[44:47], v[252:255], v[244:247], v[44:47]\n"
    "s_waitcnt lgkmcnt(5)\n"
    "v_mfma_f32_16x16x32_bf16 v[48:51], v[252:255], v[216:219], v[48:51]\n"
    "ds_read_b128 v[120:123], v5 offset:4864\n"
    "v_mfma_f32_16x16x32_bf16 v[36:39], v[132:135], v[228:231], v[36:39]\n"
    "ds_read_b128 v[152:155], v5 offset:64\n"
    "v_mfma_f32_16x16x32_bf16 v[40:43], v[132:135], v[244:247], v[40:43]\n"
    "ds_read_b128 v[208:211], v5 offset:192\n"
    "v_mfma_f32_16x16x32_bf16 v[44:47], v[132:135], v[216:219], v[44:47]\n"
    "ds_read_b128 v[172:175], v5 offset:320\n"
    "v_mfma_f32_16x16x32_bf16 v[48:51], v[132:135], v[192:195], v[48:51]\n"
    "ds_read_b128 v[124:127], v5 offset:448\n"
    "s_waitcnt lgkmcnt(5)\n"
    "v_mfma_f32_16x16x32_bf16 v[36:39], v[168:171], v[244:247], v[36:39]\n"
    "v_mfma_f32_16x16x32_bf16 v[40:43], v[168:171], v[216:219], v[40:43]\n"
    "v_mfma_f32_16x16x32_bf16 v[44:47], v[168:171], v[192:195], v[44:47]\n"
    "v_mfma_f32_16x16x32_bf16 v[48:51], v[168:171], v[84:87], v[48:51]\n"
    "ds_read_b128 v[228:231], v5 offset:576\n"
    "v_mfma_f32_16x16x32_bf16 v[36:39], v[220:223], v[216:219], v[36:39]\n"
    "v_mfma_f32_16x16x32_bf16 v[40:43], v[220:223], v[192:195], v[40:43]\n"
    "v_mfma_f32_16x16x32_bf16 v[44:47], v[220:223], v[84:87], v[44:47]\n"
    "s_waitcnt lgkmcnt(1)\n"
    "v_mfma_f32_16x16x32_bf16 v[48:51], v[220:223], v[120:123], v[48:51]\n"
    "ds_read_b128 v[244:247], v5 offset:704\n"
    "v_mfma_f32_16x16x32_bf16 v[20:23], v[96:99], v[152:155], v[20:23]\n"
    "v_mfma_f32_16x16x32_bf16 v[24:27], v[96:99], v[208:211], v[24:27]\n"
    "v_mfma_f32_16x16x32_bf16 v[28:31], v[96:99], v[172:175], v[28:31]\n"
    "v_add_f32 v36, v36, v200\n"
    "v_add_f32 v37, v37, v201\n"
    "v_mfma_f32_16x16x32_bf16 v[32:35], v[96:99], v[124:127], v[32:35]\n"
    "ds_read_b128 v[216:219], v5 offset:832\n"
    "v_add_f32 v38, v38, v202\n"
    "v_mfma_f32_16x16x32_bf16 v[20:23], v[104:107], v[208:211], v[20:23]\n"
    "v_add_f32 v39, v39, v203\n"
    "v_add_f32 v40, v40, v200\n"
    "v_mfma_f32_16x16x32_bf16 v[24:27], v[104:107], v[172:175], v[24:27]\n"
    "v_add_f32 v41, v41, v201\n"
    "v_add_f32 v42, v42, v202\n"
    "v_mfma_f32_16x16x32_bf16 v[28:31], v[104:107], v[124:127], v[28:31]\n"
    "v_add_f32 v43, v43, v203\n"
    "v_add_f32 v44, v44, v204\n"
    "s_waitcnt lgkmcnt(1)\n"
    "v_mfma_f32_16x16x32_bf16 v[32:35], v[104:107], v[228:231], v[32:35]\n"
    "ds_read_b128 v[192:195], v5 offset:960\n"
    "v_add_f32 v45, v45, v205\n"
    "v_mfma_f32_16x16x32_bf16 v[20:23], v[108:111], v[172:175], v[20:23]\n"
    "v_add_f32 v46, v46, v206\n"
    "v_add_f32 v47, v47, v207\n"
    "v_mfma_f32_16x16x32_bf16 v[24:27], v[108:111], v[124:127], v[24:27]\n"
    "v_add_f32 v48, v48, v204\n"
    "v_add_f32 v49, v49, v205\n"
    "v_mfma_f32_16x16x32_bf16 v[28:31], v[108:111], v[228:231], v[28:31]\n"
    "v_add_f32 v50, v50, v206\n"
    "v_add_f32 v51, v51, v207\n"
    "v_mfma_f32_16x16x32_bf16 v[32:35], v[108:111], v[244:247], v[32:35]\n"
    "ds_read_b128 v[84:87], v5 offset:1088\n"
    "v_mfma_f32_16x16x32_bf16 v[20:23], v[180:183], v[124:127], v[20:23]\n"
    "v_cvt_pk_bf16_f32 v36, v36, v37\n"
    "v_cvt_pk_bf16_f32 v37, v38, v39\n"
    "v_mfma_f32_16x16x32_bf16 v[24:27], v[180:183], v[228:231], v[24:27]\n"
    "v_cvt_pk_bf16_f32 v38, v40, v41\n"
    "v_cvt_pk_bf16_f32 v39, v42, v43\n"
    "v_mfma_f32_16x16x32_bf16 v[28:31], v[180:183], v[244:247], v[28:31]\n"
    "s_xor_b32 s43, s41, 73728\n"
    "buffer_store_dwordx4 v[36:39], v8, s[24:27], s43 offen sc1\n"
    "s_waitcnt lgkmcnt(1)\n"
    "v_mfma_f32_16x16x32_bf16 v[32:35], v[180:183], v[216:219], v[32:35]\n"
    "ds_read_b128 v[120:123], v5 offset:1216\n"
    "v_mfma_f32_16x16x32_bf16 v[20:23], v[116:119], v[228:231], v[20:23]\n"
    "v_cvt_pk_bf16_f32 v44, v44, v45\n"
    "v_cvt_pk_bf16_f32 v45, v46, v47\n"
    "v_mfma_f32_16x16x32_bf16 v[24:27], v[116:119], v[244:247], v[24:27]\n"
    "v_cvt_pk_bf16_f32 v46, v48, v49\n"
    "v_cvt_pk_bf16_f32 v47, v50, v51\n"
    "v_mfma_f32_16x16x32_bf16 v[28:31], v[116:119], v[216:219], v[28:31]\n"
    "s_xor_b32 s43, s41, 110592\n"
    "buffer_store_dwordx4 v[44:47], v8, s[24:27], s43 offen sc1\n"
    "v_mfma_f32_16x16x32_bf16 v[32:35], v[116:119], v[192:195], v[32:35]\n"
    "ds_read_b128 v[152:155], v5 offset:1344\n"
    "v_mfma_f32_16x16x32_bf16 v[20:23], v[128:131], v[244:247], v[20:23]\n"
    "v_mfma_f32_16x16x32_bf16 v[24:27], v[128:131], v[216:219], v[24:27]\n"
    "v_mfma_f32_16x16x32_bf16 v[28:31], v[128:131], v[192:195], v[28:31]\n"
    "s_waitcnt lgkmcnt(1)\n"
    "v_mfma_f32_16x16x32_bf16 v[32:35], v[128:131], v[84:87], v[32:35]\n"
    "s_barrier\n"
    "ds_read_b128 v[208:211], v5 offset:1472\n"
    "v_mfma_f32_16x16x32_bf16 v[20:23], v[144:147], v[216:219], v[20:23]\n"
    "v_mfma_f32_16x16x32_bf16 v[24:27], v[144:147], v[192:195], v[24:27]\n"
    "v_mfma_f32_16x16x32_bf16 v[28:31], v[144:147], v[84:87], v[28:31]\n"
    "v_mfma_f32_16x16x32_bf16 v[32:35], v[144:147], v[120:123], v[32:35]\n"
    "ds_read_b128 v[200:203], v5 offset:1600\n"
    "v_mfma_f32_16x16x32_bf16 v[20:23], v[88:91], v[192:195], v[20:23]\n"
    "v_mfma_f32_16x16x32_bf16 v[24:27], v[88:91], v[84:87], v[24:27]\n"
    "v_mfma_f32_16x16x32_bf16 v[28:31], v[88:91], v[120:123], v[28:31]\n"
    "s_waitcnt lgkmcnt(1)\n"
    "v_mfma_f32_16x16x32_bf16 v[32:35], v[88:91], v[152:155], v[32:35]\n"
    "ds_read_b128 v[172:175], v5 offset:1728\n"
    "v_mfma_f32_16x16x32_bf16 v[20:23], v[140:143], v[84:87], v[20:23]\n"
    "v_mfma_f32_16x16x32_bf16 v[24:27], v[140:143], v[120:123], v[24:27]\n"
    "v_mfma_f32_16x16x32_bf16 v[28:31], v[140:143], v[152:155], v[28:31]\n"
    "v_mfma_f32_16x16x32_bf16 v[32:35], v[140:143], v[208:211], v[32:35]\n"
    "ds_read_b128 v[204:207], v5 offset:1856\n"
    "v_mfma_f32_16x16x32_bf16 v[20:23], v[156:159], v[120:123], v[20:23]\n"
    "v_mfma_f32_16x16x32_bf16 v[24:27], v[156:159], v[152:155], v[24:27]\n"
    "v_mfma_f32_16x16x32_bf16 v[28:31], v[156:159], v[208:211], v[28:31]\n"
    "s_waitcnt lgkmcnt(1)\n"
    "v_mfma_f32_16x16x32_bf16 v[32:35], v[156:159], v[200:203], v[32:35]\n"
    "ds_read_b128 v[124:127], v5 offset:1984\n"
    "v_mfma_f32_16x16x32_bf16 v[20:23], v[188:191], v[152:155], v[20:23]\n"
    "v_mfma_f32_16x16x32_bf16 v[24:27], v[188:191], v[208:211], v[24:27]\n"
    "v_mfma_f32_16x16x32_bf16 v[28:31], v[188:191], v[200:203], v[28:31]\n"
    "v_mfma_f32_16x16x32_bf16 v[32:35], v[188:191], v[172:175], v[32:35]\n"
    "ds_read_b128 v[228:231], v5 offset:2144\n"
    "v_mfma_f32_16x16x32_bf16 v[20:23], v[92:95], v[208:211], v[20:23]\n"
    "v_mfma_f32_16x16x32_bf16 v[24:27], v[92:95], v[200:203], v[24:27]\n"
    "v_mfma_f32_16x16x32_bf16 v[28:31], v[92:95], v[172:175], v[28:31]\n"
    "s_waitcnt lgkmcnt(1)\n"
    "v_mfma_f32_16x16x32_bf16 v[32:35], v[92:95], v[204:207], v[32:35]\n"
    "ds_read_b128 v[244:247], v5 offset:2272\n"
    "v_mfma_f32_16x16x32_bf16 v[20:23], v[224:227], v[200:203], v[20:23]\n"
    "v_mfma_f32_16x16x32_bf16 v[24:27], v[224:227], v[172:175], v[24:27]\n"
    "v_mfma_f32_16x16x32_bf16 v[28:31], v[224:227], v[204:207], v[28:31]\n"
    "v_mfma_f32_16x16x32_bf16 v[32:35], v[224:227], v[124:127], v[32:35]\n"
    "ds_read_b128 v[216:219], v5 offset:2400\n"
    "v_mfma_f32_16x16x32_bf16 v[20:23], v[232:235], v[172:175], v[20:23]\n"
    "v_mfma_f32_16x16x32_bf16 v[24:27], v[232:235], v[204:207], v[24:27]\n"
    "v_mfma_f32_16x16x32_bf16 v[28:31], v[232:235], v[124:127], v[28:31]\n"
    "s_waitcnt lgkmcnt(1)\n"
    "v_mfma_f32_16x16x32_bf16 v[32:35], v[232:235], v[228:231], v[32:35]\n"
    "ds_read_b128 v[192:195], v5 offset:2528\n"
    "v_mfma_f32_16x16x32_bf16 v[20:23], v[240:243], v[204:207], v[20:23]\n"
    "v_mfma_f32_16x16x32_bf16 v[24:27], v[240:243], v[124:127], v[24:27]\n"
    "v_mfma_f32_16x16x32_bf16 v[28:31], v[240:243], v[228:231], v[28:31]\n"
    "v_mfma_f32_16x16x32_bf16 v[32:35], v[240:243], v[244:247], v[32:35]\n"
    "ds_read_b128 v[84:87], v5 offset:2656\n"
    "v_mfma_f32_16x16x32_bf16 v[20:23], v[248:251], v[124:127], v[20:23]\n"
    "v_mfma_f32_16x16x32_bf16 v[24:27], v[248:251], v[228:231], v[24:27]\n"
    "v_mfma_f32_16x16x32_bf16 v[28:31], v[248:251], v[244:247], v[28:31]\n"
    "s_waitcnt lgkmcnt(1)\n"
    "v_mfma_f32_16x16x32_bf16 v[32:35], v[248:251], v[216:219], v[32:35]\n"
    "ds_read_b128 v[120:123], v5 offset:2784\n"
    "v_mfma_f32_16x16x32_bf16 v[20:23], v[184:187], v[228:231], v[20:23]\n"
    "v_mfma_f32_16x16x32_bf16 v[24:27], v[184:187], v[244:247], v[24:27]\n"
    "v_mfma_f32_16x16x32_bf16 v[28:31], v[184:187], v[216:219], v[28:31]\n"
    "v_mfma_f32_16x16x32_bf16 v[32:35], v[184:187], v[192:195], v[32:35]\n"
    "ds_read_b128 v[152:155], v5 offset:2912\n"
    "v_mfma_f32_16x16x32_bf16 v[20:23], v[100:103], v[244:247], v[20:23]\n"
    "v_mfma_f32_16x16x32_bf16 v[24:27], v[100:103], v[216:219], v[24:27]\n"
    "v_mfma_f32_16x16x32_bf16 v[28:31], v[100:103], v[192:195], v[28:31]\n"
    "s_waitcnt lgkmcnt(1)\n"
    "v_mfma_f32_16x16x32_bf16 v[32:35], v[100:103], v[84:87], v[32:35]\n"
    "ds_read_b128 v[208:211], v5 offset:3040\n"
    "v_mfma_f32_16x16x32_bf16 v[20:23], v[160:163], v[216:219], v[20:23]\n"
    "v_mfma_f32_16x16x32_bf16 v[24:27], v[160:163], v[192:195], v[24:27]\n"
    "v_mfma_f32_16x16x32_bf16 v[28:31], v[160:163], v[84:87], v[28:31]\n"
    "v_mfma_f32_16x16x32_bf16 v[32:35], v[160:163], v[120:123], v[32:35]\n"
    "ds_read_b128 v[200:203], v5 offset:3168\n"
    "v_mfma_f32_16x16x32_bf16 v[20:23], v[176:179], v[192:195], v[20:23]\n"
    "v_mfma_f32_16x16x32_bf16 v[24:27], v[176:179], v[84:87], v[24:27]\n"
    "v_mfma_f32_16x16x32_bf16 v[28:31], v[176:179], v[120:123], v[28:31]\n"
    "s_waitcnt lgkmcnt(1)\n"
    "v_mfma_f32_16x16x32_bf16 v[32:35], v[176:179], v[152:155], v[32:35]\n"
    "ds_read_b128 v[172:175], v5 offset:3296\n"
    "v_mfma_f32_16x16x32_bf16 v[20:23], v[196:199], v[84:87], v[20:23]\n"
    "v_mfma_f32_16x16x32_bf16 v[24:27], v[196:199], v[120:123], v[24:27]\n"
    "v_mfma_f32_16x16x32_bf16 v[28:31], v[196:199], v[152:155], v[28:31]\n"
    "v_mfma_f32_16x16x32_bf16 v[32:35], v[196:199], v[208:211], v[32:35]\n"
    "ds_read_b128 v[204:207], v5 offset:3424\n"
    "v_mfma_f32_16x16x32_bf16 v[20:23], v[136:139], v[120:123], v[20:23]\n"
    "v_mfma_f32_16x16x32_bf16 v[24:27], v[136:139], v[152:155], v[24:27]\n"
    "v_mfma_f32_16x16x32_bf16 v[28:31], v[136:139], v[208:211], v[28:31]\n"
    "s_waitcnt lgkmcnt(1)\n"
    "v_mfma_f32_16x16x32_bf16 v[32:35], v[136:139], v[200:203], v[32:35]\n"
    "ds_read_b128 v[124:127], v5 offset:3552\n"
    "v_mfma_f32_16x16x32_bf16 v[20:23], v[112:115], v[152:155], v[20:23]\n"
    "v_mfma_f32_16x16x32_bf16 v[24:27], v[112:115], v[208:211], v[24:27]\n"
    "v_mfma_f32_16x16x32_bf16 v[28:31], v[112:115], v[200:203], v[28:31]\n"
    "v_mfma_f32_16x16x32_bf16 v[32:35], v[112:115], v[172:175], v[32:35]\n"
    "ds_read_b128 v[228:231], v5 offset:3680\n"
    "v_mfma_f32_16x16x32_bf16 v[20:23], v[148:151], v[208:211], v[20:23]\n"
    "v_mfma_f32_16x16x32_bf16 v[24:27], v[148:151], v[200:203], v[24:27]\n"
    "v_mfma_f32_16x16x32_bf16 v[28:31], v[148:151], v[172:175], v[28:31]\n"
    "s_waitcnt lgkmcnt(1)\n"
    "v_mfma_f32_16x16x32_bf16 v[32:35], v[148:151], v[204:207], v[32:35]\n"
    "ds_read_b128 v[244:247], v5 offset:3808\n"
    "v_mfma_f32_16x16x32_bf16 v[20:23], v[164:167], v[200:203], v[20:23]\n"
    "v_mfma_f32_16x16x32_bf16 v[24:27], v[164:167], v[172:175], v[24:27]\n"
    "v_mfma_f32_16x16x32_bf16 v[28:31], v[164:167], v[204:207], v[28:31]\n"
    "v_mfma_f32_16x16x32_bf16 v[32:35], v[164:167], v[124:127], v[32:35]\n"
    "s_waitcnt vmcnt(0)\n"
    "v_add_u32 v18, 1, v18\n"
    "ds_read_b128 v[216:219], v5 offset:3936\n"
    "v_mfma_f32_16x16x32_bf16 v[20:23], v[212:215], v[172:175], v[20:23]\n"
    "buffer_store_dword v18, v19, s[24:27], 0 offen sc1\n"
    "v_mfma_f32_16x16x32_bf16 v[24:27], v[212:215], v[204:207], v[24:27]\n"
    "v_mfma_f32_16x16x32_bf16 v[28:31], v[212:215], v[124:127], v[28:31]\n"
    "s_waitcnt lgkmcnt(1)\n"
    "v_mfma_f32_16x16x32_bf16 v[32:35], v[212:215], v[228:231], v[32:35]\n"
    "ds_read_b128 v[192:195], v5 offset:4064\n"
    "v_mfma_f32_16x16x32_bf16 v[20:23], v[236:239], v[204:207], v[20:23]\n"
    "v_mfma_f32_16x16x32_bf16 v[24:27], v[236:239], v[124:127], v[24:27]\n"
    "v_mfma_f32_16x16x32_bf16 v[28:31], v[236:239], v[228:231], v[28:31]\n"
    "v_mfma_f32_16x16x32_bf16 v[32:35], v[236:239], v[244:247], v[32:35]\n"
    "ds_read_b128 v[84:87], v5 offset:4224\n"
    "v_mfma_f32_16x16x32_bf16 v[20:23], v[252:255], v[124:127], v[20:23]\n"
    "v_mfma_f32_16x16x32_bf16 v[24:27], v[252:255], v[228:231], v[24:27]\n"
    "v_mfma_f32_16x16x32_bf16 v[28:31], v[252:255], v[244:247], v[28:31]\n"
    "s_waitcnt lgkmcnt(1)\n"
    "v_mfma_f32_16x16x32_bf16 v[32:35], v[252:255], v[216:219], v[32:35]\n"
    "ds_read_b128 v[120:123], v5 offset:4352\n"
    "v_mfma_f32_16x16x32_bf16 v[20:23], v[132:135], v[228:231], v[20:23]\n"
    "ds_read_b128 v[152:155], v5 offset:1600\n"
    "v_mfma_f32_16x16x32_bf16 v[24:27], v[132:135], v[244:247], v[24:27]\n"
    "ds_read_b128 v[208:211], v5 offset:1728\n"
    "v_mfma_f32_16x16x32_bf16 v[28:31], v[132:135], v[216:219], v[28:31]\n"
    "ds_read_b128 v[200:203], v5 offset:1856\n"
    "v_mfma_f32_16x16x32_bf16 v[32:35], v[132:135], v[192:195], v[32:35]\n"
    "ds_read_b128 v[172:175], v5 offset:1984\n"
    "v_mfma_f32_16x16x32_bf16 v[20:23], v[168:171], v[244:247], v[20:23]\n"
    "v_mfma_f32_16x16x32_bf16 v[24:27], v[168:171], v[216:219], v[24:27]\n"
    "v_mfma_f32_16x16x32_bf16 v[28:31], v[168:171], v[192:195], v[28:31]\n"
    "s_waitcnt lgkmcnt(4)\n"
    "v_mfma_f32_16x16x32_bf16 v[32:35], v[168:171], v[84:87], v[32:35]\n"
    "ds_read_b128 v[204:207], v5 offset:2144\n"
    "v_mfma_f32_16x16x32_bf16 v[20:23], v[220:223], v[216:219], v[20:23]\n"
    "v_mfma_f32_16x16x32_bf16 v[24:27], v[220:223], v[192:195], v[24:27]\n"
    "v_mfma_f32_16x16x32_bf16 v[28:31], v[220:223], v[84:87], v[28:31]\n"
    "v_mfma_f32_16x16x32_bf16 v[32:35], v[220:223], v[120:123], v[32:35]\n"
    "ds_read_b128 v[124:127], v5 offset:2272\n"
    "s_waitcnt lgkmcnt(2)\n"
    "v_mfma_f32_16x16x32_bf16 v[68:71], v[96:99], v[152:155], v[68:71]\n"
    "v_mfma_f32_16x16x32_bf16 v[72:75], v[96:99], v[208:211], v[72:75]\n"
    "v_mfma_f32_16x16x32_bf16 v[76:79], v[96:99], v[200:203], v[76:79]\n"
    "v_cvt_pk_bf16_f32 v20, v20, v21\n"
    "v_cvt_pk_bf16_f32 v21, v22, v23\n"
    "v_mfma_f32_16x16x32_bf16 v[80:83], v[96:99], v[172:175], v[80:83]\n"
    "ds_read_b128 v[228:231], v5 offset:2400\n"
    "v_mfma_f32_16x16x32_bf16 v[68:71], v[104:107], v[208:211], v[68:71]\n"
    "v_cvt_pk_bf16_f32 v22, v24, v25\n"
    "v_cvt_pk_bf16_f32 v23, v26, v27\n"
    "v_mfma_f32_16x16x32_bf16 v[72:75], v[104:107], v[200:203], v[72:75]\n"
    "v_min_u32 v20, -2, v20\n"
    "v_min_u32 v21, -2, v21\n"
    "v_mfma_f32_16x16x32_bf16 v[76:79], v[104:107], v[172:175], v[76:79]\n"
    "v_min_u32 v22, -2, v22\n"
    "v_min_u32 v23, -2, v23\n"
    "s_waitcnt lgkmcnt(1)\n"
    "v_mfma_f32_16x16x32_bf16 v[80:83], v[104:107], v[204:207], v[80:83]\n"
    "ds_read_b128 v[244:247], v5 offset:2528\n"
    "v_mfma_f32_16x16x32_bf16 v[68:71], v[108:111], v[200:203], v[68:71]\n"
    "s_xor_b32 s43, s41, 0\n"
    "buffer_store_dwordx4 v[20:23], v8, s[24:27], s43 offen sc1\n"
    "v_mfma_f32_16x16x32_bf16 v[72:75], v[108:111], v[172:175], v[72:75]\n"
    "v_cvt_pk_bf16_f32 v28, v28, v29\n"
    "v_cvt_pk_bf16_f32 v29, v30, v31\n"
    "v_mfma_f32_16x16x32_bf16 v[76:79], v[108:111], v[204:207], v[76:79]\n"
    "v_cvt_pk_bf16_f32 v30, v32, v33\n"
    "v_cvt_pk_bf16_f32 v31, v34, v35\n"
    "v_mfma_f32_16x16x32_bf16 v[80:83], v[108:111], v[124:127], v[80:83]\n"
    "ds_read_b128 v[216:219], v5 offset:2656\n"
    "v_mfma_f32_16x16x32_bf16 v[68:71], v[180:183], v[172:175], v[68:71]\n"
    "v_min_u32 v28, -2, v28\n"
    "v_min_u32 v29, -2, v29\n"
    "v_mfma_f32_16x16x32_bf16 v[72:75], v[180:183], v[204:207], v[72:75]\n"
    "v_min_u32 v30, -2, v30\n"
    "v_min_u32 v31, -2, v31\n"
    "v_mfma_f32_16x16x32_bf16 v[76:79], v[180:183], v[124:127], v[76:79]\n"
    "s_xor_b32 s43, s41, 36864\n"
    "buffer_store_dwordx4 v[28:31], v8, s[24:27], s43 offen sc1\n"
    "s_waitcnt lgkmcnt(1)\n"
    "v_mfma_f32_16x16x32_bf16 v[80:83], v[180:183], v[228:231], v[80:83]\n"
    "ds_read_b128 v[192:195], v5 offset:2784\n"
    "v_mfma_f32_16x16x32_bf16 v[68:71], v[116:119], v[204:207], v[68:71]\n"
    "v_mfma_f32_16x16x32_bf16 v[72:75], v[116:119], v[124:127], v[72:75]\n"
    "v_mfma_f32_16x16x32_bf16 v[76:79], v[116:119], v[228:231], v[76:79]\n"
    "v_mfma_f32_16x16x32_bf16 v[80:83], v[116:119], v[244:247], v[80:83]\n"
    "ds_read_b128 v[84:87], v5 offset:2912\n"
    "v_mfma_f32_16x16x32_bf16 v[68:71], v[128:131], v[124:127], v[68:71]\n"
    "v_mfma_f32_16x16x32_bf16 v[72:75], v[128:131], v[228:231], v[72:75]\n"
    "v_mfma_f32_16x16x32_bf16 v[76:79], v[128:131], v[244:247], v[76:79]\n"
    "s_waitcnt lgkmcnt(1)\n"
    "v_mfma_f32_16x16x32_bf16 v[80:83], v[128:131], v[216:219], v[80:83]\n"
    "ds_read_b128 v[120:123], v5 offset:3040\n"
    "v_mfma_f32_16x16x32_bf16 v[68:71], v[144:147], v[228:231], v[68:71]\n"
    "v_mfma_f32_16x16x32_bf16 v[72:75], v[144:147], v[244:247], v[72:75]\n"
    "v_mfma_f32_16x16x32_bf16 v[76:79], v[144:147], v[216:219], v[76:79]\n"
    "v_mfma_f32_16x16x32_bf16 v[80:83], v[144:147], v[192:195], v[80:83]\n"
    "ds_read_b128 v[152:155], v5 offset:3168\n"
    "v_mfma_f32_16x16x32_bf16 v[68:71], v[88:91], v[244:247], v[68:71]\n"
    "v_mfma_f32_16x16x32_bf16 v[72:75], v[88:91], v[216:219], v[72:75]\n"
    "v_mfma_f32_16x16x32_bf16 v[76:79], v[88:91], v[192:195], v[76:79]\n"
    "s_waitcnt lgkmcnt(1)\n"
    "v_mfma_f32_16x16x32_bf16 v[80:83], v[88:91], v[84:87], v[80:83]\n"
    "ds_read_b128 v[208:211], v5 offset:3296\n"
    "v_mfma_f32_16x16x32_bf16 v[68:71], v[140:143], v[216:219], v[68:71]\n"
    "v_mfma_f32_16x16x32_bf16 v[72:75], v[140:143], v[192:195], v[72:75]\n"
    "v_mfma_f32_16x16x32_bf16 v[76:79], v[140:143], v[84:87], v[76:79]\n"
    "v_mfma_f32_16x16x32_bf16 v[80:83], v[140:143], v[120:123], v[80:83]\n"
    "ds_read_b128 v[200:203], v5 offset:3424\n"
    "v_mfma_f32_16x16x32_bf16 v[68:71], v[156:159], v[192:195], v[68:71]\n"
    "v_mfma_f32_16x16x32_bf16 v[72:75], v[156:159], v[84:87], v[72:75]\n"
    "v_mfma_f32_16x16x32_bf16 v[76:79], v[156:159], v[120:123], v[76:79]\n"
    "s_waitcnt lgkmcnt(1)\n"
    "v_mfma_f32_16x16x32_bf16 v[80:83], v[156:159], v[152:155], v[80:83]\n"
    "ds_read_b128 v[172:175], v5 offset:3552\n"
    "v_mfma_f32_16x16x32_bf16 v[68:71], v[188:191], v[84:87], v[68:71]\n"
    "v_mfma_f32_16x16x32_bf16 v[72:75], v[188:191], v[120:123], v[72:75]\n"
    "v_mfma_f32_16x16x32_bf16 v[76:79], v[188:191], v[152:155], v[76:79]\n"
    "v_mfma_f32_16x16x32_bf16 v[80:83], v[188:191], v[208:211], v[80:83]\n"
    "ds_read_b128 v[204:207], v5 offset:3680\n"
    "v_mfma_f32_16x16x32_bf16 v[68:71], v[92:95], v[120:123], v[68:71]\n"
    "v_mfma_f32_16x16x32_bf16 v[72:75], v[92:95], v[152:155], v[72:75]\n"
    "v_mfma_f32_16x16x32_bf16 v[76:79], v[92:95], v[208:211], v[76:79]\n"
    "s_waitcnt lgkmcnt(1)\n"
    "v_mfma_f32_16x16x32_bf16 v[80:83], v[92:95], v[200:203], v[80:83]\n"
    "ds_read_b128 v[124:127], v5 offset:3808\n"
    "v_mfma_f32_16x16x32_bf16 v[68:71], v[224:227], v[152:155], v[68:71]\n"
    "v_mfma_f32_16x16x32_bf16 v[72:75], v[224:227], v[208:211], v[72:75]\n"
    "v_mfma_f32_16x16x32_bf16 v[76:79], v[224:227], v[200:203], v[76:79]\n"
    "v_mfma_f32_16x16x32_bf16 v[80:83], v[224:227], v[172:175], v[80:83]\n"
    "ds_read_b128 v[228:231], v5 offset:3936\n"
    "v_mfma_f32_16x16x32_bf16 v[68:71], v[232:235], v[208:211], v[68:71]\n"
    "v_mfma_f32_16x16x32_bf16 v[72:75], v[232:235], v[200:203], v[72:75]\n"
    "v_mfma_f32_16x16x32_bf16 v[76:79], v[232:235], v[172:175], v[76:79]\n"
    "s_waitcnt lgkmcnt(1)\n"
    "v_mfma_f32_16x16x32_bf16 v[80:83], v[232:235], v[204:207], v[80:83]\n"
    "ds_read_b128 v[244:247], v5 offset:4064\n"
    "v_mfma_f32_16x16x32_bf16 v[68:71], v[240:243], v[200:203], v[68:71]\n"
    "v_mfma_f32_16x16x32_bf16 v[72:75], v[240:243], v[172:175], v[72:75]\n"
    "v_mfma_f32_16x16x32_bf16 v[76:79], v[240:243], v[204:207], v[76:79]\n"
    "v_mfma_f32_16x16x32_bf16 v[80:83], v[240:243], v[124:127], v[80:83]\n"
    "ds_read_b128 v[216:219], v5 offset:4224\n"
    "v_mfma_f32_16x16x32_bf16 v[68:71], v[248:251], v[172:175], v[68:71]\n"
    "v_mfma_f32_16x16x32_bf16 v[72:75], v[248:251], v[204:207], v[72:75]\n"
    "v_mfma_f32_16x16x32_bf16 v[76:79], v[248:251], v[124:127], v[76:79]\n"
    "s_waitcnt lgkmcnt(1)\n"
    "v_mfma_f32_16x16x32_bf16 v[80:83], v[248:251], v[228:231], v[80:83]\n"
    "ds_read_b128 v[192:195], v5 offset:4352\n"
    ".Lpad10%=:\n"
    "s_cbranch_execz .Lpad10r%=\n"
    "v_mfma_f32_16x16x32_bf16 v[68:71], v[184:187], v[204:207], v[68:71]\n"
    "v_mfma_f32_16x16x32_bf16 v[72:75], v[184:187], v[124:127], v[72:75]\n"
    "v_mfma_f32_16x16x32_bf16 v[76:79], v[184:187], v[228:231], v[76:79]\n"
    "v_mfma_f32_16x16x32_bf16 v[80:83], v[184:187], v[244:247], v[80:83]\n"
    "ds_read_b128 v[84:87], v5 offset:4480\n"
    "v_mfma_f32_16x16x32_bf16 v[68:71], v[100:103], v[124:127], v[68:71]\n"
    "v_mfma_f32_16x16x32_bf16 v[72:75], v[100:103], v[228:231], v[72:75]\n"
    "v_mfma_f32_16x16x32_bf16 v[76:79], v[100:103], v[244:247], v[76:79]\n"
    "s_waitcnt lgkmcnt(1)\n"
    "v_mfma_f32_16x16x32_bf16 v[80:83], v[100:103], v[216:219], v[80:83]\n"
    "ds_read_b128 v[120:123], v5 offset:4608\n"
    "v_mfma_f32_16x16x32_bf16 v[68:71], v[160:163], v[228:231], v[68:71]\n"
    "v_mfma_f32_16x16x32_bf16 v[72:75], v[160:163], v[244:247], v[72:75]\n"
    "v_mfma_f32_16x16x32_bf16 v[76:79], v[160:163], v[216:219], v[76:79]\n"
    "v_mfma_f32_16x16x32_bf16 v[80:83], v[160:163], v[192:195], v[80:83]\n"
    "ds_read_b128 v[152:155], v5 offset:4736\n"
    "v_mfma_f32_16x16x32_bf16 v[68:71], v[176:179], v[244:247], v[68:71]\n"
    "v_mfma_f32_16x16x32_bf16 v[72:75], v[176:179], v[216:219], v[72:75]\n"
    "v_mfma_f32_16x16x32_bf16 v[76:79], v[176:179], v[192:195], v[76:79]\n"
    "s_waitcnt lgkmcnt(1)\n"
    "v_mfma_f32_16x16x32_bf16 v[80:83], v[176:179], v[84:87], v[80:83]\n"
    "ds_read_b128 v[208:211], v5 offset:4864\n"
    "v_mfma_f32_16x16x32_bf16 v[68:71], v[196:199], v[216:219], v[68:71]\n"
    "v_mfma_f32_16x16x32_bf16 v[72:75], v[196:199], v[192:195], v[72:75]\n"
    "v_mfma_f32_16x16x32_bf16 v[76:79], v[196:199], v[84:87], v[76:79]\n"
    "v_mfma_f32_16x16x32_bf16 v[80:83], v[196:199], v[120:123], v[80:83]\n"
    "ds_read_b128 v[200:203], v5 offset:4992\n"
    "v_mfma_f32_16x16x32_bf16 v[68:71], v[136:139], v[192:195], v[68:71]\n"
    "v_mfma_f32_16x16x32_bf16 v[72:75], v[136:139], v[84:87], v[72:75]\n"
    "v_mfma_f32_16x16x32_bf16 v[76:79], v[136:139], v[120:123], v[76:79]\n"
    "s_waitcnt lgkmcnt(1)\n"
    "v_mfma_f32_16x16x32_bf16 v[80:83], v[136:139], v[152:155], v[80:83]\n"
    "ds_read_b128 v[172:175], v5 offset:5120\n"
    "v_mfma_f32_16x16x32_bf16 v[68:71], v[112:115], v[84:87], v[68:71]\n"
    "v_mfma_f32_16x16x32_bf16 v[72:75], v[112:115], v[120:123], v[72:75]\n"
    "v_mfma_f32_16x16x32_bf16 v[76:79], v[112:115], v[152:155], v[76:79]\n"
    "v_mfma_f32_16x16x32_bf16 v[80:83], v[112:115], v[208:211], v[80:83]\n"
    "ds_read_b128 v[204:207], v5 offset:5248\n"
    "v_mfma_f32_16x16x32_bf16 v[68:71], v[148:151], v[120:123], v[68:71]\n"
    "v_mfma_f32_16x16x32_bf16 v[72:75], v[148:151], v[152:155], v[72:75]\n"
    "v_mfma_f32_16x16x32_bf16 v[76:79], v[148:151], v[208:211], v[76:79]\n"
    "s_waitcnt lgkmcnt(1)\n"
    "v_mfma_f32_16x16x32_bf16 v[80:83], v[148:151], v[200:203], v[80:83]\n"
    "ds_read_b128 v[124:127], v5 offset:5376\n"
    "v_mfma_f32_16x16x32_bf16 v[68:71], v[164:167], v[152:155], v[68:71]\n"
    "v_mfma_f32_16x16x32_bf16 v[72:75], v[164:167], v[208:211], v[72:75]\n"
    "v_mfma_f32_16x16x32_bf16 v[76:79], v[164:167], v[200:203], v[76:79]\n"
    "v_mfma_f32_16x16x32_bf16 v[80:83], v[164:167], v[172:175], v[80:83]\n"
    "ds_read_b128 v[228:231], v5 offset:5504\n"
    "v_mfma_f32_16x16x32_bf16 v[68:71], v[212:215], v[208:211], v[68:71]\n"
    "v_mfma_f32_16x16x32_bf16 v[72:75], v[212:215], v[200:203], v[72:75]\n"
    "v_mfma_f32_16x16x32_bf16 v[76:79], v[212:215], v[172:175], v[76:79]\n"
    "s_waitcnt lgkmcnt(1)\n"
    "v_mfma_f32_16x16x32_bf16 v[80:83], v[212:215], v[204:207], v[80:83]\n"
    "ds_read_b128 v[244:247], v5 offset:5632\n"
    "v_mfma_f32_16x16x32_bf16 v[68:71], v[236:239], v[200:203], v[68:71]\n"
    "v_mfma_f32_16x16x32_bf16 v[72:75], v[236:239], v[172:175], v[72:75]\n"
    "v_mfma_f32_16x16x32_bf16 v[76:79], v[236:239], v[204:207], v[76:79]\n"
    "v_mfma_f32_16x16x32_bf16 v[80:83], v[236:239], v[124:127], v[80:83]\n"
    "ds_read_b128 v[216:219], v5 offset:5760\n"
    "v_mfma_f32_16x16x32_bf16 v[68:71], v[252:255], v[172:175], v[68:71]\n"
    "v_mfma_f32_16x16x32_bf16 v[72:75], v[252:255], v[204:207], v[72:75]\n"
    "v_mfma_f32_16x16x32_bf16 v[76:79], v[252:255], v[124:127], v[76:79]\n"
    "s_cmp_ge_u32 s39, 5\n"
    "s_cbranch_scc1 .Lt5_1%=\n"
    ".Lt4_1%=:\n"
    "s_waitcnt lgkmcnt(1)\n"
    "v_mfma_f32_16x16x32_bf16 v[80:83], v[252:255], v[228:231], v[80:83]\n"
    "ds_read_b128 v[192:195], v5 offset:5888\n"
    "v_mfma_f32_16x16x32_bf16 v[68:71], v[132:135], v[204:207], v[68:71]\n"
    "ds_read_b128 v[84:87], v5 offset:1344\n"
    "v_mfma_f32_16x16x32_bf16 v[72:75], v[132:135], v[124:127], v[72:75]\n"
    "ds_read_b128 v[120:123], v5 offset:1472\n"
    "v_mfma_f32_16x16x32_bf16 v[76:79], v[132:135], v[228:231], v[76:79]\n"
    "v_mfma_f32_16x16x32_bf16 v[80:83], v[132:135], v[244:247], v[80:83]\n"
    "ds_read_b128 v[152:155], v5 offset:1600\n"
    "v_mfma_f32_16x16x32_bf16 v[68:71], v[168:171], v[124:127], v[68:71]\n"
    "v_mfma_f32_16x16x32_bf16 v[72:75], v[168:171], v[228:231], v[72:75]\n"
    "ds_read_b128 v[208:211], v5 offset:1728\n"
    "v_mfma_f32_16x16x32_bf16 v[76:79], v[168:171], v[244:247], v[76:79]\n"
    "s_waitcnt lgkmcnt(4)\n"
    "v_mfma_f32_16x16x32_bf16 v[80:83], v[168:171], v[216:219], v[80:83]\n"
    "ds_read_b128 v[200:203], v5 offset:1856\n"
    "v_mfma_f32_16x16x32_bf16 v[68:71], v[220:223], v[228:231], v[68:71]\n"
    "v_mfma_f32_16x16x32_bf16 v[72:75], v[220:223], v[244:247], v[72:75]\n"
    "ds_read_b128 v[172:175], v5 offset:1984\n"
    "v_mfma_f32_16x16x32_bf16 v[76:79], v[220:223], v[216:219], v[76:79]\n"
    "v_mfma_f32_16x16x32_bf16 v[80:83], v[220:223], v[192:195], v[80:83]\n"
    "ds_read_b128 v[204:207], v5 offset:2144\n"
    "s_waitcnt lgkmcnt(4)\n"
    "v_mfma_f32_16x16x32_bf16 v[60:63], v[96:99], v[84:87], v[60:63]\n"
    "v_mfma_f32_16x16x32_bf16 v[64:67], v[96:99], v[120:123], v[64:67]\n"
    "ds_read_b128 v[124:127], v5 offset:2272\n"
    "v_mfma_f32_16x16x32_bf16 v[60:63], v[104:107], v[120:123], v[60:63]\n"
    "v_cvt_pk_bf16_f32 v68, v68, v69\n"
    "v_cvt_pk_bf16_f32 v69, v70, v71\n"
    "v_mfma_f32_16x16x32_bf16 v[64:67], v[104:107], v[152:155], v[64:67]\n"
    "ds_read_b128 v[228:231], v5 offset:2400\n"
    "v_mfma_f32_16x16x32_bf16 v[60:63], v[108:111], v[152:155], v[60:63]\n"
    "v_cvt_pk_bf16_f32 v70, v72, v73\n"
    "v_cvt_pk_bf16_f32 v71, v74, v75\n"
    "s_waitcnt lgkmcnt(3)\n"
    "v_mfma_f32_16x16x32_bf16 v[64:67], v[108:111], v[208:211], v[64:67]\n"
    "ds_read_b128 v[244:247], v5 offset:2528\n"
    "v_mfma_f32_16x16x32_bf16 v[60:63], v[180:183], v[208:211], v[60:63]\n"
    "v_min_u32 v68, -2, v68\n"
    "v_min_u32 v69, -2, v69\n"
    "v_mfma_f32_16x16x32_bf16 v[64:67], v[180:183], v[200:203], v[64:67]\n"
    "ds_read_b128 v[216:219], v5 offset:2656\n"
    "v_mfma_f32_16x16x32_bf16 v[60:63], v[116:119], v[200:203], v[60:63]\n"
    "v_min_u32 v70, -2, v70\n"
    "v_min_u32 v71, -2, v71\n"
    "v_mfma_f32_16x16x32_bf16 v[64:67], v[116:119], v[172:175], v[64:67]\n"
    "ds_read_b128 v[192:195], v5 offset:2784\n"
    "v_mfma_f32_16x16x32_bf16 v[60:63], v[128:131], v[172:175], v[60:63]\n"
    "s_xor_b32 s43, s41, 221184\n"
    "buffer_store_dwordx4 v[68:71], v8, s[24:27], s43 offen sc1\n"
    "s_waitcnt lgkmcnt(3)\n"
    "v_mfma_f32_16x16x32_bf16 v[64:67], v[128:131], v[204:207], v[64:67]\n"
    "ds_read_b128 v[84:87], v5 offset:2912\n"
    "v_mfma_f32_16x16x32_bf16 v[60:63], v[144:147], v[204:207], v[60:63]\n"
    "v_cvt_pk_bf16_f32 v76, v76, v77\n"
    "v_cvt_pk_bf16_f32 v77, v78, v79\n"
    "v_mfma_f32_16x16x32_bf16 v[64:67], v[144:147], v[124:127], v[64:67]\n"
    "ds_read_b128 v[120:123], v5 offset:3040\n"
    "v_mfma_f32_16x16x32_bf16 v[60:63], v[88:91], v[124:127], v[60:63]\n"
    "v_cvt_pk_bf16_f32 v78, v80, v81\n"
    "v_cvt_pk_bf16_f32 v79, v82, v83\n"
    "v_mfma_f32_16x16x32_bf16 v[64:67], v[88:91], v[228:231], v[64:67]\n"
    "ds_read_b128 v[152:155], v5 offset:3168\n"
    "v_mfma_f32_16x16x32_bf16 v[60:63], v[140:143], v[228:231], v[60:63]\n"
    "v_min_u32 v76, -2, v76\n"
    "v_min_u32 v77, -2, v77\n"
    "s_waitcnt lgkmcnt(3)\n"
    "v_mfma_f32_16x16x32_bf16 v[64:67], v[140:143], v[244:247], v[64:67]\n"
    "ds_read_b128 v[208:211], v5 offset:3296\n"
    "v_mfma_f32_16x16x32_bf16 v[60:63], v[156:159], v[244:247], v[60:63]\n"
    "v_min_u32 v78, -2, v78\n"
    "v_min_u32 v79, -2, v79\n"
    "v_mfma_f32_16x16x32_bf16 v[64:67], v[156:159], v[216:219], v[64:67]\n"
    "ds_read_b128 v[200:203], v5 offset:3424\n"
    "v_mfma_f32_16x16x32_bf16 v[60:63], v[188:191], v[216:219], v[60:63]\n"
    "s_xor_b32 s43, s41, 258048\n"
    "buffer_store_dwordx4 v[76:79], v8, s[24:27], s43 offen sc1\n"
    "v_mfma_f32_16x16x32_bf16 v[64:67], v[188:191], v[192:195], v[64:67]\n"
    "ds_read_b128 v[172:175], v5 offset:3552\n"
    "v_mfma_f32_16x16x32_bf16 v[60:63], v[92:95], v[192:195], v[60:63]\n"
    "s_waitcnt lgkmcnt(3)\n"
    "v_mfma_f32_16x16x32_bf16 v[64:67], v[92:95], v[84:87], v[64:67]\n"
    "ds_read_b128 v[204:207], v5 offset:3680\n"
    "v_mfma_f32_16x16x32_bf16 v[60:63], v[224:227], v[84:87], v[60:63]\n"
    "v_mfma_f32_16x16x32_bf16 v[64:67], v[224:227], v[120:123], v[64:67]\n"
    "ds_read_b128 v[124:127], v5 offset:3808\n"
    "v_mfma_f32_16x16x32_bf16 v[60:63], v[232:235], v[120:123], v[60:63]\n"
    "v_mfma_f32_16x16x32_bf16 v[64:67], v[232:235], v[152:155], v[64:67]\n"
    "ds_read_b128 v[228:231], v5 offset:3936\n"
    "v_mfma_f32_16x16x32_bf16 v[60:63], v[240:243], v[152:155], v[60:63]\n"
    "s_waitcnt lgkmcnt(3)\n"
    "v_mfma_f32_16x16x32_bf16 v[64:67], v[240:243], v[208:211], v[64:67]\n"
    "ds_read_b128 v[244:247], v5 offset:4064\n"
    "v_mfma_f32_16x16x32_bf16 v[60:63], v[248:251], v[208:211], v[60:63]\n"
    "v_mfma_f32_16x16x32_bf16 v[64:67], v[248:251], v[200:203], v[64:67]\n"
    "ds_read_b128 v[216:219], v5 offset:4224\n"
    "v_mfma_f32_16x16x32_bf16 v[60:63], v[184:187], v[200:203], v[60:63]\n"
    "v_mfma_f32_16x16x32_bf16 v[64:67], v[184:187], v[172:175], v[64:67]\n"
    "ds_read_b128 v[192:195], v5 offset:4352\n"
    "v_mfma_f32_16x16x32_bf16 v[60:63], v[100:103], v[172:175], v[60:63]\n"
    "s_waitcnt lgkmcnt(3)\n"
    "v_mfma_f32_16x16x32_bf16 v[64:67], v[100:103], v[204:207], v[64:67]\n"
    "ds_read_b128 v[84:87], v5 offset:4480\n"
    "v_mfma_f32_16x16x32_bf16 v[60:63], v[160:163], v[204:207], v[60:63]\n"
    "v_mfma_f32_16x16x32_bf16 v[64:67], v[160:163], v[124:127], v[64:67]\n"
    "ds_read_b128 v[120:123], v5 offset:4608\n"
    "v_mfma_f32_16x16x32_bf16 v[60:63], v[176:179], v[124:127], v[60:63]\n"
    "v_mfma_f32_16x16x32_bf16 v[64:67], v[176:179], v[228:231], v[64:67]\n"
    "ds_read_b128 v[152:155], v5 offset:4736\n"
    "v_mfma_f32_16x16x32_bf16 v[60:63], v[196:199], v[228:231], v[60:63]\n"
    "s_waitcnt lgkmcnt(3)\n"
    "v_mfma_f32_16x16x32_bf16 v[64:67], v[196:199], v[244:247], v[64:67]\n"
    "ds_read_b128 v[208:211], v5 offset:4864\n"
    "v_mfma_f32_16x16x32_bf16 v[60:63], v[136:139], v[244:247], v[60:63]\n"
    "v_mfma_f32_16x16x32_bf16 v[64:67], v[136:139], v[216:219], v[64:67]\n"
    "ds_read_b128 v[200:203], v5 offset:4992\n"
    "v_mfma_f32_16x16x32_bf16 v[60:63], v[112:115], v[216:219], v[60:63]\n"
    "v_mfma_f32_16x16x32_bf16 v[64:67], v[112:115], v[192:195], v[64:67]\n"
    "ds_read_b128 v[172:175], v5 offset:5120\n"
    "v_mfma_f32_16x16x32_bf16 v[60:63], v[148:151], v[192:195], v[60:63]\n"
    "s_waitcnt lgkmcnt(3)\n"
    "v_mfma_f32_16x16x32_bf16 v[64:67], v[148:151], v[84:87], v[64:67]\n"
    "ds_read_b128 v[204:207], v5 offset:5248\n"
    "v_mfma_f32_16x16x32_bf16 v[60:63], v[164:167], v[84:87], v[60:63]\n"
    "v_mfma_f32_16x16x32_bf16 v[64:67], v[164:167], v[120:123], v[64:67]\n"
    "ds_read_b128 v[124:127], v5 offset:5376\n"
    "v_mfma_f32_16x16x32_bf16 v[60:63], v[212:215], v[120:123], v[60:63]\n"
    "ds_read_b128 v[228:231], v5 offset:1088\n"
    "v_mfma_f32_16x16x32_bf16 v[64:67], v[212:215], v[152:155], v[64:67]\n"
    "ds_read_b128 v[244:247], v5 offset:1216\n"
    "v_mfma_f32_16x16x32_bf16 v[60:63], v[236:239], v[152:155], v[60:63]\n"
    "s_waitcnt lgkmcnt(4)\n"
    "v_mfma_f32_16x16x32_bf16 v[64:67], v[236:239], v[208:211], v[64:67]\n"
    "ds_read_b128 v[216:219], v5 offset:1344\n"
    "v_mfma_f32_16x16x32_bf16 v[60:63], v[252:255], v[208:211], v[60:63]\n"
    "v_mfma_f32_16x16x32_bf16 v[64:67], v[252:255], v[200:203], v[64:67]\n"
    "ds_read_b128 v[192:195], v5 offset:1472\n"
    "v_mfma_f32_16x16x32_bf16 v[60:63], v[132:135], v[200:203], v[60:63]\n"
    "v_mfma_f32_16x16x32_bf16 v[64:67], v[132:135], v[172:175], v[64:67]\n"
    "ds_read_b128 v[84:87], v5 offset:1600\n"
    "v_mfma_f32_16x16x32_bf16 v[60:63], v[168:171], v[172:175], v[60:63]\n"
    "s_waitcnt lgkmcnt(3)\n"
    "v_mfma_f32_16x16x32_bf16 v[64:67], v[168:171], v[204:207], v[64:67]\n"
    "ds_read_b128 v[120:123], v5 offset:1728\n"
    "v_mfma_f32_16x16x32_bf16 v[60:63], v[220:223], v[204:207], v[60:63]\n"
    "v_mfma_f32_16x16x32_bf16 v[64:67], v[220:223], v[124:127], v[64:67]\n"
    "ds_read_b128 v[152:155], v5 offset:1856\n"
    "v_mfma_f32_16x16x32_bf16 v[52:55], v[96:99], v[228:231], v[52:55]\n"
    "v_mfma_f32_16x16x32_bf16 v[56:59], v[96:99], v[244:247], v[56:59]\n"
    "ds_read_b128 v[208:211], v5 offset:1984\n"
    "v_mfma_f32_16x16x32_bf16 v[52:55], v[104:107], v[244:247], v[52:55]\n"
    "s_nop 1\n"
    "v_cvt_pk_bf16_f32 v60, v60, v61\n"
    "v_cvt_pk_bf16_f32 v61, v62, v63\n"
    "v_cvt_pk_bf16_f32 v62, v64, v65\n"
    "v_cvt_pk_bf16_f32 v63, v66, v67\n"
    "s_waitcnt lgkmcnt(3)\n"
    "v_mfma_f32_16x16x32_bf16 v[56:59], v[104:107], v[216:219], v[56:59]\n"
    "ds_read_b128 v[200:203], v5 offset:2144\n"
    "v_min_u32 v60, -2, v60\n"
    "v_min_u32 v61, -2, v61\n"
    "v_mfma_f32_16x16x32_bf16 v[52:55], v[108:111], v[216:219], v[52:55]\n"
    "v_min_u32 v62, -2, v62\n"
    "v_min_u32 v63, -2, v63\n"
    "s_xor_b32 s43, s41, 184320\n"
    "buffer_store_dwordx4 v[60:63], v8, s[24:27], s43 offen sc1\n"
    "v_mfma_f32_16x16x32_bf16 v[56:59], v[108:111], v[192:195], v[56:59]\n"
    "ds_read_b128 v[172:175], v5 offset:2272\n"
    "v_mfma_f32_16x16x32_bf16 v[52:55], v[180:183], v[192:195], v[52:55]\n"
    "v_mfma_f32_16x16x32_bf16 v[56:59], v[180:183], v[84:87], v[56:59]\n"
    "ds_read_b128 v[204:207], v5 offset:2400\n"
    "v_mfma_f32_16x16x32_bf16 v[52:55], v[116:119], v[84:87], v[52:55]\n"
    "s_waitcnt lgkmcnt(3)\n"
    "v_mfma_f32_16x16x32_bf16 v[56:59], v[116:119], v[120:123], v[56:59]\n"
    "ds_read_b128 v[124:127], v5 offset:2528\n"
    "v_mfma_f32_16x16x32_bf16 v[52:55], v[128:131], v[120:123], v[52:55]\n"
    "v_mfma_f32_16x16x32_bf16 v[56:59], v[128:131], v[152:155], v[56:59]\n"
    "ds_read_b128 v[228:231], v5 offset:2656\n"
    "v_mfma_f32_16x16x32_bf16 v[52:55], v[144:147], v[152:155], v[52:55]\n"
    "v_mfma_f32_16x16x32_bf16 v[56:59], v[144:147], v[208:211], v[56:59]\n"
    "ds_read_b128 v[96:99], v5 offset:2784\n"
    "v_mfma_f32_16x16x32_bf16 v[52:55], v[88:91], v[208:211], v[52:55]\n"
    "s_waitcnt lgkmcnt(3)\n"
    "v_mfma_f32_16x16x32_bf16 v[56:59], v[88:91], v[200:203], v[56:59]\n"
    "ds_read_b128 v[244:247], v5 offset:2912\n"
    "v_mfma_f32_16x16x32_bf16 v[52:55], v[140:143], v[200:203], v[52:55]\n"
    "v_mfma_f32_16x16x32_bf16 v[56:59], v[140:143], v[172:175], v[56:59]\n"
    "ds_read_b128 v[104:107], v5 offset:3040\n"
    "v_mfma_f32_16x16x32_bf16 v[52:55], v[156:159], v[172:175], v[52:55]\n"
    "v_mfma_f32_16x16x32_bf16 v[56:59], v[156:159], v[204:207], v[56:59]\n"
    "ds_read_b128 v[216:219], v5 offset:3168\n"
    "v_mfma_f32_16x16x32_bf16 v[52:55], v[188:191], v[204:207], v[52:55]\n"
    "s_waitcnt lgkmcnt(3)\n"
    "v_mfma_f32_16x16x32_bf16 v[56:59], v[188:191], v[124:127], v[56:59]\n"
    "ds_read_b128 v[108:111], v5 offset:3296\n"
    "v_mfma_f32_16x16x32_bf16 v[52:55], v[92:95], v[124:127], v[52:55]\n"
    "v_mfma_f32_16x16x32_bf16 v[56:59], v[92:95], v[228:231], v[56:59]\n"
    "ds_read_b128 v[192:195], v5 offset:3424\n"
    "v_mfma_f32_16x16x32_bf16 v[52:55], v[224:227], v[228:231], v[52:55]\n"
    "v_mfma_f32_16x16x32_bf16 v[56:59], v[224:227], v[96:99], v[56:59]\n"
    "ds_read_b128 v[180:183], v5 offset:3552\n"
    "v_mfma_f32_16x16x32_bf16 v[52:55], v[232:235], v[96:99], v[52:55]\n"
    "s_waitcnt lgkmcnt(3)\n"
    "v_mfma_f32_16x16x32_bf16 v[56:59], v[232:235], v[244:247], v[56:59]\n"
    "ds_read_b128 v[84:87], v5 offset:3680\n"
    "v_mfma_f32_16x16x32_bf16 v[52:55], v[240:243], v[244:247], v[52:55]\n"
    "v_mfma_f32_16x16x32_bf16 v[56:59], v[240:243], v[104:107], v[56:59]\n"
    "ds_read_b128 v[116:119], v5 offset:3808\n"
    "v_mfma_f32_16x16x32_bf16 v[52:55], v[248:251], v[104:107], v[52:55]\n"
    "v_mfma_f32_16x16x32_bf16 v[56:59], v[248:251], v[216:219], v[56:59]\n"
    "ds_read_b128 v[120:123], v5 offset:3936\n"
    "v_mfma_f32_16x16x32_bf16 v[52:55], v[184:187], v[216:219], v[52:55]\n"
    "s_waitcnt lgkmcnt(3)\n"
    "v_mfma_f32_16x16x32_bf16 v[56:59], v[184:187], v[108:111], v[56:59]\n"
    "ds_read_b128 v[128:131], v5 offset:4064\n"
    "v_mfma_f32_16x16x32_bf16 v[52:55], v[100:103], v[108:111], v[52:55]\n"
    "v_mfma_f32_16x16x32_bf16 v[56:59], v[100:103], v[192:195], v[56:59]\n"
    "ds_read_b128 v[152:155], v5 offset:4224\n"
    "v_mfma_f32_16x16x32_bf16 v[52:55], v[160:163], v[192:195], v[52:55]\n"
    "v_mfma_f32_16x16x32_bf16 v[56:59], v[160:163], v[180:183], v[56:59]\n"
    "ds_read_b128 v[144:147], v5 offset:4352\n"
    "v_mfma_f32_16x16x32_bf16 v[52:55], v[176:179], v[180:183], v[52:55]\n"
    "s_waitcnt lgkmcnt(3)\n"
    "v_mfma_f32_16x16x32_bf16 v[56:59], v[176:179], v[84:87], v[56:59]\n"
    "ds_read_b128 v[208:211], v5 offset:4480\n"
    "v_mfma_f32_16x16x32_bf16 v[52:55], v[196:199], v[84:87], v[52:55]\n"
    "v_mfma_f32_16x16x32_bf16 v[56:59], v[196:199], v[116:119], v[56:59]\n"
    "ds_read_b128 v[88:91], v5 offset:4608\n"
    "v_mfma_f32_16x16x32_bf16 v[52:55], v[136:139], v[116:119], v[52:55]\n"
    "v_mfma_f32_16x16x32_bf16 v[56:59], v[136:139], v[120:123], v[56:59]\n"
    "ds_read_b128 v[200:203], v5 offset:4736\n"
    "v_mfma_f32_16x16x32_bf16 v[52:55], v[112:115], v[120:123], v[52:55]\n"
    "s_waitcnt lgkmcnt(3)\n"
    "v_mfma_f32_16x16x32_bf16 v[56:59], v[112:115], v[128:131], v[56:59]\n"
    "ds_read_b128 v[140:143], v5 offset:4864\n"
    "v_mfma_f32_16x16x32_bf16 v[52:55], v[148:151], v[128:131], v[52:55]\n"
    "v_mfma_f32_16x16x32_bf16 v[56:59], v[148:151], v[152:155], v[56:59]\n"
    "ds_read_b128 v[172:175], v5 offset:4992\n"
    "v_mfma_f32_16x16x32_bf16 v[52:55], v[164:167], v[152:155], v[52:55]\n"
    "v_mfma_f32_16x16x32_bf16 v[56:59], v[164:167], v[144:147], v[56:59]\n"
    "ds_read_b128 v[156:159], v5 offset:5120\n"
    "v_mfma_f32_16x16x32_bf16 v[52:55], v[212:215], v[144:147], v[52:55]\n"
    "s_waitcnt lgkmcnt(3)\n"
    "v_mfma_f32_16x16x32_bf16 v[56:59], v[212:215], v[208:211], v[56:59]\n"
    "v_mfma_f32_16x16x32_bf16 v[52:55], v[236:239], v[208:211], v[52:55]\n"
    "v_mfma_f32_16x16x32_bf16 v[56:59], v[236:239], v[88:91], v[56:59]\n"
    "v_mfma_f32_16x16x32_bf16 v[52:55], v[252:255], v[88:91], v[52:55]\n"
    "v_mfma_f32_16x16x32_bf16 v[56:59], v[252:255], v[200:203], v[56:59]\n"
    "v_mfma_f32_16x16x32_bf16 v[52:55], v[132:135], v[200:203], v[52:55]\n"
    "s_waitcnt lgkmcnt(0)\n"
    "v_mfma_f32_16x16x32_bf16 v[56:59], v[132:135], v[140:143], v[56:59]\n"
    "v_mfma_f32_16x16x32_bf16 v[52:55], v[168:171], v[140:143], v[52:55]\n"
    "v_mfma_f32_16x16x32_bf16 v[56:59], v[168:171], v[172:175], v[56:59]\n"
    "v_mfma_f32_16x16x32_bf16 v[52:55], v[220:223], v[172:175], v[52:55]\n"
    "v_mfma_f32_16x16x32_bf16 v[56:59], v[220:223], v[156:159], v[56:59]\n"
    "s_nop 6\n"
    "ds_write_b128 v16, v[52:55]\n"
    "ds_write_b128 v16, v[56:59] offset:1088\n"
    "s_waitcnt lgkmcnt(0)\n"
    "s_barrier\n"
    "ds_read_b128 v[96:99], v14\n"
    "ds_read_b128 v[100:103], v14 offset:2176\n"
    "ds_read_b128 v[104:107], v14 offset:4352\n"
    "s_waitcnt lgkmcnt(1)\n"
    "v_fmac_f32 v100, s56, v96\n"
    "v_fmac_f32 v101, s56, v97\n"
    "v_fmac_f32 v102, s56, v98\n"
    "v_fmac_f32 v103, s56, v99\n"
    "v_exp_f32 v100, v100\n"
    "v_exp_f32 v101, v101\n"
    "v_exp_f32 v102, v102\n"
    "v_exp_f32 v103, v103\n"
    "v_add_f32 v100, 1.0, v100\n"
    "v_add_f32 v101, 1.0, v101\n"
    "v_add_f32 v102, 1.0, v102\n"
    "v_add_f32 v103, 1.0, v103\n"
    "v_rcp_f32 v100, v100\n"
    "v_rcp_f32 v101, v101\n"
    "v_rcp_f32 v102, v102\n"
    "v_rcp_f32 v103, v103\n"
    "v_fma_f32 v100, v100, s58, v17\n"
    "v_fma_f32 v101, v101, s58, v17\n"
    "v_fma_f32 v102, v102, s58, v17\n"
    "v_fma_f32 v103, v103, s58, v17\n"
    "s_waitcnt lgkmcnt(0)\n"
    "v_mul_f32 v100, v100, v104\n"
    "v_mul_f32 v101, v101, v105\n"
    "v_mul_f32 v102, v102, v106\n"
    "v_mul_f32 v103, v103, v107\n"
    "v_cvt_pk_bf16_f32 v96, v100, v101\n"
    "v_cvt_pk_bf16_f32 v97, v102, v103\n"
    "buffer_store_dwordx2 v[96:97], v15, s[28:31], 0 offen\n"
    "s_endpgm\n"
    ".Lt5_1%=:\n"
    ".Lpad11%=:\n"
    "s_cbranch_execz .Lpad11r%=\n"
    "s_waitcnt lgkmcnt(1)\n"
    "v_mfma_f32_16x16x32_bf16 v[80:83], v[252:255], v[228:231], v[80:83]\n"
    "ds_read_b128 v[192:195], v5 offset:5888\n"
    "v_mfma_f32_16x16x32_bf16 v[68:71], v[132:135], v[204:207], v[68:71]\n"
    "ds_read_b128 v[84:87], v5 offset:1088\n"
    "v_mfma_f32_16x16x32_bf16 v[72:75], v[132:135], v[124:127], v[72:75]\n"
    "ds_read_b128 v[120:123], v5 offset:1216\n"
    "v_mfma_f32_16x16x32_bf16 v[76:79], v[132:135], v[228:231], v[76:79]\n"
    "v_mfma_f32_16x16x32_bf16 v[80:83], v[132:135], v[244:247], v[80:83]\n"
    "ds_read_b128 v[152:155], v5 offset:1344\n"
    "v_mfma_f32_16x16x32_bf16 v[68:71], v[168:171], v[124:127], v[68:71]\n"
    "v_mfma_f32_16x16x32_bf16 v[72:75], v[168:171], v[228:231], v[72:75]\n"
    "ds_read_b128 v[208:211], v5 offset:1472\n"
    "v_mfma_f32_16x16x32_bf16 v[76:79], v[168:171], v[244:247], v[76:79]\n"
    "s_waitcnt lgkmcnt(4)\n"
    "v_mfma_f32_16x16x32_bf16 v[80:83], v[168:171], v[216:219], v[80:83]\n"
    "ds_read_b128 v[200:203], v5 offset:1600\n"
    "v_mfma_f32_16x16x32_bf16 v[68:71], v[220:223], v[228:231], v[68:71]\n"
    "v_mfma_f32_16x16x32_bf16 v[72:75], v[220:223], v[244:247], v[72:75]\n"
    "ds_read_b128 v[172:175], v5 offset:1728\n"
    "v_mfma_f32_16x16x32_bf16 v[76:79], v[220:223], v[216:219], v[76:79]\n"
    "v_mfma_f32_16x16x32_bf16 v[80:83], v[220:223], v[192:195], v[80:83]\n"
    "ds_read_b128 v[204:207], v5 offset:1856\n"
    "s_waitcnt lgkmcnt(4)\n"
    "v_mfma_f32_16x16x32_bf16 v[52:55], v[96:99], v[84:87], v[52:55]\n"
    "v_mfma_f32_16x16x32_bf16 v[56:59], v[96:99], v[120:123], v[56:59]\n"
    "ds_read_b128 v[124:127], v5 offset:1984\n"
    "v_mfma_f32_16x16x32_bf16 v[52:55], v[104:107], v[120:123], v[52:55]\n"
    "v_cvt_pk_bf16_f32 v68, v68, v69\n"
    "v_cvt_pk_bf16_f32 v69, v70, v71\n"
    "v_mfma_f32_16x16x32_bf16 v[56:59], v[104:107], v[152:155], v[56:59]\n"
    "ds_read_b128 v[228:231], v5 offset:2144\n"
    "v_mfma_f32_16x16x32_bf16 v[52:55], v[108:111], v[152:155], v[52:55]\n"
    "v_cvt_pk_bf16_f32 v70, v72, v73\n"
    "v_cvt_pk_bf16_f32 v71, v74, v75\n"
    "s_waitcnt lgkmcnt(3)\n"
    "v_mfma_f32_16x16x32_bf16 v[56:59], v[108:111], v[208:211], v[56:59]\n"
    "ds_read_b128 v[244:247], v5 offset:2272\n"
    "v_mfma_f32_16x16x32_bf16 v[52:55], v[180:183], v[208:211], v[52:55]\n"
    "v_min_u32 v68, -2, v68\n"
    "v_min_u32 v69, -2, v69\n"
    "v_mfma_f32_16x16x32_bf16 v[56:59], v[180:183], v[200:203], v[56:59]\n"
    "ds_read_b128 v[216:219], v5 offset:2400\n"
    "v_mfma_f32_16x16x32_bf16 v[52:55], v[116:119], v[200:203], v[52:55]\n"
    "v_min_u32 v70, -2, v70\n"
    "v_min_u32 v71, -2, v71\n"
    "v_mfma_f32_16x16x32_bf16 v[56:59], v[116:119], v[172:175], v[56:59]\n"
    "ds_read_b128 v[192:195], v5 offset:2528\n"
    "v_mfma_f32_16x16x32_bf16 v[52:55], v[128:131], v[172:175], v[52:55]\n"
    "s_xor_b32 s43, s41, 221184\n"
    "buffer_store_dwordx4 v[68:71], v8, s[24:27], s43 offen sc1\n"
    "s_waitcnt lgkmcnt(3)\n"
    "v_mfma_f32_16x16x32_bf16 v[56:59], v[128:131], v[204:207], v[56:59]\n"
    "ds_read_b128 v[84:87], v5 offset:2656\n"
    "v_mfma_f32_16x16x32_bf16 v[52:55], v[144:147], v[204:207], v[52:55]\n"
    "v_cvt_pk_bf16_f32 v76, v76, v77\n"
    "v_cvt_pk_bf16_f32 v77, v78, v79\n"
    "v_mfma_f32_16x16x32_bf16 v[56:59], v[144:147], v[124:127], v[56:59]\n"
    "ds_read_b128 v[120:123], v5 offset:2784\n"
    "v_mfma_f32_16x16x32_bf16 v[52:55], v[88:91], v[124:127], v[52:55]\n"
    "v_cvt_pk_bf16_f32 v78, v80, v81\n"
    "v_cvt_pk_bf16_f32 v79, v82, v83\n"
    "v_mfma_f32_16x16x32_bf16 v[56:59], v[88:91], v[228:231], v[56:59]\n"
    "ds_read_b128 v[152:155], v5 offset:2912\n"
    "v_mfma_f32_16x16x32_bf16 v[52:55], v[140:143], v[228:231], v[52:55]\n"
    "v_min_u32 v76, -2, v76\n"
    "v_min_u32 v77, -2, v77\n"
    "s_waitcnt lgkmcnt(3)\n"
    "v_mfma_f32_16x16x32_bf16 v[56:59], v[140:143], v[244:247], v[56:59]\n"
    "ds_read_b128 v[208:211], v5 offset:3040\n"
    "v_mfma_f32_16x16x32_bf16 v[52:55], v[156:159], v[244:247], v[52:55]\n"
    "v_min_u32 v78, -2, v78\n"
    "v_min_u32 v79, -2, v79\n"
    "v_mfma_f32_16x16x32_bf16 v[56:59], v[156:159], v[216:219], v[56:59]\n"
    "ds_read_b128 v[200:203], v5 offset:3168\n"
    "v_mfma_f32_16x16x32_bf16 v[52:55], v[188:191], v[216:219], v[52:55]\n"
    "s_xor_b32 s43, s41, 258048\n"
    "buffer_store_dwordx4 v[76:79], v8, s[24:27], s43 offen sc1\n"
    "v_mfma_f32_16x16x32_bf16 v[56:59], v[188:191], v[192:195], v[56:59]\n"
    "ds_read_b128 v[172:175], v5 offset:3296\n"
    "v_mfma_f32_16x16x32_bf16 v[52:55], v[92:95], v[192:195], v[52:55]\n"
    "s_waitcnt lgkmcnt(3)\n"
    "v_mfma_f32_16x16x32_bf16 v[56:59], v[92:95], v[84:87], v[56:59]\n"
    "ds_read_b128 v[204:207], v5 offset:3424\n"
    "v_mfma_f32_16x16x32_bf16 v[52:55], v[224:227], v[84:87], v[52:55]\n"
    "v_mfma_f32_16x16x32_bf16 v[56:59], v[224:227], v[120:123], v[56:59]\n"
    "ds_read_b128 v[124:127], v5 offset:3552\n"
    "v_mfma_f32_16x16x32_bf16 v[52:55], v[232:235], v[120:123], v[52:55]\n"
    "v_mfma_f32_16x16x32_bf16 v[56:59], v[232:235], v[152:155], v[56:59]\n"
    "ds_read_b128 v[228:231], v5 offset:3680\n"
    "v_mfma_f32_16x16x32_bf16 v[52:55], v[240:243], v[152:155], v[52:55]\n"
    "s_waitcnt lgkmcnt(3)\n"
    "v_mfma_f32_16x16x32_bf16 v[56:59], v[240:243], v[208:211], v[56:59]\n"
    "ds_read_b128 v[244:247], v5 offset:3808\n"
    "v_mfma_f32_16x16x32_bf16 v[52:55], v[248:251], v[208:211], v[52:55]\n"
    "v_mfma_f32_16x16x32_bf16 v[56:59], v[248:251], v[200:203], v[56:59]\n"
    "ds_read_b128 v[216:219], v5 offset:3936\n"
    "v_mfma_f32_16x16x32_bf16 v[52:55], v[184:187], v[200:203], v[52:55]\n"
    "v_mfma_f32_16x16x32_bf16 v[56:59], v[184:187], v[172:175], v[56:59]\n"
    "ds_read_b128 v[192:195], v5 offset:4064\n"
    "v_mfma_f32_16x16x32_bf16 v[52:55], v[100:103], v[172:175], v[52:55]\n"
    "s_waitcnt lgkmcnt(3)\n"
    "v_mfma_f32_16x16x32_bf16 v[56:59], v[100:103], v[204:207], v[56:59]\n"
    "ds_read_b128 v[84:87], v5 offset:4224\n"
    "v_mfma_f32_16x16x32_bf16 v[52:55], v[160:163], v[204:207], v[52:55]\n"
    "v_mfma_f32_16x16x32_bf16 v[56:59], v[160:163], v[124:127], v[56:59]\n"
    "ds_read_b128 v[120:123], v5 offset:4352\n"
    "v_mfma_f32_16x16x32_bf16 v[52:55], v[176:179], v[124:127], v[52:55]\n"
    "v_mfma_f32_16x16x32_bf16 v[56:59], v[176:179], v[228:231], v[56:59]\n"
    "ds_read_b128 v[152:155], v5 offset:4480\n"
    "v_mfma_f32_16x16x32_bf16 v[52:55], v[196:199], v[228:231], v[52:55]\n"
    "s_waitcnt lgkmcnt(3)\n"
    "v_mfma_f32_16x16x32_bf16 v[56:59], v[196:199], v[244:247], v[56:59]\n"
    "ds_read_b128 v[208:211], v5 offset:4608\n"
    "v_mfma_f32_16x16x32_bf16 v[52:55], v[136:139], v[244:247], v[52:55]\n"
    "v_mfma_f32_16x16x32_bf16 v[56:59], v[136:139], v[216:219], v[56:59]\n"
    "ds_read_b128 v[200:203], v5 offset:4736\n"
    "v_mfma_f32_16x16x32_bf16 v[52:55], v[112:115], v[216:219], v[52:55]\n"
    "v_mfma_f32_16x16x32_bf16 v[56:59], v[112:115], v[192:195], v[56:59]\n"
    "ds_read_b128 v[172:175], v5 offset:4864\n"
    "v_mfma_f32_16x16x32_bf16 v[52:55], v[148:151], v[192:195], v[52:55]\n"
    "s_waitcnt lgkmcnt(3)\n"
    "v_mfma_f32_16x16x32_bf16 v[56:59], v[148:151], v[84:87], v[56:59]\n"
    "ds_read_b128 v[204:207], v5 offset:4992\n"
    "v_mfma_f32_16x16x32_bf16 v[52:55], v[164:167], v[84:87], v[52:55]\n"
    "v_mfma_f32_16x16x32_bf16 v[56:59], v[164:167], v[120:123], v[56:59]\n"
    "ds_read_b128 v[124:127], v5 offset:5120\n"
    "v_mfma_f32_16x16x32_bf16 v[52:55], v[212:215], v[120:123], v[52:55]\n"
    "ds_read_b128 v[228:231], v5 offset:1344\n"
    "v_mfma_f32_16x16x32_bf16 v[56:59], v[212:215], v[152:155], v[56:59]\n"
    "ds_read_b128 v[244:247], v5 offset:1472\n"
    "v_mfma_f32_16x16x32_bf16 v[52:55], v[236:239], v[152:155], v[52:55]\n"
    "s_waitcnt lgkmcnt(4)\n"
    "v_mfma_f32_16x16x32_bf16 v[56:59], v[236:239], v[208:211], v[56:59]\n"
    "ds_read_b128 v[216:219], v5 offset:1600\n"
    "v_mfma_f32_16x16x32_bf16 v[52:55], v[252:255], v[208:211], v[52:55]\n"
    "v_mfma_f32_16x16x32_bf16 v[56:59], v[252:255], v[200:203], v[56:59]\n"
    "ds_read_b128 v[192:195], v5 offset:1728\n"
    "v_mfma_f32_16x16x32_bf16 v[52:55], v[132:135], v[200:203], v[52:55]\n"
    "v_mfma_f32_16x16x32_bf16 v[56:59], v[132:135], v[172:175], v[56:59]\n"
    "ds_read_b128 v[84:87], v5 offset:1856\n"
    "v_mfma_f32_16x16x32_bf16 v[52:55], v[168:171], v[172:175], v[52:55]\n"
    "s_waitcnt lgkmcnt(3)\n"
    "v_mfma_f32_16x16x32_bf16 v[56:59], v[168:171], v[204:207], v[56:59]\n"
    "ds_read_b128 v[120:123], v5 offset:1984\n"
    "v_mfma_f32_16x16x32_bf16 v[52:55], v[220:223], v[204:207], v[52:55]\n"
    "v_mfma_f32_16x16x32_bf16 v[56:59], v[220:223], v[124:127], v[56:59]\n"
    "ds_read_b128 v[152:155], v5 offset:2144\n"
    "v_mfma_f32_16x16x32_bf16 v[60:63], v[96:99], v[228:231], v[60:63]\n"
    "v_mfma_f32_16x16x32_bf16 v[64:67], v[96:99], v[244:247], v[64:67]\n"
    "ds_read_b128 v[208:211], v5 offset:2272\n"
    "v_mfma_f32_16x16x32_bf16 v[60:63], v[104:107], v[244:247], v[60:63]\n"
    "s_nop 1\n"
    "v_cvt_pk_bf16_f32 v52, v52, v53\n"
    "v_cvt_pk_bf16_f32 v53, v54, v55\n"
    "v_cvt_pk_bf16_f32 v54, v56, v57\n"
    "v_cvt_pk_bf16_f32 v55, v58, v59\n"
    "s_waitcnt lgkmcnt(3)\n"
    "v_mfma_f32_16x16x32_bf16 v[64:67], v[104:107], v[216:219], v[64:67]\n"
    "ds_read_b128 v[200:203], v5 offset:2400\n"
    "v_min_u32 v52, -2, v52\n"
    "v_min_u32 v53, -2, v53\n"
    "v_mfma_f32_16x16x32_bf16 v[60:63], v[108:111], v[216:219], v[60:63]\n"
    "v_min_u32 v54, -2, v54\n"
    "v_min_u32 v55, -2, v55\n"
    "s_xor_b32 s43, s41, 147456\n"
    "buffer_store_dwordx4 v[52:55], v8, s[24:27], s43 offen sc1\n"
    "v_mfma_f32_16x16x32_bf16 v[64:67], v[108:111], v[192:195], v[64:67]\n"
    "ds_read_b128 v[172:175], v5 offset:2528\n"
    "v_mfma_f32_16x16x32_bf16 v[60:63], v[180:183], v[192:195], v[60:63]\n"
    "v_mfma_f32_16x16x32_bf16 v[64:67], v[180:183], v[84:87], v[64:67]\n"
    "ds_read_b128 v[204:207], v5 offset:2656\n"
    "v_mfma_f32_16x16x32_bf16 v[60:63], v[116:119], v[84:87], v[60:63]\n"
    "s_waitcnt lgkmcnt(3)\n"
    "v_mfma_f32_16x16x32_bf16 v[64:67], v[116:119], v[120:123], v[64:67]\n"
    "ds_read_b128 v[124:127], v5 offset:2784\n"
    "v_mfma_f32_16x16x32_bf16 v[60:63], v[128:131], v[120:123], v[60:63]\n"
    "v_mfma_f32_16x16x32_bf16 v[64:67], v[128:131], v[152:155], v[64:67]\n"
    "ds_read_b128 v[228:231], v5 offset:2912\n"
    "v_mfma_f32_16x16x32_bf16 v[60:63], v[144:147], v[152:155], v[60:63]\n"
    "v_mfma_f32_16x16x32_bf16 v[64:67], v[144:147], v[208:211], v[64:67]\n"
    "ds_read_b128 v[96:99], v5 offset:3040\n"
    "v_mfma_f32_16x16x32_bf16 v[60:63], v[88:91], v[208:211], v[60:63]\n"
    "s_waitcnt lgkmcnt(3)\n"
    "v_mfma_f32_16x16x32_bf16 v[64:67], v[88:91], v[200:203], v[64:67]\n"
    "ds_read_b128 v[244:247], v5 offset:3168\n"
    "v_mfma_f32_16x16x32_bf16 v[60:63], v[140:143], v[200:203], v[60:63]\n"
    "v_mfma_f32_16x16x32_bf16 v[64:67], v[140:143], v[172:175], v[64:67]\n"
    "ds_read_b128 v[104:107], v5 offset:3296\n"
    "v_mfma_f32_16x16x32_bf16 v[60:63], v[156:159], v[172:175], v[60:63]\n"
    "v_mfma_f32_16x16x32_bf16 v[64:67], v[156:159], v[204:207], v[64:67]\n"
    "ds_read_b128 v[216:219], v5 offset:3424\n"
    "v_mfma_f32_16x16x32_bf16 v[60:63], v[188:191], v[204:207], v[60:63]\n"
    "s_waitcnt lgkmcnt(3)\n"
    "v_mfma_f32_16x16x32_bf16 v[64:67], v[188:191], v[124:127], v[64:67]\n"
    "ds_read_b128 v[108:111], v5 offset:3552\n"
    "v_mfma_f32_16x16x32_bf16 v[60:63], v[92:95], v[124:127], v[60:63]\n"
    "v_mfma_f32_16x16x32_bf16 v[64:67], v[92:95], v[228:231], v[64:67]\n"
    "ds_read_b128 v[192:195], v5 offset:3680\n"
    "v_mfma_f32_16x16x32_bf16 v[60:63], v[224:227], v[228:231], v[60:63]\n"
    "v_mfma_f32_16x16x32_bf16 v[64:67], v[224:227], v[96:99], v[64:67]\n"
    "ds_read_b128 v[180:183], v5 offset:3808\n"
    ".Lpad12%=:\n"
    "s_cbranch_execz .Lpad12r%=\n"
    "v_mfma_f32_16x16x32_bf16 v[60:63], v[232:235], v[96:99], v[60:63]\n"
    "s_waitcnt lgkmcnt(3)\n"
    "v_mfma_f32_16x16x32_bf16 v[64:67], v[232:235], v[244:247], v[64:67]\n"
    "ds_read_b128 v[84:87], v5 offset:3936\n"
    "v_mfma_f32_16x16x32_bf16 v[60:63], v[240:243], v[244:247], v[60:63]\n"
    "v_mfma_f32_16x16x32_bf16 v[64:67], v[240:243], v[104:107], v[64:67]\n"
    "ds_read_b128 v[116:119], v5 offset:4064\n"
    "v_mfma_f32_16x16x32_bf16 v[60:63], v[248:251], v[104:107], v[60:63]\n"
    "v_mfma_f32_16x16x32_bf16 v[64:67], v[248:251], v[216:219], v[64:67]\n"
    "ds_read_b128 v[120:123], v5 offset:4224\n"
    "v_mfma_f32_16x16x32_bf16 v[60:63], v[184:187], v[216:219], v[60:63]\n"
    "s_waitcnt lgkmcnt(3)\n"
    "v_mfma_f32_16x16x32_bf16 v[64:67], v[184:187], v[108:111], v[64:67]\n"
    "ds_read_b128 v[128:131], v5 offset:4352\n"
    "v_mfma_f32_16x16x32_bf16 v[60:63], v[100:103], v[108:111], v[60:63]\n"
    "v_mfma_f32_16x16x32_bf16 v[64:67], v[100:103], v[192:195], v[64:67]\n"
    "ds_read_b128 v[152:155], v5 offset:4480\n"
    "v_mfma_f32_16x16x32_bf16 v[60:63], v[160:163], v[192:195], v[60:63]\n"
    "v_mfma_f32_16x16x32_bf16 v[64:67], v[160:163], v[180:183], v[64:67]\n"
    "ds_read_b128 v[144:147], v5 offset:4608\n"
    "v_mfma_f32_16x16x32_bf16 v[60:63], v[176:179], v[180:183], v[60:63]\n"
    "s_waitcnt lgkmcnt(3)\n"
    "v_mfma_f32_16x16x32_bf16 v[64:67], v[176:179], v[84:87], v[64:67]\n"
    "ds_read_b128 v[208:211], v5 offset:4736\n"
    "v_mfma_f32_16x16x32_bf16 v[60:63], v[196:199], v[84:87], v[60:63]\n"
    "v_mfma_f32_16x16x32_bf16 v[64:67], v[196:199], v[116:119], v[64:67]\n"
    "ds_read_b128 v[88:91], v5 offset:4864\n"
    "v_mfma_f32_16x16x32_bf16 v[60:63], v[136:139], v[116:119], v[60:63]\n"
    "v_mfma_f32_16x16x32_bf16 v[64:67], v[136:139], v[120:123], v[64:67]\n"
    "ds_read_b128 v[200:203], v5 offset:4992\n"
    "v_mfma_f32_16x16x32_bf16 v[60:63], v[112:115], v[120:123], v[60:63]\n"
    "s_waitcnt lgkmcnt(3)\n"
    "v_mfma_f32_16x16x32_bf16 v[64:67], v[112:115], v[128:131], v[64:67]\n"
    "ds_read_b128 v[140:143], v5 offset:5120\n"
    "v_mfma_f32_16x16x32_bf16 v[60:63], v[148:151], v[128:131], v[60:63]\n"
    "v_mfma_f32_16x16x32_bf16 v[64:67], v[148:151], v[152:155], v[64:67]\n"
    "ds_read_b128 v[172:175], v5 offset:5248\n"
    "v_mfma_f32_16x16x32_bf16 v[60:63], v[164:167], v[152:155], v[60:63]\n"
    "v_mfma_f32_16x16x32_bf16 v[64:67], v[164:167], v[144:147], v[64:67]\n"
    "ds_read_b128 v[156:159], v5 offset:5376\n"
    "v_mfma_f32_16x16x32_bf16 v[60:63], v[212:215], v[144:147], v[60:63]\n"
    "s_waitcnt lgkmcnt(3)\n"
    "v_mfma_f32_16x16x32_bf16 v[64:67], v[212:215], v[208:211], v[64:67]\n"
    "v_mfma_f32_16x16x32_bf16 v[60:63], v[236:239], v[208:211], v[60:63]\n"
    "v_mfma_f32_16x16x32_bf16 v[64:67], v[236:239], v[88:91], v[64:67]\n"
    "v_mfma_f32_16x16x32_bf16 v[60:63], v[252:255], v[88:91], v[60:63]\n"
    "v_mfma_f32_16x16x32_bf16 v[64:67], v[252:255], v[200:203], v[64:67]\n"
    "v_mfma_f32_16x16x32_bf16 v[60:63], v[132:135], v[200:203], v[60:63]\n"
    "s_waitcnt lgkmcnt(0)\n"
    "v_mfma_f32_16x16x32_bf16 v[64:67], v[132:135], v[140:143], v[64:67]\n"
    "v_mfma_f32_16x16x32_bf16 v[60:63], v[168:171], v[140:143], v[60:63]\n"
    "v_mfma_f32_16x16x32_bf16 v[64:67], v[168:171], v[172:175], v[64:67]\n"
    "v_mfma_f32_16x16x32_bf16 v[60:63], v[220:223], v[172:175], v[60:63]\n"
    "v_mfma_f32_16x16x32_bf16 v[64:67], v[220:223], v[156:159], v[64:67]\n"
    "s_nop 6\n"
    "ds_write_b128 v16, v[60:63]\n"
    "ds_write_b128 v16, v[64:67] offset:1088\n"
    "s_waitcnt lgkmcnt(0)\n"
    "s_barrier\n"
    "ds_read_b128 v[96:99], v14\n"
    "ds_read_b128 v[100:103], v14 offset:2176\n"
    "ds_read_b128 v[104:107], v14 offset:4352\n"
    "s_waitcnt lgkmcnt(1)\n"
    "v_fmac_f32 v100, s56, v96\n"
    "v_fmac_f32 v101, s56, v97\n"
    "v_fmac_f32 v102, s56, v98\n"
    "v_fmac_f32 v103, s56, v99\n"
    "v_exp_f32 v100, v100\n"
    "v_exp_f32 v101, v101\n"
    "v_exp_f32 v102, v102\n"
    "v_exp_f32 v103, v103\n"
    "v_add_f32 v100, 1.0, v100\n"
    "v_add_f32 v101, 1.0, v101\n"
    "v_add_f32 v102, 1.0, v102\n"
    "v_add_f32 v103, 1.0, v103\n"
    "v_rcp_f32 v100, v100\n"
    "v_rcp_f32 v101, v101\n"
    "v_rcp_f32 v102, v102\n"
    "v_rcp_f32 v103, v103\n"
    "v_fma_f32 v100, v100, s58, v17\n"
    "v_fma_f32 v101, v101, s58, v17\n"
    "v_fma_f32 v102, v102, s58, v17\n"
    "v_fma_f32 v103, v103, s58, v17\n"
    "s_waitcnt lgkmcnt(0)\n"
    "v_mul_f32 v100, v100, v104\n"
    "v_mul_f32 v101, v101, v105\n"
    "v_mul_f32 v102, v102, v106\n"
    "v_mul_f32 v103, v103, v107\n"
    "v_cvt_pk_bf16_f32 v96, v100, v101\n"
    "v_cvt_pk_bf16_f32 v97, v102, v103\n"
    "buffer_store_dwordx2 v[96:97], v15, s[28:31], 0 offen\n"
    "s_endpgm\n"
    ".Lt6_2%=:\n"
    ".Lpad13%=:\n"
    "s_cbranch_execz .Lpad13r%=\n"
    "v_mfma_f32_16x16x32_bf16 v[48:51], v[252:255], v[136:139], v[48:51]\n"
    "ds_read_b128 v[124:127], v5 offset:5696\n"
    "v_mfma_f32_16x16x32_bf16 v[56:59], v[252:255], v[144:147], v[56:59]\n"
    "s_barrier\n"
    "ds_read_b64_tr_b16 v[96:97], v6 offset:20480\n"
    "v_mfma_f32_16x16x32_bf16 v[64:67], v[252:255], v[152:155], v[64:67]\n"
    "ds_read_b64_tr_b16 v[98:99], v6 offset:20992\n"
    "ds_read_b128 v[184:187], v5 offset:64\n"
    "v_mfma_f32_16x16x32_bf16 v[72:75], v[252:255], v[88:91], v[72:75]\n"
    "ds_read_b128 v[132:135], v5 offset:192\n"
    "ds_read_b128 v[100:103], v5 offset:320\n"
    "s_waitcnt lgkmcnt(6)\n"
    "v_mfma_f32_16x16x32_bf16 v[80:83], v[252:255], v[112:115], v[80:83]\n"
    "ds_read_b128 v[160:163], v5 offset:448\n"
    "v_mfma_f32_16x16x32_bf16 v[192:195], v[224:227], v[140:143], v[192:195]\n"
    "ds_read_b64_tr_b16 v[104:105], v6 offset:24576\n"
    "ds_read_b64_tr_b16 v[106:107], v6 offset:25088\n"
    "v_mfma_f32_16x16x32_bf16 v[196:199], v[224:227], v[148:151], v[196:199]\n"
    "v_mfma_f32_16x16x32_bf16 v[200:203], v[224:227], v[156:159], v[200:203]\n"
    "v_mfma_f32_16x16x32_bf16 v[204:207], v[224:227], v[172:175], v[204:207]\n"
    "ds_read_b128 v[168:171], v5 offset:576\n"
    "v_mfma_f32_16x16x32_bf16 v[208:211], v[224:227], v[188:191], v[208:211]\n"
    "ds_read_b64_tr_b16 v[108:109], v6 offset:28672\n"
    "ds_read_b64_tr_b16 v[110:111], v6 offset:29184\n"
    "v_mfma_f32_16x16x32_bf16 v[212:215], v[224:227], v[164:167], v[212:215]\n"
    "v_add_f32 v20, v20, v192\n"
    "v_mfma_f32_16x16x32_bf16 v[216:219], v[224:227], v[92:95], v[216:219]\n"
    "v_add_f32 v21, v21, v193\n"
    "s_waitcnt lgkmcnt(5)\n"
    "v_mfma_f32_16x16x32_bf16 v[220:223], v[224:227], v[124:127], v[220:223]\n"
    "ds_read_b128 v[176:179], v5 offset:704\n"
    "v_add_f32 v24, v24, v192\n"
    "v_mfma_f32_16x16x32_bf16 v[20:23], v[96:99], v[184:187], v[20:23]\n"
    "ds_read_b64_tr_b16 v[180:181], v6 offset:32768\n"
    "ds_read_b64_tr_b16 v[182:183], v6 offset:33280\n"
    "v_mfma_f32_16x16x32_bf16 v[24:27], v[96:99], v[132:135], v[24:27]\n"
    "v_add_f32 v32, v32, v196\n"
    "v_mfma_f32_16x16x32_bf16 v[28:31], v[96:99], v[100:103], v[28:31]\n"
    "v_add_f32 v36, v36, v200\n"
    "v_mfma_f32_16x16x32_bf16 v[32:35], v[96:99], v[160:163], v[32:35]\n"
    "ds_read_b128 v[84:87], v5 offset:832\n"
    "v_add_f32 v37, v37, v201\n"
    "s_waitcnt lgkmcnt(4)\n"
    "v_mfma_f32_16x16x32_bf16 v[20:23], v[104:107], v[132:135], v[20:23]\n"
    "ds_read_b64_tr_b16 v[116:117], v6 offset:36864\n"
    "ds_read_b64_tr_b16 v[118:119], v6 offset:37376\n"
    "v_mfma_f32_16x16x32_bf16 v[24:27], v[104:107], v[100:103], v[24:27]\n"
    "v_add_f32 v38, v38, v202\n"
    "v_mfma_f32_16x16x32_bf16 v[28:31], v[104:107], v[160:163], v[28:31]\n"
    "v_add_f32 v39, v39, v203\n"
    "v_mfma_f32_16x16x32_bf16 v[32:35], v[104:107], v[168:171], v[32:35]\n"
    "ds_read_b128 v[120:123], v5 offset:960\n"
    "v_add_f32 v40, v40, v200\n"
    "v_mfma_f32_16x16x32_bf16 v[20:23], v[108:111], v[100:103], v[20:23]\n"
    "ds_read_b64_tr_b16 v[128:129], v6 offset:40960\n"
    "ds_read_b64_tr_b16 v[130:131], v6 offset:41472\n"
    "v_mfma_f32_16x16x32_bf16 v[24:27], v[108:111], v[160:163], v[24:27]\n"
    "v_add_f32 v41, v41, v201\n"
    "v_mfma_f32_16x16x32_bf16 v[28:31], v[108:111], v[168:171], v[28:31]\n"
    "v_add_f32 v42, v42, v202\n"
    "s_waitcnt lgkmcnt(5)\n"
    "v_mfma_f32_16x16x32_bf16 v[32:35], v[108:111], v[176:179], v[32:35]\n"
    "ds_read_b128 v[136:139], v5 offset:1088\n"
    "v_mfma_f32_16x16x32_bf16 v[20:23], v[180:183], v[160:163], v[20:23]\n"
    "ds_read_b64_tr_b16 v[144:145], v6 offset:45056\n"
    "ds_read_b64_tr_b16 v[146:147], v6 offset:45568\n"
    "v_mfma_f32_16x16x32_bf16 v[24:27], v[180:183], v[168:171], v[24:27]\n"
    "v_add_f32 v43, v43, v203\n"
    "v_mfma_f32_16x16x32_bf16 v[28:31], v[180:183], v[176:179], v[28:31]\n"
    "v_add_f32 v44, v44, v204\n"
    "v_mfma_f32_16x16x32_bf16 v[32:35], v[180:183], v[84:87], v[32:35]\n"
    "ds_read_b128 v[152:155], v5 offset:1216\n"
    "s_waitcnt lgkmcnt(4)\n"
    "v_mfma_f32_16x16x32_bf16 v[20:23], v[116:119], v[168:171], v[20:23]\n"
    "ds_read_b64_tr_b16 v[88:89], v6 offset:49152\n"
    "ds_read_b64_tr_b16 v[90:91], v6 offset:49664\n"
    "v_mfma_f32_16x16x32_bf16 v[24:27], v[116:119], v[176:179], v[24:27]\n"
    "v_add_f32 v45, v45, v205\n"
    "v_mfma_f32_16x16x32_bf16 v[28:31], v[116:119], v[84:87], v[28:31]\n"
    "v_add_f32 v46, v46, v206\n"
    "v_mfma_f32_16x16x32_bf16 v[32:35], v[116:119], v[120:123], v[32:35]\n"
    "ds_read_b128 v[112:115], v5 offset:1344\n"
    "v_mfma_f32_16x16x32_bf16 v[20:23], v[128:131], v[176:179], v[20:23]\n"
    "ds_read_b64_tr_b16 v[140:141], v6 offset:53248\n"
    "ds_read_b64_tr_b16 v[142:143], v6 offset:53760\n"
    "v_mfma_f32_16x16x32_bf16 v[24:27], v[128:131], v[84:87], v[24:27]\n"
    "v_add_f32 v47, v47, v207\n"
    "v_mfma_f32_16x16x32_bf16 v[28:31], v[128:131], v[120:123], v[28:31]\n"
    "v_add_f32 v48, v48, v204\n"
    "s_waitcnt lgkmcnt(5)\n"
    "v_mfma_f32_16x16x32_bf16 v[32:35], v[128:131], v[136:139], v[32:35]\n"
    "ds_read_b128 v[148:151], v5 offset:1472\n"
    "v_mfma_f32_16x16x32_bf16 v[20:23], v[144:147], v[84:87], v[20:23]\n"
    "s_barrier\n"
    "ds_read_b64_tr_b16 v[156:157], v6 offset:57344\n"
    "v_mfma_f32_16x16x32_bf16 v[24:27], v[144:147], v[120:123], v[24:27]\n"
    "ds_read_b64_tr_b16 v[158:159], v6 offset:57856\n"
    "v_add_f32 v49, v49, v205\n"
    "v_mfma_f32_16x16x32_bf16 v[28:31], v[144:147], v[136:139], v[28:31]\n"
    "v_add_f32 v50, v50, v206\n"
    "v_mfma_f32_16x16x32_bf16 v[32:35], v[144:147], v[152:155], v[32:35]\n"
    "ds_read_b128 v[172:175], v5 offset:1600\n"
    "s_waitcnt lgkmcnt(4)\n"
    "v_mfma_f32_16x16x32_bf16 v[20:23], v[88:91], v[120:123], v[20:23]\n"
    "ds_read_b64_tr_b16 v[188:189], v6 offset:61440\n"
    "ds_read_b64_tr_b16 v[190:191], v6 offset:61952\n"
    "v_mfma_f32_16x16x32_bf16 v[24:27], v[88:91], v[136:139], v[24:27]\n"
    "v_add_f32 v51, v51, v207\n"
    "v_mfma_f32_16x16x32_bf16 v[28:31], v[88:91], v[152:155], v[28:31]\n"
    "v_add_f32 v52, v52, v208\n"
    "v_mfma_f32_16x16x32_bf16 v[32:35], v[88:91], v[112:115], v[32:35]\n"
    "ds_read_b128 v[164:167], v5 offset:1728\n"
    "v_mfma_f32_16x16x32_bf16 v[20:23], v[140:143], v[136:139], v[20:23]\n"
    "ds_read_b64_tr_b16 v[92:93], v7 offset:0\n"
    "ds_read_b64_tr_b16 v[94:95], v7 offset:512\n"
    "v_mfma_f32_16x16x32_bf16 v[24:27], v[140:143], v[152:155], v[24:27]\n"
    "v_add_f32 v53, v53, v209\n"
    "v_mfma_f32_16x16x32_bf16 v[28:31], v[140:143], v[112:115], v[28:31]\n"
    "v_add_f32 v54, v54, v210\n"
    "s_waitcnt lgkmcnt(5)\n"
    "v_mfma_f32_16x16x32_bf16 v[32:35], v[140:143], v[148:151], v[32:35]\n"
    "ds_read_b128 v[124:127], v5 offset:1856\n"
    "v_mfma_f32_16x16x32_bf16 v[20:23], v[156:159], v[152:155], v[20:23]\n"
    "ds_read_b64_tr_b16 v[224:225], v7 offset:4096\n"
    "ds_read_b64_tr_b16 v[226:227], v7 offset:4608\n"
    "v_mfma_f32_16x16x32_bf16 v[24:27], v[156:159], v[112:115], v[24:27]\n"
    "v_add_f32 v55, v55, v211\n"
    "v_mfma_f32_16x16x32_bf16 v[28:31], v[156:159], v[148:151], v[28:31]\n"
    "v_add_f32 v56, v56, v208\n"
    "v_mfma_f32_16x16x32_bf16 v[32:35], v[156:159], v[172:175], v[32:35]\n"
    "ds_read_b128 v[228:231], v5 offset:1984\n"
    "s_waitcnt lgkmcnt(4)\n"
    "v_mfma_f32_16x16x32_bf16 v[20:23], v[188:191], v[112:115], v[20:23]\n"
    "ds_read_b64_tr_b16 v[232:233], v7 offset:8192\n"
    "ds_read_b64_tr_b16 v[234:235], v7 offset:8704\n"
    "v_mfma_f32_16x16x32_bf16 v[24:27], v[188:191], v[148:151], v[24:27]\n"
    "v_add_f32 v57, v57, v209\n"
    "v_mfma_f32_16x16x32_bf16 v[28:31], v[188:191], v[172:175], v[28:31]\n"
    "v_add_f32 v58, v58, v210\n"
    "v_mfma_f32_16x16x32_bf16 v[32:35], v[188:191], v[164:167], v[32:35]\n"
    "ds_read_b128 v[236:239], v5 offset:2144\n"
    "v_mfma_f32_16x16x32_bf16 v[20:23], v[92:95], v[148:151], v[20:23]\n"
    "ds_read_b64_tr_b16 v[240:241], v7 offset:12288\n"
    "ds_read_b64_tr_b16 v[242:243], v7 offset:12800\n"
    "v_mfma_f32_16x16x32_bf16 v[24:27], v[92:95], v[172:175], v[24:27]\n"
    "v_add_f32 v59, v59, v211\n"
    "v_mfma_f32_16x16x32_bf16 v[28:31], v[92:95], v[164:167], v[28:31]\n"
    "v_add_f32 v60, v60, v212\n"
    "s_waitcnt lgkmcnt(5)\n"
    "v_mfma_f32_16x16x32_bf16 v[32:35], v[92:95], v[124:127], v[32:35]\n"
    "ds_read_b128 v[244:247], v5 offset:2272\n"
    "v_mfma_f32_16x16x32_bf16 v[20:23], v[224:227], v[172:175], v[20:23]\n"
    "ds_read_b64_tr_b16 v[248:249], v7 offset:16384\n"
    "ds_read_b64_tr_b16 v[250:251], v7 offset:16896\n"
    "v_mfma_f32_16x16x32_bf16 v[24:27], v[224:227], v[164:167], v[24:27]\n"
    "v_add_f32 v61, v61, v213\n"
    "v_mfma_f32_16x16x32_bf16 v[28:31], v[224:227], v[124:127], v[28:31]\n"
    "v_add_f32 v62, v62, v214\n"
    "v_mfma_f32_16x16x32_bf16 v[32:35], v[224:227], v[228:231], v[32:35]\n"
    "ds_read_b128 v[252:255], v5 offset:2400\n"
    "s_waitcnt lgkmcnt(4)\n"
    "v_mfma_f32_16x16x32_bf16 v[20:23], v[232:235], v[164:167], v[20:23]\n"
    "ds_read_b64_tr_b16 v[184:185], v7 offset:20480\n"
    "ds_read_b64_tr_b16 v[186:187], v7 offset:20992\n"
    "v_mfma_f32_16x16x32_bf16 v[24:27], v[232:235], v[124:127], v[24:27]\n"
    "v_add_f32 v63, v63, v215\n"
    "v_mfma_f32_16x16x32_bf16 v[28:31], v[232:235], v[228:231], v[28:31]\n"
    "v_add_f32 v64, v64, v212\n"
    "v_mfma_f32_16x16x32_bf16 v[32:35], v[232:235], v[236:239], v[32:35]\n"
    "ds_read_b128 v[132:135], v5 offset:2528\n"
    "v_mfma_f32_16x16x32_bf16 v[20:23], v[240:243], v[124:127], v[20:23]\n"
    "ds_read_b64_tr_b16 v[100:101], v7 offset:24576\n"
    "ds_read_b64_tr_b16 v[102:103], v7 offset:25088\n"
    "v_mfma_f32_16x16x32_bf16 v[24:27], v[240:243], v[228:231], v[24:27]\n"
    "v_add_f32 v65, v65, v213\n"
    "v_mfma_f32_16x16x32_bf16 v[28:31], v[240:243], v[236:239], v[28:31]\n"
    "v_add_f32 v66, v66, v214\n"
    "s_waitcnt lgkmcnt(5)\n"
    "v_mfma_f32_16x16x32_bf16 v[32:35], v[240:243], v[244:247], v[32:35]\n"
    "ds_read_b128 v[160:163], v5 offset:2656\n"
    "v_mfma_f32_16x16x32_bf16 v[20:23], v[248:251], v[228:231], v[20:23]\n"
    "ds_read_b64_tr_b16 v[200:201], v7 offset:28672\n"
    "ds_read_b64_tr_b16 v[202:203], v7 offset:29184\n"
    "v_mfma_f32_16x16x32_bf16 v[24:27], v[248:251], v[236:239], v[24:27]\n"
    "v_add_f32 v67, v67, v215\n"
    "v_mfma_f32_16x16x32_bf16 v[28:31], v[248:251], v[244:247], v[28:31]\n"
    "v_add_f32 v68, v68, v216\n"
    "v_mfma_f32_16x16x32_bf16 v[32:35], v[248:251], v[252:255], v[32:35]\n"
    "ds_read_b128 v[168:171], v5 offset:2784\n"
    "s_waitcnt lgkmcnt(4)\n"
    "v_mfma_f32_16x16x32_bf16 v[20:23], v[184:187], v[236:239], v[20:23]\n"
    "s_barrier\n"
    "ds_read_b64_tr_b16 v[176:177], v7 offset:32768\n"
    "v_mfma_f32_16x16x32_bf16 v[24:27], v[184:187], v[244:247], v[24:27]\n"
    "ds_read_b64_tr_b16 v[178:179], v7 offset:33280\n"
    "v_add_f32 v69, v69, v217\n"
    "v_mfma_f32_16x16x32_bf16 v[28:31], v[184:187], v[252:255], v[28:31]\n"
    "v_add_f32 v70, v70, v218\n"
    "v_mfma_f32_16x16x32_bf16 v[32:35], v[184:187], v[132:135], v[32:35]\n"
    "ds_read_b128 v[84:87], v5 offset:2912\n"
    "v_mfma_f32_16x16x32_bf16 v[20:23], v[100:103], v[244:247], v[20:23]\n"
    "ds_read_b64_tr_b16 v[120:121], v7 offset:36864\n"
    "ds_read_b64_tr_b16 v[122:123], v7 offset:37376\n"
    "v_mfma_f32_16x16x32_bf16 v[24:27], v[100:103], v[252:255], v[24:27]\n"
    "v_add_f32 v71, v71, v219\n"
    "v_mfma_f32_16x16x32_bf16 v[28:31], v[100:103], v[132:135], v[28:31]\n"
    "v_add_f32 v72, v72, v216\n"
    "s_waitcnt lgkmcnt(5)\n"
    "v_mfma_f32_16x16x32_bf16 v[32:35], v[100:103], v[160:163], v[32:35]\n"
    "ds_read_b128 v[204:207], v5 offset:3040\n"
    "v_mfma_f32_16x16x32_bf16 v[20:23], v[200:203], v[252:255], v[20:23]\n"
    "ds_read_b64_tr_b16 v[136:137], v6 offset:0\n"
    "ds_read_b64_tr_b16 v[138:139], v6 offset:512\n"
    "v_mfma_f32_16x16x32_bf16 v[24:27], v[200:203], v[132:135], v[24:27]\n"
    "v_add_f32 v73, v73, v217\n"
    "v_mfma_f32_16x16x32_bf16 v[28:31], v[200:203], v[160:163], v[28:31]\n"
    "v_add_f32 v74, v74, v218\n"
    "v_mfma_f32_16x16x32_bf16 v[32:35], v[200:203], v[168:171], v[32:35]\n"
    "ds_read_b128 v[152:155], v5 offset:3168\n"
    "s_waitcnt lgkmcnt(4)\n"
    "v_mfma_f32_16x16x32_bf16 v[20:23], v[176:179], v[132:135], v[20:23]\n"
    "ds_read_b64_tr_b16 v[112:113], v6 offset:4096\n"
    "ds_read_b64_tr_b16 v[114:115], v6 offset:4608\n"
    "v_mfma_f32_16x16x32_bf16 v[24:27], v[176:179], v[160:163], v[24:27]\n"
    "v_add_f32 v75, v75, v219\n"
    "v_mfma_f32_16x16x32_bf16 v[28:31], v[176:179], v[168:171], v[28:31]\n"
    "v_add_f32 v76, v76, v220\n"
    "v_mfma_f32_16x16x32_bf16 v[32:35], v[176:179], v[84:87], v[32:35]\n"
    "ds_read_b128 v[148:151], v5 offset:3296\n"
    "v_mfma_f32_16x16x32_bf16 v[20:23], v[120:123], v[160:163], v[20:23]\n"
    "ds_read_b64_tr_b16 v[208:209], v6 offset:8192\n"
    "ds_read_b64_tr_b16 v[210:211], v6 offset:8704\n"
    "v_mfma_f32_16x16x32_bf16 v[24:27], v[120:123], v[168:171], v[24:27]\n"
    "v_add_f32 v77, v77, v221\n"
    "v_mfma_f32_16x16x32_bf16 v[28:31], v[120:123], v[84:87], v[28:31]\n"
    "v_add_f32 v78, v78, v222\n"
    "s_waitcnt lgkmcnt(5)\n"
    "v_mfma_f32_16x16x32_bf16 v[32:35], v[120:123], v[204:207], v[32:35]\n"
    "ds_read_b128 v[172:175], v5 offset:3424\n"
    "v_mfma_f32_16x16x32_bf16 v[20:23], v[136:139], v[168:171], v[20:23]\n"
    "ds_read_b64_tr_b16 v[164:165], v6 offset:12288\n"
    "ds_read_b64_tr_b16 v[166:167], v6 offset:12800\n"
    "v_mfma_f32_16x16x32_bf16 v[24:27], v[136:139], v[84:87], v[24:27]\n"
    "v_add_f32 v79, v79, v223\n"
    "v_mfma_f32_16x16x32_bf16 v[28:31], v[136:139], v[204:207], v[28:31]\n"
    "v_add_f32 v80, v80, v220\n"
    "v_mfma_f32_16x16x32_bf16 v[32:35], v[136:139], v[152:155], v[32:35]\n"
    "ds_read_b128 v[124:127], v5 offset:3552\n"
    "s_waitcnt lgkmcnt(4)\n"
    "v_mfma_f32_16x16x32_bf16 v[20:23], v[112:115], v[84:87], v[20:23]\n"
    "ds_read_b64_tr_b16 v[228:229], v6 offset:16384\n"
    "ds_read_b64_tr_b16 v[230:231], v6 offset:16896\n"
    "v_mfma_f32_16x16x32_bf16 v[24:27], v[112:115], v[204:207], v[24:27]\n"
    "v_add_f32 v81, v81, v221\n"
    "v_mfma_f32_16x16x32_bf16 v[28:31], v[112:115], v[152:155], v[28:31]\n"
    "v_add_f32 v82, v82, v222\n"
    "v_mfma_f32_16x16x32_bf16 v[32:35], v[112:115], v[148:151], v[32:35]\n"
    "ds_read_b128 v[212:215], v5 offset:3680\n"
    "v_mfma_f32_16x16x32_bf16 v[20:23], v[208:211], v[204:207], v[20:23]\n"
    "s_barrier\n"
    "ds_read_b64_tr_b16 v[236:237], v6 offset:20480\n"
    "v_mfma_f32_16x16x32_bf16 v[24:27], v[208:211], v[152:155], v[24:27]\n"
    "ds_read_b64_tr_b16 v[238:239], v6 offset:20992\n"
    "v_add_f32 v83, v83, v223\n"
    "v_mfma_f32_16x16x32_bf16 v[28:31], v[208:211], v[148:151], v[28:31]\n"
    "s_waitcnt lgkmcnt(5)\n"
    "v_mfma_f32_16x16x32_bf16 v[32:35], v[208:211], v[172:175], v[32:35]\n"
    "ds_read_b128 v[244:247], v5 offset:3808\n"
    "v_mfma_f32_16x16x32_bf16 v[20:23], v[164:167], v[152:155], v[20:23]\n"
    "ds_read_b64_tr_b16 v[252:253], v6 offset:24576\n"
    "ds_read_b64_tr_b16 v[254:255], v6 offset:25088\n"
    "v_mfma_f32_16x16x32_bf16 v[24:27], v[164:167], v[148:151], v[24:27]\n"
    "v_mfma_f32_16x16x32_bf16 v[28:31], v[164:167], v[172:175], v[28:31]\n"
    "v_mfma_f32_16x16x32_bf16 v[32:35], v[164:167], v[124:127], v[32:35]\n"
    "ds_read_b128 v[132:135], v5 offset:3936\n"
    "s_waitcnt lgkmcnt(5)\n"
    "v_mfma_f32_16x16x32_bf16 v[20:23], v[228:231], v[148:151], v[20:23]\n"
    "ds_read_b64_tr_b16 v[216:217], v6 offset:28672\n"
    "ds_read_b64_tr_b16 v[218:219], v6 offset:29184\n"
    "v_mfma_f32_16x16x32_bf16 v[24:27], v[228:231], v[172:175], v[24:27]\n"
    "v_mfma_f32_16x16x32_bf16 v[28:31], v[228:231], v[124:127], v[28:31]\n"
    "v_mfma_f32_16x16x32_bf16 v[32:35], v[228:231], v[212:215], v[32:35]\n"
    "ds_read_b128 v[160:163], v5 offset:4064\n"
    "s_waitcnt lgkmcnt(4)\n"
    "v_mfma_f32_16x16x32_bf16 v[20:23], v[236:239], v[172:175], v[20:23]\n"
    "ds_read_b64_tr_b16 v[168:169], v6 offset:32768\n"
    "ds_read_b64_tr_b16 v[170:171], v6 offset:33280\n"
    "v_mfma_f32_16x16x32_bf16 v[24:27], v[236:239], v[124:127], v[24:27]\n"
    "v_mfma_f32_16x16x32_bf16 v[28:31], v[236:239], v[212:215], v[28:31]\n"
    "v_mfma_f32_16x16x32_bf16 v[32:35], v[236:239], v[244:247], v[32:35]\n"
    "ds_read_b128 v[84:87], v5 offset:4224\n"
    "v_mfma_f32_16x16x32_bf16 v[20:23], v[252:255], v[124:127], v[20:23]\n"
    "ds_read_b64_tr_b16 v[204:205], v6 offset:36864\n"
    "ds_read_b64_tr_b16 v[206:207], v6 offset:37376\n"
    "v_mfma_f32_16x16x32_bf16 v[24:27], v[252:255], v[212:215], v[24:27]\n"
    "v_mfma_f32_16x16x32_bf16 v[28:31], v[252:255], v[244:247], v[28:31]\n"
    "s_waitcnt lgkmcnt(5)\n"
    "v_mfma_f32_16x16x32_bf16 v[32:35], v[252:255], v[132:135], v[32:35]\n"
    "ds_read_b128 v[220:223], v5 offset:4352\n"
    "v_mfma_f32_16x16x32_bf16 v[20:23], v[216:219], v[212:215], v[20:23]\n"
    "ds_read_b128 v[152:155], v5 offset:576\n"
    "v_mfma_f32_16x16x32_bf16 v[24:27], v[216:219], v[244:247], v[24:27]\n"
    "ds_read_b128 v[148:151], v5 offset:704\n"
    "v_mfma_f32_16x16x32_bf16 v[28:31], v[216:219], v[132:135], v[28:31]\n"
    "ds_read_b128 v[172:175], v5 offset:832\n"
    "v_mfma_f32_16x16x32_bf16 v[32:35], v[216:219], v[160:163], v[32:35]\n"
    "ds_read_b128 v[124:127], v5 offset:960\n"
    "s_waitcnt lgkmcnt(5)\n"
    "v_mfma_f32_16x16x32_bf16 v[20:23], v[168:171], v[244:247], v[20:23]\n"
    "v_mfma_f32_16x16x32_bf16 v[24:27], v[168:171], v[132:135], v[24:27]\n"
    "v_mfma_f32_16x16x32_bf16 v[28:31], v[168:171], v[160:163], v[28:31]\n"
    "v_mfma_f32_16x16x32_bf16 v[32:35], v[168:171], v[84:87], v[32:35]\n"
    "ds_read_b128 v[212:215], v5 offset:1088\n"
    "v_mfma_f32_16x16x32_bf16 v[20:23], v[204:207], v[132:135], v[20:23]\n"
    "v_mfma_f32_16x16x32_bf16 v[24:27], v[204:207], v[160:163], v[24:27]\n"
    "v_mfma_f32_16x16x32_bf16 v[28:31], v[204:207], v[84:87], v[28:31]\n"
    "s_waitcnt lgkmcnt(1)\n"
    "v_mfma_f32_16x16x32_bf16 v[32:35], v[204:207], v[220:223], v[32:35]\n"
    "ds_read_b128 v[244:247], v5 offset:1216\n"
    "v_mfma_f32_16x16x32_bf16 v[36:39], v[96:99], v[152:155], v[36:39]\n"
    "v_mfma_f32_16x16x32_bf16 v[40:43], v[96:99], v[148:151], v[40:43]\n"
    "v_mfma_f32_16x16x32_bf16 v[44:47], v[96:99], v[172:175], v[44:47]\n"
    "v_add_f32 v22, v22, v194\n"
    "v_add_f32 v23, v23, v195\n"
    "v_mfma_f32_16x16x32_bf16 v[48:51], v[96:99], v[124:127], v[48:51]\n"
    "ds_read_b128 v[132:135], v5 offset:1344\n"
    "v_mfma_f32_16x16x32_bf16 v[36:39], v[104:107], v[148:151], v[36:39]\n"
    "v_add_f32 v25, v25, v193\n"
    "v_add_f32 v26, v26, v194\n"
    "v_mfma_f32_16x16x32_bf16 v[40:43], v[104:107], v[172:175], v[40:43]\n"
    "v_add_f32 v27, v27, v195\n"
    "v_add_f32 v28, v28, v196\n"
    "v_mfma_f32_16x16x32_bf16 v[44:47], v[104:107], v[124:127], v[44:47]\n"
    "v_add_f32 v29, v29, v197\n"
    "v_add_f32 v30, v30, v198\n"
    "s_waitcnt lgkmcnt(1)\n"
    "v_mfma_f32_16x16x32_bf16 v[48:51], v[104:107], v[212:215], v[48:51]\n"
    "ds_read_b128 v[160:163], v5 offset:1472\n"
    "v_mfma_f32_16x16x32_bf16 v[36:39], v[108:111], v[172:175], v[36:39]\n"
    "v_add_f32 v31, v31, v199\n"
    "v_add_f32 v33, v33, v197\n"
    "v_mfma_f32_16x16x32_bf16 v[40:43], v[108:111], v[124:127], v[40:43]\n"
    "v_add_f32 v34, v34, v198\n"
    "v_add_f32 v35, v35, v199\n"
    "v_mfma_f32_16x16x32_bf16 v[44:47], v[108:111], v[212:215], v[44:47]\n"
    "v_cvt_pk_bf16_f32 v20, v20, v21\n"
    "v_cvt_pk_bf16_f32 v21, v22, v23\n"
    "v_mfma_f32_16x16x32_bf16 v[48:51], v[108:111], v[244:247], v[48:51]\n"
    "ds_read_b128 v[84:87], v5 offset:1600\n"
    "v_mfma_f32_16x16x32_bf16 v[36:39], v[180:183], v[124:127], v[36:39]\n"
    "v_cvt_pk_bf16_f32 v22, v24, v25\n"
    "v_cvt_pk_bf16_f32 v23, v26, v27\n"
    "v_mfma_f32_16x16x32_bf16 v[40:43], v[180:183], v[212:215], v[40:43]\n"
    "s_xor_b32 s43, s41, 0\n"
    "buffer_store_dwordx4 v[20:23], v8, s[24:27], s43 offen sc1\n"
    "v_mfma_f32_16x16x32_bf16 v[44:47], v[180:183], v[244:247], v[44:47]\n"
    "v_cvt_pk_bf16_f32 v28, v28, v29\n"
    "v_cvt_pk_bf16_f32 v29, v30, v31\n"
    "s_waitcnt lgkmcnt(1)\n"
    "v_mfma_f32_16x16x32_bf16 v[48:51], v[180:183], v[132:135], v[48:51]\n"
    "ds_read_b128 v[220:223], v5 offset:1728\n"
    "v_mfma_f32_16x16x32_bf16 v[36:39], v[116:119], v[212:215], v[36:39]\n"
    "v_cvt_pk_bf16_f32 v30, v32, v33\n"
    "v_cvt_pk_bf16_f32 v31, v34, v35\n"
    "v_mfma_f32_16x16x32_bf16 v[40:43], v[116:119], v[244:247], v[40:43]\n"
    "s_xor_b32 s43, s41, 36864\n"
    "buffer_store_dwordx4 v[28:31], v8, s[24:27], s43 offen sc1\n"
    "v_mfma_f32_16x16x32_bf16 v[44:47], v[116:119], v[132:135], v[44:47]\n"
    "v_mfma_f32_16x16x32_bf16 v[48:51], v[116:119], v[160:163], v[48:51]\n"
    "ds_read_b128 v[152:155], v5 offset:1856\n"
    "v_mfma_f32_16x16x32_bf16 v[36:39], v[128:131], v[244:247], v[36:39]\n"
    "v_mfma_f32_16x16x32_bf16 v[40:43], v[128:131], v[132:135], v[40:43]\n"
    "v_mfma_f32_16x16x32_bf16 v[44:47], v[128:131], v[160:163], v[44:47]\n"
    "s_waitcnt lgkmcnt(1)\n"
    "v_mfma_f32_16x16x32_bf16 v[48:51], v[128:131], v[84:87], v[48:51]\n"
    "s_barrier\n"
    "ds_read_b128 v[148:151], v5 offset:1984\n"
    "v_mfma_f32_16x16x32_bf16 v[36:39], v[144:147], v[132:135], v[36:39]\n"
    "v_mfma_f32_16x16x32_bf16 v[40:43], v[144:147], v[160:163], v[40:43]\n"
    "v_mfma_f32_16x16x32_bf16 v[44:47], v[144:147], v[84:87], v[44:47]\n"
    "v_mfma_f32_16x16x32_bf16 v[48:51], v[144:147], v[220:223], v[48:51]\n"
    "ds_read_b128 v[192:195], v5 offset:2144\n"
    "v_mfma_f32_16x16x32_bf16 v[36:39], v[88:91], v[160:163], v[36:39]\n"
    "v_mfma_f32_16x16x32_bf16 v[40:43], v[88:91], v[84:87], v[40:43]\n"
    "v_mfma_f32_16x16x32_bf16 v[44:47], v[88:91], v[220:223], v[44:47]\n"
    "s_waitcnt lgkmcnt(1)\n"
    "v_mfma_f32_16x16x32_bf16 v[48:51], v[88:91], v[152:155], v[48:51]\n"
    "ds_read_b128 v[172:175], v5 offset:2272\n"
    "v_mfma_f32_16x16x32_bf16 v[36:39], v[140:143], v[84:87], v[36:39]\n"
    "v_mfma_f32_16x16x32_bf16 v[40:43], v[140:143], v[220:223], v[40:43]\n"
    "v_mfma_f32_16x16x32_bf16 v[44:47], v[140:143], v[152:155], v[44:47]\n"
    "v_mfma_f32_16x16x32_bf16 v[48:51], v[140:143], v[148:151], v[48:51]\n"
    "ds_read_b128 v[196:199], v5 offset:2400\n"
    "v_mfma_f32_16x16x32_bf16 v[36:39], v[156:159], v[220:223], v[36:39]\n"
    "v_mfma_f32_16x16x32_bf16 v[40:43], v[156:159], v[152:155], v[40:43]\n"
    "v_mfma_f32_16x16x32_bf16 v[44:47], v[156:159], v[148:151], v[44:47]\n"
    "s_waitcnt lgkmcnt(1)\n"
    "v_mfma_f32_16x16x32_bf16 v[48:51], v[156:159], v[192:195], v[48:51]\n"
    "ds_read_b128 v[124:127], v5 offset:2528\n"
    "v_mfma_f32_16x16x32_bf16 v[36:39], v[188:191], v[152:155], v[36:39]\n"
    "v_mfma_f32_16x16x32_bf16 v[40:43], v[188:191], v[148:151], v[40:43]\n"
    "v_mfma_f32_16x16x32_bf16 v[44:47], v[188:191], v[192:195], v[44:47]\n"
    "v_mfma_f32_16x16x32_bf16 v[48:51], v[188:191], v[172:175], v[48:51]\n"
    "ds_read_b128 v[212:215], v5 offset:2656\n"
    "v_mfma_f32_16x16x32_bf16 v[36:39], v[92:95], v[148:151], v[36:39]\n"
    "v_mfma_f32_16x16x32_bf16 v[40:43], v[92:95], v[192:195], v[40:43]\n"
    "v_mfma_f32_16x16x32_bf16 v[44:47], v[92:95], v[172:175], v[44:47]\n"
    "s_waitcnt lgkmcnt(1)\n"
    "v_mfma_f32_16x16x32_bf16 v[48:51], v[92:95], v[196:199], v[48:51]\n"
    "ds_read_b128 v[244:247], v5 offset:2784\n"
    "v_mfma_f32_16x16x32_bf16 v[36:39], v[224:227], v[192:195], v[36:39]\n"
    "v_mfma_f32_16x16x32_bf16 v[40:43], v[224:227], v[172:175], v[40:43]\n"
    "v_mfma_f32_16x16x32_bf16 v[44:47], v[224:227], v[196:199], v[44:47]\n"
    "v_mfma_f32_16x16x32_bf16 v[48:51], v[224:227], v[124:127], v[48:51]\n"
    "ds_read_b128 v[132:135], v5 offset:2912\n"
    "v_mfma_f32_16x16x32_bf16 v[36:39], v[232:235], v[172:175], v[36:39]\n"
    "v_mfma_f32_16x16x32_bf16 v[40:43], v[232:235], v[196:199], v[40:43]\n"
    "v_mfma_f32_16x16x32_bf16 v[44:47], v[232:235], v[124:127], v[44:47]\n"
    "s_waitcnt lgkmcnt(1)\n"
    "v_mfma_f32_16x16x32_bf16 v[48:51], v[232:235], v[212:215], v[48:51]\n"
    "ds_read_b128 v[160:163], v5 offset:3040\n"
    "v_mfma_f32_16x16x32_bf16 v[36:39], v[240:243], v[196:199], v[36:39]\n"
    "v_mfma_f32_16x16x32_bf16 v[40:43], v[240:243], v[124:127], v[40:43]\n"
    "v_mfma_f32_16x16x32_bf16 v[44:47], v[240:243], v[212:215], v[44:47]\n"
    "v_mfma_f32_16x16x32_bf16 v[48:51], v[240:243], v[244:247], v[48:51]\n"
    "ds_read_b128 v[84:87], v5 offset:3168\n"
    "v_mfma_f32_16x16x32_bf16 v[36:39], v[248:251], v[124:127], v[36:39]\n"
    "v_mfma_f32_16x16x32_bf16 v[40:43], v[248:251], v[212:215], v[40:43]\n"
    "v_mfma_f32_16x16x32_bf16 v[44:47], v[248:251], v[244:247], v[44:47]\n"
    "s_waitcnt lgkmcnt(1)\n"
    "v_mfma_f32_16x16x32_bf16 v[48:51], v[248:251], v[132:135], v[48:51]\n"
    "ds_read_b128 v[220:223], v5 offset:3296\n"
    "v_mfma_f32_16x16x32_bf16 v[36:39], v[184:187], v[212:215], v[36:39]\n"
    "v_mfma_f32_16x16x32_bf16 v[40:43], v[184:187], v[244:247], v[40:43]\n"
    "v_mfma_f32_16x16x32_bf16 v[44:47], v[184:187], v[132:135], v[44:47]\n"
    "v_mfma_f32_16x16x32_bf16 v[48:51], v[184:187], v[160:163], v[48:51]\n"
    "ds_read_b128 v[152:155], v5 offset:3424\n"
    "v_mfma_f32_16x16x32_bf16 v[36:39], v[100:103], v[244:247], v[36:39]\n"
    "v_mfma_f32_16x16x32_bf16 v[40:43], v[100:103], v[132:135], v[40:43]\n"
    "v_mfma_f32_16x16x32_bf16 v[44:47], v[100:103], v[160:163], v[44:47]\n"
    "s_waitcnt lgkmcnt(1)\n"
    "v_mfma_f32_16x16x32_bf16 v[48:51], v[100:103], v[84:87], v[48:51]\n"
    "ds_read_b128 v[148:151], v5 offset:3552\n"
    "v_mfma_f32_16x16x32_bf16 v[36:39], v[200:203], v[132:135], v[36:39]\n"
    "v_mfma_f32_16x16x32_bf16 v[40:43], v[200:203], v[160:163], v[40:43]\n"
    "v_mfma_f32_16x16x32_bf16 v[44:47], v[200:203], v[84:87], v[44:47]\n"
    "v_mfma_f32_16x16x32_bf16 v[48:51], v[200:203], v[220:223], v[48:51]\n"
    "ds_read_b128 v[192:195], v5 offset:3680\n"
    "v_mfma_f32_16x16x32_bf16 v[36:39], v[176:179], v[160:163], v[36:39]\n"
    "v_mfma_f32_16x16x32_bf16 v[40:43], v[176:179], v[84:87], v[40:43]\n"
    "v_mfma_f32_16x16x32_bf16 v[44:47], v[176:179], v[220:223], v[44:47]\n"
    "s_waitcnt lgkmcnt(1)\n"
    "v_mfma_f32_16x16x32_bf16 v[48:51], v[176:179], v[152:155], v[48:51]\n"
    "ds_read_b128 v[172:175], v5 offset:3808\n"
    "v_mfma_f32_16x16x32_bf16 v[36:39], v[120:123], v[84:87], v[36:39]\n"
    "v_mfma_f32_16x16x32_bf16 v[40:43], v[120:123], v[220:223], v[40:43]\n"
    "v_mfma_f32_16x16x32_bf16 v[44:47], v[120:123], v[152:155], v[44:47]\n"
    "v_mfma_f32_16x16x32_bf16 v[48:51], v[120:123], v[148:151], v[48:51]\n"
    "ds_read_b128 v[196:199], v5 offset:3936\n"
    "v_mfma_f32_16x16x32_bf16 v[36:39], v[136:139], v[220:223], v[36:39]\n"
    "v_mfma_f32_16x16x32_bf16 v[40:43], v[136:139], v[152:155], v[40:43]\n"
    "v_mfma_f32_16x16x32_bf16 v[44:47], v[136:139], v[148:151], v[44:47]\n"
    "s_waitcnt lgkmcnt(1)\n"
    "v_mfma_f32_16x16x32_bf16 v[48:51], v[136:139], v[192:195], v[48:51]\n"
    "ds_read_b128 v[124:127], v5 offset:4064\n"
    "v_mfma_f32_16x16x32_bf16 v[36:39], v[112:115], v[152:155], v[36:39]\n"
    "v_mfma_f32_16x16x32_bf16 v[40:43], v[112:115], v[148:151], v[40:43]\n"
    "v_mfma_f32_16x16x32_bf16 v[44:47], v[112:115], v[192:195], v[44:47]\n"
    "v_mfma_f32_16x16x32_bf16 v[48:51], v[112:115], v[172:175], v[48:51]\n"
    "ds_read_b128 v[212:215], v5 offset:4224\n"
    "v_mfma_f32_16x16x32_bf16 v[36:39], v[208:211], v[148:151], v[36:39]\n"
    "v_mfma_f32_16x16x32_bf16 v[40:43], v[208:211], v[192:195], v[40:43]\n"
    "v_mfma_f32_16x16x32_bf16 v[44:47], v[208:211], v[172:175], v[44:47]\n"
    "s_waitcnt lgkmcnt(1)\n"
    "v_mfma_f32_16x16x32_bf16 v[48:51], v[208:211], v[196:199], v[48:51]\n"
    "ds_read_b128 v[244:247], v5 offset:4352\n"
    "v_mfma_f32_16x16x32_bf16 v[36:39], v[164:167], v[192:195], v[36:39]\n"
    "v_mfma_f32_16x16x32_bf16 v[40:43], v[164:167], v[172:175], v[40:43]\n"
    "v_mfma_f32_16x16x32_bf16 v[44:47], v[164:167], v[196:199], v[44:47]\n"
    "v_mfma_f32_16x16x32_bf16 v[48:51], v[164:167], v[124:127], v[48:51]\n"
    "s_waitcnt vmcnt(0)\n"
    "v_add_u32 v18, 1, v18\n"
    "ds_read_b128 v[132:135], v5 offset:4480\n"
    "v_mfma_f32_16x16x32_bf16 v[36:39], v[228:231], v[172:175], v[36:39]\n"
    "buffer_store_dword v18, v19, s[24:27], 0 offen sc1\n"
    "v_mfma_f32_16x16x32_bf16 v[40:43], v[228:231], v[196:199], v[40:43]\n"
    "v_mfma_f32_16x16x32_bf16 v[44:47], v[228:231], v[124:127], v[44:47]\n"
    "s_waitcnt lgkmcnt(1)\n"
    "v_mfma_f32_16x16x32_bf16 v[48:51], v[228:231], v[212:215], v[48:51]\n"
    ".Lpad14%=:\n"
    "s_cbranch_execz .Lpad14r%=\n"
    "ds_read_b128 v[160:163], v5 offset:4608\n"
    "v_mfma_f32_16x16x32_bf16 v[36:39], v[236:239], v[196:199], v[36:39]\n"
    "v_mfma_f32_16x16x32_bf16 v[40:43], v[236:239], v[124:127], v[40:43]\n"
    "v_mfma_f32_16x16x32_bf16 v[44:47], v[236:239], v[212:215], v[44:47]\n"
    "v_mfma_f32_16x16x32_bf16 v[48:51], v[236:239], v[244:247], v[48:51]\n"
    "ds_read_b128 v[84:87], v5 offset:4736\n"
    "v_mfma_f32_16x16x32_bf16 v[36:39], v[252:255], v[124:127], v[36:39]\n"
    "v_mfma_f32_16x16x32_bf16 v[40:43], v[252:255], v[212:215], v[40:43]\n"
    "v_mfma_f32_16x16x32_bf16 v[44:47], v[252:255], v[244:247], v[44:47]\n"
    "s_waitcnt lgkmcnt(1)\n"
    "v_mfma_f32_16x16x32_bf16 v[48:51], v[252:255], v[132:135], v[48:51]\n"
    "ds_read_b128 v[220:223], v5 offset:4864\n"
    "v_mfma_f32_16x16x32_bf16 v[36:39], v[216:219], v[212:215], v[36:39]\n"
    "ds_read_b128 v[152:155], v5 offset:1088\n"
    "v_mfma_f32_16x16x32_bf16 v[40:43], v[216:219], v[244:247], v[40:43]\n"
    "ds_read_b128 v[148:151], v5 offset:1216\n"
    "v_mfma_f32_16x16x32_bf16 v[44:47], v[216:219], v[132:135], v[44:47]\n"
    "ds_read_b128 v[192:195], v5 offset:1344\n"
    "v_mfma_f32_16x16x32_bf16 v[48:51], v[216:219], v[160:163], v[48:51]\n"
    "ds_read_b128 v[172:175], v5 offset:1472\n"
    "v_mfma_f32_16x16x32_bf16 v[36:39], v[168:171], v[244:247], v[36:39]\n"
    "v_mfma_f32_16x16x32_bf16 v[40:43], v[168:171], v[132:135], v[40:43]\n"
    "v_mfma_f32_16x16x32_bf16 v[44:47], v[168:171], v[160:163], v[44:47]\n"
    "s_waitcnt lgkmcnt(4)\n"
    "v_mfma_f32_16x16x32_bf16 v[48:51], v[168:171], v[84:87], v[48:51]\n"
    "ds_read_b128 v[196:199], v5 offset:1600\n"
    "v_mfma_f32_16x16x32_bf16 v[36:39], v[204:207], v[132:135], v[36:39]\n"
    "v_mfma_f32_16x16x32_bf16 v[40:43], v[204:207], v[160:163], v[40:43]\n"
    "v_mfma_f32_16x16x32_bf16 v[44:47], v[204:207], v[84:87], v[44:47]\n"
    "v_mfma_f32_16x16x32_bf16 v[48:51], v[204:207], v[220:223], v[48:51]\n"
    "ds_read_b128 v[124:127], v5 offset:1728\n"
    "s_waitcnt lgkmcnt(2)\n"
    "v_mfma_f32_16x16x32_bf16 v[52:55], v[96:99], v[152:155], v[52:55]\n"
    "v_mfma_f32_16x16x32_bf16 v[56:59], v[96:99], v[148:151], v[56:59]\n"
    "v_mfma_f32_16x16x32_bf16 v[60:63], v[96:99], v[192:195], v[60:63]\n"
    "v_cvt_pk_bf16_f32 v36, v36, v37\n"
    "v_cvt_pk_bf16_f32 v37, v38, v39\n"
    "v_mfma_f32_16x16x32_bf16 v[64:67], v[96:99], v[172:175], v[64:67]\n"
    "ds_read_b128 v[212:215], v5 offset:1856\n"
    "v_mfma_f32_16x16x32_bf16 v[52:55], v[104:107], v[148:151], v[52:55]\n"
    "v_cvt_pk_bf16_f32 v38, v40, v41\n"
    "v_cvt_pk_bf16_f32 v39, v42, v43\n"
    "v_mfma_f32_16x16x32_bf16 v[56:59], v[104:107], v[192:195], v[56:59]\n"
    "v_min_u32 v36, -2, v36\n"
    "v_min_u32 v37, -2, v37\n"
    "v_mfma_f32_16x16x32_bf16 v[60:63], v[104:107], v[172:175], v[60:63]\n"
    "v_min_u32 v38, -2, v38\n"
    "v_min_u32 v39, -2, v39\n"
    "s_waitcnt lgkmcnt(1)\n"
    "v_mfma_f32_16x16x32_bf16 v[64:67], v[104:107], v[196:199], v[64:67]\n"
    "ds_read_b128 v[244:247], v5 offset:1984\n"
    "v_mfma_f32_16x16x32_bf16 v[52:55], v[108:111], v[192:195], v[52:55]\n"
    "s_xor_b32 s43, s41, 73728\n"
    "buffer_store_dwordx4 v[36:39], v8, s[24:27], s43 offen sc1\n"
    "v_mfma_f32_16x16x32_bf16 v[56:59], v[108:111], v[172:175], v[56:59]\n"
    "v_cvt_pk_bf16_f32 v44, v44, v45\n"
    "v_cvt_pk_bf16_f32 v45, v46, v47\n"
    "v_mfma_f32_16x16x32_bf16 v[60:63], v[108:111], v[196:199], v[60:63]\n"
    "v_cvt_pk_bf16_f32 v46, v48, v49\n"
    "v_cvt_pk_bf16_f32 v47, v50, v51\n"
    "v_mfma_f32_16x16x32_bf16 v[64:67], v[108:111], v[124:127], v[64:67]\n"
    "ds_read_b128 v[132:135], v5 offset:2144\n"
    "v_mfma_f32_16x16x32_bf16 v[52:55], v[180:183], v[172:175], v[52:55]\n"
    "v_min_u32 v44, -2, v44\n"
    "v_min_u32 v45, -2, v45\n"
    "v_mfma_f32_16x16x32_bf16 v[56:59], v[180:183], v[196:199], v[56:59]\n"
    "v_min_u32 v46, -2, v46\n"
    "v_min_u32 v47, -2, v47\n"
    "v_mfma_f32_16x16x32_bf16 v[60:63], v[180:183], v[124:127], v[60:63]\n"
    "s_xor_b32 s43, s41, 110592\n"
    "buffer_store_dwordx4 v[44:47], v8, s[24:27], s43 offen sc1\n"
    "s_waitcnt lgkmcnt(1)\n"
    "v_mfma_f32_16x16x32_bf16 v[64:67], v[180:183], v[212:215], v[64:67]\n"
    "ds_read_b128 v[160:163], v5 offset:2272\n"
    "v_mfma_f32_16x16x32_bf16 v[52:55], v[116:119], v[196:199], v[52:55]\n"
    "v_mfma_f32_16x16x32_bf16 v[56:59], v[116:119], v[124:127], v[56:59]\n"
    "v_mfma_f32_16x16x32_bf16 v[60:63], v[116:119], v[212:215], v[60:63]\n"
    "v_mfma_f32_16x16x32_bf16 v[64:67], v[116:119], v[244:247], v[64:67]\n"
    "ds_read_b128 v[84:87], v5 offset:2400\n"
    "v_mfma_f32_16x16x32_bf16 v[52:55], v[128:131], v[124:127], v[52:55]\n"
    "v_mfma_f32_16x16x32_bf16 v[56:59], v[128:131], v[212:215], v[56:59]\n"
    "v_mfma_f32_16x16x32_bf16 v[60:63], v[128:131], v[244:247], v[60:63]\n"
    "s_waitcnt lgkmcnt(1)\n"
    "v_mfma_f32_16x16x32_bf16 v[64:67], v[128:131], v[132:135], v[64:67]\n"
    "ds_read_b128 v[220:223], v5 offset:2528\n"
    "v_mfma_f32_16x16x32_bf16 v[52:55], v[144:147], v[212:215], v[52:55]\n"
    "v_mfma_f32_16x16x32_bf16 v[56:59], v[144:147], v[244:247], v[56:59]\n"
    "v_mfma_f32_16x16x32_bf16 v[60:63], v[144:147], v[132:135], v[60:63]\n"
    "v_mfma_f32_16x16x32_bf16 v[64:67], v[144:147], v[160:163], v[64:67]\n"
    "ds_read_b128 v[152:155], v5 offset:2656\n"
    "v_mfma_f32_16x16x32_bf16 v[52:55], v[88:91], v[244:247], v[52:55]\n"
    "v_mfma_f32_16x16x32_bf16 v[56:59], v[88:91], v[132:135], v[56:59]\n"
    "v_mfma_f32_16x16x32_bf16 v[60:63], v[88:91], v[160:163], v[60:63]\n"
    "s_waitcnt lgkmcnt(1)\n"
    "v_mfma_f32_16x16x32_bf16 v[64:67], v[88:91], v[84:87], v[64:67]\n"
    "ds_read_b128 v[148:151], v5 offset:2784\n"
    "v_mfma_f32_16x16x32_bf16 v[52:55], v[140:143], v[132:135], v[52:55]\n"
    "v_mfma_f32_16x16x32_bf16 v[56:59], v[140:143], v[160:163], v[56:59]\n"
    "v_mfma_f32_16x16x32_bf16 v[60:63], v[140:143], v[84:87], v[60:63]\n"
    "v_mfma_f32_16x16x32_bf16 v[64:67], v[140:143], v[220:223], v[64:67]\n"
    "ds_read_b128 v[192:195], v5 offset:2912\n"
    "v_mfma_f32_16x16x32_bf16 v[52:55], v[156:159], v[160:163], v[52:55]\n"
    "v_mfma_f32_16x16x32_bf16 v[56:59], v[156:159], v[84:87], v[56:59]\n"
    "v_mfma_f32_16x16x32_bf16 v[60:63], v[156:159], v[220:223], v[60:63]\n"
    "s_waitcnt lgkmcnt(1)\n"
    "v_mfma_f32_16x16x32_bf16 v[64:67], v[156:159], v[152:155], v[64:67]\n"
    "ds_read_b128 v[172:175], v5 offset:3040\n"
    "v_mfma_f32_16x16x32_bf16 v[52:55], v[188:191], v[84:87], v[52:55]\n"
    "v_mfma_f32_16x16x32_bf16 v[56:59], v[188:191], v[220:223], v[56:59]\n"
    "v_mfma_f32_16x16x32_bf16 v[60:63], v[188:191], v[152:155], v[60:63]\n"
    "v_mfma_f32_16x16x32_bf16 v[64:67], v[188:191], v[148:151], v[64:67]\n"
    "ds_read_b128 v[196:199], v5 offset:3168\n"
    "v_mfma_f32_16x16x32_bf16 v[52:55], v[92:95], v[220:223], v[52:55]\n"
    "v_mfma_f32_16x16x32_bf16 v[56:59], v[92:95], v[152:155], v[56:59]\n"
    "v_mfma_f32_16x16x32_bf16 v[60:63], v[92:95], v[148:151], v[60:63]\n"
    "s_waitcnt lgkmcnt(1)\n"
    "v_mfma_f32_16x16x32_bf16 v[64:67], v[92:95], v[192:195], v[64:67]\n"
    "ds_read_b128 v[124:127], v5 offset:3296\n"
    "v_mfma_f32_16x16x32_bf16 v[52:55], v[224:227], v[152:155], v[52:55]\n"
    "v_mfma_f32_16x16x32_bf16 v[56:59], v[224:227], v[148:151], v[56:59]\n"
    "v_mfma_f32_16x16x32_bf16 v[60:63], v[224:227], v[192:195], v[60:63]\n"
    "v_mfma_f32_16x16x32_bf16 v[64:67], v[224:227], v[172:175], v[64:67]\n"
    "ds_read_b128 v[212:215], v5 offset:3424\n"
    "v_mfma_f32_16x16x32_bf16 v[52:55], v[232:235], v[148:151], v[52:55]\n"
    "v_mfma_f32_16x16x32_bf16 v[56:59], v[232:235], v[192:195], v[56:59]\n"
    "v_mfma_f32_16x16x32_bf16 v[60:63], v[232:235], v[172:175], v[60:63]\n"
    "s_waitcnt lgkmcnt(1)\n"
    "v_mfma_f32_16x16x32_bf16 v[64:67], v[232:235], v[196:199], v[64:67]\n"
    "ds_read_b128 v[244:247], v5 offset:3552\n"
    "v_mfma_f32_16x16x32_bf16 v[52:55], v[240:243], v[192:195], v[52:55]\n"
    "v_mfma_f32_16x16x32_bf16 v[56:59], v[240:243], v[172:175], v[56:59]\n"
    "v_mfma_f32_16x16x32_bf16 v[60:63], v[240:243], v[196:199], v[60:63]\n"
    "v_mfma_f32_16x16x32_bf16 v[64:67], v[240:243], v[124:127], v[64:67]\n"
    "ds_read_b128 v[132:135], v5 offset:3680\n"
    "v_mfma_f32_16x16x32_bf16 v[52:55], v[248:251], v[172:175], v[52:55]\n"
    "v_mfma_f32_16x16x32_bf16 v[56:59], v[248:251], v[196:199], v[56:59]\n"
    "v_mfma_f32_16x16x32_bf16 v[60:63], v[248:251], v[124:127], v[60:63]\n"
    "s_waitcnt lgkmcnt(1)\n"
    "v_mfma_f32_16x16x32_bf16 v[64:67], v[248:251], v[212:215], v[64:67]\n"
    "ds_read_b128 v[160:163], v5 offset:3808\n"
    "v_mfma_f32_16x16x32_bf16 v[52:55], v[184:187], v[196:199], v[52:55]\n"
    "v_mfma_f32_16x16x32_bf16 v[56:59], v[184:187], v[124:127], v[56:59]\n"
    "v_mfma_f32_16x16x32_bf16 v[60:63], v[184:187], v[212:215], v[60:63]\n"
    "v_mfma_f32_16x16x32_bf16 v[64:67], v[184:187], v[244:247], v[64:67]\n"
    "ds_read_b128 v[84:87], v5 offset:3936\n"
    "v_mfma_f32_16x16x32_bf16 v[52:55], v[100:103], v[124:127], v[52:55]\n"
    "v_mfma_f32_16x16x32_bf16 v[56:59], v[100:103], v[212:215], v[56:59]\n"
    "v_mfma_f32_16x16x32_bf16 v[60:63], v[100:103], v[244:247], v[60:63]\n"
    "s_waitcnt lgkmcnt(1)\n"
    "v_mfma_f32_16x16x32_bf16 v[64:67], v[100:103], v[132:135], v[64:67]\n"
    "ds_read_b128 v[220:223], v5 offset:4064\n"
    "v_mfma_f32_16x16x32_bf16 v[52:55], v[200:203], v[212:215], v[52:55]\n"
    "v_mfma_f32_16x16x32_bf16 v[56:59], v[200:203], v[244:247], v[56:59]\n"
    "v_mfma_f32_16x16x32_bf16 v[60:63], v[200:203], v[132:135], v[60:63]\n"
    "v_mfma_f32_16x16x32_bf16 v[64:67], v[200:203], v[160:163], v[64:67]\n"
    "ds_read_b128 v[152:155], v5 offset:4224\n"
    "v_mfma_f32_16x16x32_bf16 v[52:55], v[176:179], v[244:247], v[52:55]\n"
    "v_mfma_f32_16x16x32_bf16 v[56:59], v[176:179], v[132:135], v[56:59]\n"
    "v_mfma_f32_16x16x32_bf16 v[60:63], v[176:179], v[160:163], v[60:63]\n"
    "s_waitcnt lgkmcnt(1)\n"
    "v_mfma_f32_16x16x32_bf16 v[64:67], v[176:179], v[84:87], v[64:67]\n"
    "ds_read_b128 v[148:151], v5 offset:4352\n"
    "v_mfma_f32_16x16x32_bf16 v[52:55], v[120:123], v[132:135], v[52:55]\n"
    "v_mfma_f32_16x16x32_bf16 v[56:59], v[120:123], v[160:163], v[56:59]\n"
    "v_mfma_f32_16x16x32_bf16 v[60:63], v[120:123], v[84:87], v[60:63]\n"
    "v_mfma_f32_16x16x32_bf16 v[64:67], v[120:123], v[220:223], v[64:67]\n"
    "ds_read_b128 v[192:195], v5 offset:4480\n"
    "v_mfma_f32_16x16x32_bf16 v[52:55], v[136:139], v[160:163], v[52:55]\n"
    "v_mfma_f32_16x16x32_bf16 v[56:59], v[136:139], v[84:87], v[56:59]\n"
    "v_mfma_f32_16x16x32_bf16 v[60:63], v[136:139], v[220:223], v[60:63]\n"
    "s_waitcnt lgkmcnt(1)\n"
    "v_mfma_f32_16x16x32_bf16 v[64:67], v[136:139], v[152:155], v[64:67]\n"
    "ds_read_b128 v[172:175], v5 offset:4608\n"
    "v_mfma_f32_16x16x32_bf16 v[52:55], v[112:115], v[84:87], v[52:55]\n"
    "v_mfma_f32_16x16x32_bf16 v[56:59], v[112:115], v[220:223], v[56:59]\n"
    "v_mfma_f32_16x16x32_bf16 v[60:63], v[112:115], v[152:155], v[60:63]\n"
    "v_mfma_f32_16x16x32_bf16 v[64:67], v[112:115], v[148:151], v[64:67]\n"
    "ds_read_b128 v[196:199], v5 offset:4736\n"
    "v_mfma_f32_16x16x32_bf16 v[52:55], v[208:211], v[220:223], v[52:55]\n"
    "v_mfma_f32_16x16x32_bf16 v[56:59], v[208:211], v[152:155], v[56:59]\n"
    "v_mfma_f32_16x16x32_bf16 v[60:63], v[208:211], v[148:151], v[60:63]\n"
    "s_waitcnt lgkmcnt(1)\n"
    "v_mfma_f32_16x16x32_bf16 v[64:67], v[208:211], v[192:195], v[64:67]\n"
    "ds_read_b128 v[124:127], v5 offset:4864\n"
    "v_mfma_f32_16x16x32_bf16 v[52:55], v[164:167], v[152:155], v[52:55]\n"
    "v_mfma_f32_16x16x32_bf16 v[56:59], v[164:167], v[148:151], v[56:59]\n"
    "v_mfma_f32_16x16x32_bf16 v[60:63], v[164:167], v[192:195], v[60:63]\n"
    "v_mfma_f32_16x16x32_bf16 v[64:67], v[164:167], v[172:175], v[64:67]\n"
    "ds_read_b128 v[212:215], v5 offset:4992\n"
    "v_mfma_f32_16x16x32_bf16 v[52:55], v[228:231], v[148:151], v[52:55]\n"
    "v_mfma_f32_16x16x32_bf16 v[56:59], v[228:231], v[192:195], v[56:59]\n"
    "v_mfma_f32_16x16x32_bf16 v[60:63], v[228:231], v[172:175], v[60:63]\n"
    "s_waitcnt lgkmcnt(1)\n"
    "v_mfma_f32_16x16x32_bf16 v[64:67], v[228:231], v[196:199], v[64:67]\n"
    "ds_read_b128 v[244:247], v5 offset:5120\n"
    "v_mfma_f32_16x16x32_bf16 v[52:55], v[236:239], v[192:195], v[52:55]\n"
    "v_mfma_f32_16x16x32_bf16 v[56:59], v[236:239], v[172:175], v[56:59]\n"
    "v_mfma_f32_16x16x32_bf16 v[60:63], v[236:239], v[196:199], v[60:63]\n"
    "v_mfma_f32_16x16x32_bf16 v[64:67], v[236:239], v[124:127], v[64:67]\n"
    "ds_read_b128 v[132:135], v5 offset:5248\n"
    "v_mfma_f32_16x16x32_bf16 v[52:55], v[252:255], v[172:175], v[52:55]\n"
    "v_mfma_f32_16x16x32_bf16 v[56:59], v[252:255], v[196:199], v[56:59]\n"
    "v_mfma_f32_16x16x32_bf16 v[60:63], v[252:255], v[124:127], v[60:63]\n"
    "s_cmp_ge_u32 s39, 7\n"
    "s_cbranch_scc1 .Lt7_1%=\n"
    ".Lt6_1%=:\n"
    "s_waitcnt lgkmcnt(1)\n"
    "v_mfma_f32_16x16x32_bf16 v[64:67], v[252:255], v[212:215], v[64:67]\n"
    "ds_read_b128 v[160:163], v5 offset:5376\n"
    "v_mfma_f32_16x16x32_bf16 v[52:55], v[216:219], v[196:199], v[52:55]\n"
    "ds_read_b128 v[84:87], v5 offset:1856\n"
    "v_mfma_f32_16x16x32_bf16 v[56:59], v[216:219], v[124:127], v[56:59]\n"
    "ds_read_b128 v[220:223], v5 offset:1984\n"
    "v_mfma_f32_16x16x32_bf16 v[60:63], v[216:219], v[212:215], v[60:63]\n"
    "v_mfma_f32_16x16x32_bf16 v[64:67], v[216:219], v[244:247], v[64:67]\n"
    "ds_read_b128 v[152:155], v5 offset:2144\n"
    "v_mfma_f32_16x16x32_bf16 v[52:55], v[168:171], v[124:127], v[52:55]\n"
    "v_mfma_f32_16x16x32_bf16 v[56:59], v[168:171], v[212:215], v[56:59]\n"
    "ds_read_b128 v[148:151], v5 offset:2272\n"
    "v_mfma_f32_16x16x32_bf16 v[60:63], v[168:171], v[244:247], v[60:63]\n"
    "s_waitcnt lgkmcnt(4)\n"
    "v_mfma_f32_16x16x32_bf16 v[64:67], v[168:171], v[132:135], v[64:67]\n"
    "ds_read_b128 v[192:195], v5 offset:2400\n"
    "v_mfma_f32_16x16x32_bf16 v[52:55], v[204:207], v[212:215], v[52:55]\n"
    "v_mfma_f32_16x16x32_bf16 v[56:59], v[204:207], v[244:247], v[56:59]\n"
    "ds_read_b128 v[172:175], v5 offset:2528\n"
    "v_mfma_f32_16x16x32_bf16 v[60:63], v[204:207], v[132:135], v[60:63]\n"
    "v_mfma_f32_16x16x32_bf16 v[64:67], v[204:207], v[160:163], v[64:67]\n"
    "ds_read_b128 v[196:199], v5 offset:2656\n"
    "s_waitcnt lgkmcnt(4)\n"
    "v_mfma_f32_16x16x32_bf16 v[76:79], v[96:99], v[84:87], v[76:79]\n"
    "v_mfma_f32_16x16x32_bf16 v[80:83], v[96:99], v[220:223], v[80:83]\n"
    "ds_read_b128 v[124:127], v5 offset:2784\n"
    "v_mfma_f32_16x16x32_bf16 v[76:79], v[104:107], v[220:223], v[76:79]\n"
    "v_cvt_pk_bf16_f32 v52, v52, v53\n"
    "v_cvt_pk_bf16_f32 v53, v54, v55\n"
    "v_mfma_f32_16x16x32_bf16 v[80:83], v[104:107], v[152:155], v[80:83]\n"
    "ds_read_b128 v[212:215], v5 offset:2912\n"
    "v_mfma_f32_16x16x32_bf16 v[76:79], v[108:111], v[152:155], v[76:79]\n"
    "v_cvt_pk_bf16_f32 v54, v56, v57\n"
    "v_cvt_pk_bf16_f32 v55, v58, v59\n"
    "s_waitcnt lgkmcnt(3)\n"
    "v_mfma_f32_16x16x32_bf16 v[80:83], v[108:111], v[148:151], v[80:83]\n"
    "ds_read_b128 v[244:247], v5 offset:3040\n"
    "v_mfma_f32_16x16x32_bf16 v[76:79], v[180:183], v[148:151], v[76:79]\n"
    "v_min_u32 v52, -2, v52\n"
    "v_min_u32 v53, -2, v53\n"
    "v_mfma_f32_16x16x32_bf16 v[80:83], v[180:183], v[192:195], v[80:83]\n"
    "ds_read_b128 v[132:135], v5 offset:3168\n"
    "v_mfma_f32_16x16x32_bf16 v[76:79], v[116:119], v[192:195], v[76:79]\n"
    "v_min_u32 v54, -2, v54\n"
    "v_min_u32 v55, -2, v55\n"
    "v_mfma_f32_16x16x32_bf16 v[80:83], v[116:119], v[172:175], v[80:83]\n"
    "ds_read_b128 v[160:163], v5 offset:3296\n"
    "v_mfma_f32_16x16x32_bf16 v[76:79], v[128:131], v[172:175], v[76:79]\n"
    "s_xor_b32 s43, s41, 147456\n"
    "buffer_store_dwordx4 v[52:55], v8, s[24:27], s43 offen sc1\n"
    "s_waitcnt lgkmcnt(3)\n"
    "v_mfma_f32_16x16x32_bf16 v[80:83], v[128:131], v[196:199], v[80:83]\n"
    "ds_read_b128 v[84:87], v5 offset:3424\n"
    "v_mfma_f32_16x16x32_bf16 v[76:79], v[144:147], v[196:199], v[76:79]\n"
    "v_cvt_pk_bf16_f32 v60, v60, v61\n"
    "v_cvt_pk_bf16_f32 v61, v62, v63\n"
    "v_mfma_f32_16x16x32_bf16 v[80:83], v[144:147], v[124:127], v[80:83]\n"
    "ds_read_b128 v[220:223], v5 offset:3552\n"
    "v_mfma_f32_16x16x32_bf16 v[76:79], v[88:91], v[124:127], v[76:79]\n"
    "v_cvt_pk_bf16_f32 v62, v64, v65\n"
    "v_cvt_pk_bf16_f32 v63, v66, v67\n"
    "v_mfma_f32_16x16x32_bf16 v[80:83], v[88:91], v[212:215], v[80:83]\n"
    "ds_read_b128 v[152:155], v5 offset:3680\n"
    "v_mfma_f32_16x16x32_bf16 v[76:79], v[140:143], v[212:215], v[76:79]\n"
    "v_min_u32 v60, -2, v60\n"
    "v_min_u32 v61, -2, v61\n"
    "s_waitcnt lgkmcnt(3)\n"
    "v_mfma_f32_16x16x32_bf16 v[80:83], v[140:143], v[244:247], v[80:83]\n"
    "ds_read_b128 v[148:151], v5 offset:3808\n"
    "v_mfma_f32_16x16x32_bf16 v[76:79], v[156:159], v[244:247], v[76:79]\n"
    "v_min_u32 v62, -2, v62\n"
    "v_min_u32 v63, -2, v63\n"
    "v_mfma_f32_16x16x32_bf16 v[80:83], v[156:159], v[132:135], v[80:83]\n"
    "ds_read_b128 v[192:195], v5 offset:3936\n"
    "v_mfma_f32_16x16x32_bf16 v[76:79], v[188:191], v[132:135], v[76:79]\n"
    "s_xor_b32 s43, s41, 184320\n"
    "buffer_store_dwordx4 v[60:63], v8, s[24:27], s43 offen sc1\n"
    "v_mfma_f32_16x16x32_bf16 v[80:83], v[188:191], v[160:163], v[80:83]\n"
    "ds_read_b128 v[172:175], v5 offset:4064\n"
    "v_mfma_f32_16x16x32_bf16 v[76:79], v[92:95], v[160:163], v[76:79]\n"
    "s_waitcnt lgkmcnt(3)\n"
    "v_mfma_f32_16x16x32_bf16 v[80:83], v[92:95], v[84:87], v[80:83]\n"
    "ds_read_b128 v[196:199], v5 offset:4224\n"
    "v_mfma_f32_16x16x32_bf16 v[76:79], v[224:227], v[84:87], v[76:79]\n"
    "v_mfma_f32_16x16x32_bf16 v[80:83], v[224:227], v[220:223], v[80:83]\n"
    "ds_read_b128 v[124:127], v5 offset:4352\n"
    "v_mfma_f32_16x16x32_bf16 v[76:79], v[232:235], v[220:223], v[76:79]\n"
    "v_mfma_f32_16x16x32_bf16 v[80:83], v[232:235], v[152:155], v[80:83]\n"
    "ds_read_b128 v[212:215], v5 offset:4480\n"
    "v_mfma_f32_16x16x32_bf16 v[76:79], v[240:243], v[152:155], v[76:79]\n"
    "s_waitcnt lgkmcnt(3)\n"
    "v_mfma_f32_16x16x32_bf16 v[80:83], v[240:243], v[148:151], v[80:83]\n"
    "ds_read_b128 v[244:247], v5 offset:4608\n"
    "v_mfma_f32_16x16x32_bf16 v[76:79], v[248:251], v[148:151], v[76:79]\n"
    "v_mfma_f32_16x16x32_bf16 v[80:83], v[248:251], v[192:195], v[80:83]\n"
    "ds_read_b128 v[132:135], v5 offset:4736\n"
    "v_mfma_f32_16x16x32_bf16 v[76:79], v[184:187], v[192:195], v[76:79]\n"
    "v_mfma_f32_16x16x32_bf16 v[80:83], v[184:187], v[172:175], v[80:83]\n"
    "ds_read_b128 v[160:163], v5 offset:4864\n"
    "v_mfma_f32_16x16x32_bf16 v[76:79], v[100:103], v[172:175], v[76:79]\n"
    "s_waitcnt lgkmcnt(3)\n"
    "v_mfma_f32_16x16x32_bf16 v[80:83], v[100:103], v[196:199], v[80:83]\n"
    "ds_read_b128 v[84:87], v5 offset:4992\n"
    "v_mfma_f32_16x16x32_bf16 v[76:79], v[200:203], v[196:199], v[76:79]\n"
    "v_mfma_f32_16x16x32_bf16 v[80:83], v[200:203], v[124:127], v[80:83]\n"
    "ds_read_b128 v[220:223], v5 offset:5120\n"
    "v_mfma_f32_16x16x32_bf16 v[76:79], v[176:179], v[124:127], v[76:79]\n"
    "v_mfma_f32_16x16x32_bf16 v[80:83], v[176:179], v[212:215], v[80:83]\n"
    "ds_read_b128 v[152:155], v5 offset:5248\n"
    "v_mfma_f32_16x16x32_bf16 v[76:79], v[120:123], v[212:215], v[76:79]\n"
    "s_waitcnt lgkmcnt(3)\n"
    "v_mfma_f32_16x16x32_bf16 v[80:83], v[120:123], v[244:247], v[80:83]\n"
    "ds_read_b128 v[148:151], v5 offset:5376\n"
    "v_mfma_f32_16x16x32_bf16 v[76:79], v[136:139], v[244:247], v[76:79]\n"
    "v_mfma_f32_16x16x32_bf16 v[80:83], v[136:139], v[132:135], v[80:83]\n"
    "ds_read_b128 v[192:195], v5 offset:5504\n"
    "v_mfma_f32_16x16x32_bf16 v[76:79], v[112:115], v[132:135], v[76:79]\n"
    "v_mfma_f32_16x16x32_bf16 v[80:83], v[112:115], v[160:163], v[80:83]\n"
    "ds_read_b128 v[172:175], v5 offset:5632\n"
    "v_mfma_f32_16x16x32_bf16 v[76:79], v[208:211], v[160:163], v[76:79]\n"
    "s_waitcnt lgkmcnt(3)\n"
    "v_mfma_f32_16x16x32_bf16 v[80:83], v[208:211], v[84:87], v[80:83]\n"
    "ds_read_b128 v[196:199], v5 offset:5760\n"
    "v_mfma_f32_16x16x32_bf16 v[76:79], v[164:167], v[84:87], v[76:79]\n"
    "v_mfma_f32_16x16x32_bf16 v[80:83], v[164:167], v[220:223], v[80:83]\n"
    "ds_read_b128 v[124:127], v5 offset:5888\n"
    "v_mfma_f32_16x16x32_bf16 v[76:79], v[228:231], v[220:223], v[76:79]\n"
    "ds_read_b128 v[212:215], v5 offset:1600\n"
    "v_mfma_f32_16x16x32_bf16 v[80:83], v[228:231], v[152:155], v[80:83]\n"
    "ds_read_b128 v[244:247], v5 offset:1728\n"
    "v_mfma_f32_16x16x32_bf16 v[76:79], v[236:239], v[152:155], v[76:79]\n"
    "s_waitcnt lgkmcnt(4)\n"
    "v_mfma_f32_16x16x32_bf16 v[80:83], v[236:239], v[148:151], v[80:83]\n"
    "ds_read_b128 v[132:135], v5 offset:1856\n"
    "v_mfma_f32_16x16x32_bf16 v[76:79], v[252:255], v[148:151], v[76:79]\n"
    "v_mfma_f32_16x16x32_bf16 v[80:83], v[252:255], v[192:195], v[80:83]\n"
    "ds_read_b128 v[160:163], v5 offset:1984\n"
    "v_mfma_f32_16x16x32_bf16 v[76:79], v[216:219], v[192:195], v[76:79]\n"
    "v_mfma_f32_16x16x32_bf16 v[80:83], v[216:219], v[172:175], v[80:83]\n"
    "ds_read_b128 v[84:87], v5 offset:2144\n"
    "v_mfma_f32_16x16x32_bf16 v[76:79], v[168:171], v[172:175], v[76:79]\n"
    "s_waitcnt lgkmcnt(3)\n"
    "v_mfma_f32_16x16x32_bf16 v[80:83], v[168:171], v[196:199], v[80:83]\n"
    "ds_read_b128 v[220:223], v5 offset:2272\n"
    "v_mfma_f32_16x16x32_bf16 v[76:79], v[204:207], v[196:199], v[76:79]\n"
    "v_mfma_f32_16x16x32_bf16 v[80:83], v[204:207], v[124:127], v[80:83]\n"
    "ds_read_b128 v[152:155], v5 offset:2400\n"
    "v_mfma_f32_16x16x32_bf16 v[68:71], v[96:99], v[212:215], v[68:71]\n"
    "v_mfma_f32_16x16x32_bf16 v[72:75], v[96:99], v[244:247], v[72:75]\n"
    "ds_read_b128 v[148:151], v5 offset:2528\n"
    "v_mfma_f32_16x16x32_bf16 v[68:71], v[104:107], v[244:247], v[68:71]\n"
    "s_nop 1\n"
    "v_cvt_pk_bf16_f32 v76, v76, v77\n"
    "v_cvt_pk_bf16_f32 v77, v78, v79\n"
    "v_cvt_pk_bf16_f32 v78, v80, v81\n"
    "v_cvt_pk_bf16_f32 v79, v82, v83\n"
    "s_waitcnt lgkmcnt(3)\n"
    "v_mfma_f32_16x16x32_bf16 v[72:75], v[104:107], v[132:135], v[72:75]\n"
    "ds_read_b128 v[192:195], v5 offset:2656\n"
    "v_min_u32 v76, -2, v76\n"
    "v_min_u32 v77, -2, v77\n"
    "v_mfma_f32_16x16x32_bf16 v[68:71], v[108:111], v[132:135], v[68:71]\n"
    "v_min_u32 v78, -2, v78\n"
    "v_min_u32 v79, -2, v79\n"
    "s_xor_b32 s43, s41, 258048\n"
    "buffer_store_dwordx4 v[76:79], v8, s[24:27], s43 offen sc1\n"
    "v_mfma_f32_16x16x32_bf16 v[72:75], v[108:111], v[160:163], v[72:75]\n"
    "ds_read_b128 v[172:175], v5 offset:2784\n"
    "v_mfma_f32_16x16x32_bf16 v[68:71], v[180:183], v[160:163], v[68:71]\n"
    "v_mfma_f32_16x16x32_bf16 v[72:75], v[180:183], v[84:87], v[72:75]\n"
    "ds_read_b128 v[196:199], v5 offset:2912\n"
    "v_mfma_f32_16x16x32_bf16 v[68:71], v[116:119], v[84:87], v[68:71]\n"
    "s_waitcnt lgkmcnt(3)\n"
    "v_mfma_f32_16x16x32_bf16 v[72:75], v[116:119], v[220:223], v[72:75]\n"
    "ds_read_b128 v[124:127], v5 offset:3040\n"
    "v_mfma_f32_16x16x32_bf16 v[68:71], v[128:131], v[220:223], v[68:71]\n"
    "v_mfma_f32_16x16x32_bf16 v[72:75], v[128:131], v[152:155], v[72:75]\n"
    "ds_read_b128 v[212:215], v5 offset:3168\n"
    "v_mfma_f32_16x16x32_bf16 v[68:71], v[144:147], v[152:155], v[68:71]\n"
    "v_mfma_f32_16x16x32_bf16 v[72:75], v[144:147], v[148:151], v[72:75]\n"
    "ds_read_b128 v[96:99], v5 offset:3296\n"
    "v_mfma_f32_16x16x32_bf16 v[68:71], v[88:91], v[148:151], v[68:71]\n"
    "s_waitcnt lgkmcnt(3)\n"
    "v_mfma_f32_16x16x32_bf16 v[72:75], v[88:91], v[192:195], v[72:75]\n"
    "ds_read_b128 v[244:247], v5 offset:3424\n"
    "v_mfma_f32_16x16x32_bf16 v[68:71], v[140:143], v[192:195], v[68:71]\n"
    "v_mfma_f32_16x16x32_bf16 v[72:75], v[140:143], v[172:175], v[72:75]\n"
    "ds_read_b128 v[104:107], v5 offset:3552\n"
    "v_mfma_f32_16x16x32_bf16 v[68:71], v[156:159], v[172:175], v[68:71]\n"
    "v_mfma_f32_16x16x32_bf16 v[72:75], v[156:159], v[196:199], v[72:75]\n"
    "ds_read_b128 v[132:135], v5 offset:3680\n"
    "v_mfma_f32_16x16x32_bf16 v[68:71], v[188:191], v[196:199], v[68:71]\n"
    "s_waitcnt lgkmcnt(3)\n"
    "v_mfma_f32_16x16x32_bf16 v[72:75], v[188:191], v[124:127], v[72:75]\n"
    "ds_read_b128 v[108:111], v5 offset:3808\n"
    "v_mfma_f32_16x16x32_bf16 v[68:71], v[92:95], v[124:127], v[68:71]\n"
    "v_mfma_f32_16x16x32_bf16 v[72:75], v[92:95], v[212:215], v[72:75]\n"
    "ds_read_b128 v[160:163], v5 offset:3936\n"
    "v_mfma_f32_16x16x32_bf16 v[68:71], v[224:227], v[212:215], v[68:71]\n"
    "v_mfma_f32_16x16x32_bf16 v[72:75], v[224:227], v[96:99], v[72:75]\n"
    "ds_read_b128 v[180:183], v5 offset:4064\n"
    "v_mfma_f32_16x16x32_bf16 v[68:71], v[232:235], v[96:99], v[68:71]\n"
    "s_waitcnt lgkmcnt(3)\n"
    "v_mfma_f32_16x16x32_bf16 v[72:75], v[232:235], v[244:247], v[72:75]\n"
    "ds_read_b128 v[84:87], v5 offset:4224\n"
    "v_mfma_f32_16x16x32_bf16 v[68:71], v[240:243], v[244:247], v[68:71]\n"
    "v_mfma_f32_16x16x32_bf16 v[72:75], v[240:243], v[104:107], v[72:75]\n"
    "ds_read_b128 v[116:119], v5 offset:4352\n"
    "v_mfma_f32_16x16x32_bf16 v[68:71], v[248:251], v[104:107], v[68:71]\n"
    "v_mfma_f32_16x16x32_bf16 v[72:75], v[248:251], v[132:135], v[72:75]\n"
    "ds_read_b128 v[220:223], v5 offset:4480\n"
    "v_mfma_f32_16x16x32_bf16 v[68:71], v[184:187], v[132:135], v[68:71]\n"
    "s_waitcnt lgkmcnt(3)\n"
    "v_mfma_f32_16x16x32_bf16 v[72:75], v[184:187], v[108:111], v[72:75]\n"
    "ds_read_b128 v[128:131], v5 offset:4608\n"
    "v_mfma_f32_16x16x32_bf16 v[68:71], v[100:103], v[108:111], v[68:71]\n"
    "v_mfma_f32_16x16x32_bf16 v[72:75], v[100:103], v[160:163], v[72:75]\n"
    "ds_read_b128 v[152:155], v5 offset:4736\n"
    "v_mfma_f32_16x16x32_bf16 v[68:71], v[200:203], v[160:163], v[68:71]\n"
    "v_mfma_f32_16x16x32_bf16 v[72:75], v[200:203], v[180:183], v[72:75]\n"
    "ds_read_b128 v[144:147], v5 offset:4864\n"
    "v_mfma_f32_16x16x32_bf16 v[68:71], v[176:179], v[180:183], v[68:71]\n"
    "s_waitcnt lgkmcnt(3)\n"
    "v_mfma_f32_16x16x32_bf16 v[72:75], v[176:179], v[84:87], v[72:75]\n"
    "ds_read_b128 v[148:151], v5 offset:4992\n"
    "v_mfma_f32_16x16x32_bf16 v[68:71], v[120:123], v[84:87], v[68:71]\n"
    "v_mfma_f32_16x16x32_bf16 v[72:75], v[120:123], v[116:119], v[72:75]\n"
    "ds_read_b128 v[88:91], v5 offset:5120\n"
    "v_mfma_f32_16x16x32_bf16 v[68:71], v[136:139], v[116:119], v[68:71]\n"
    "v_mfma_f32_16x16x32_bf16 v[72:75], v[136:139], v[220:223], v[72:75]\n"
    "ds_read_b128 v[192:195], v5 offset:5248\n"
    "v_mfma_f32_16x16x32_bf16 v[68:71], v[112:115], v[220:223], v[68:71]\n"
    "s_waitcnt lgkmcnt(3)\n"
    "v_mfma_f32_16x16x32_bf16 v[72:75], v[112:115], v[128:131], v[72:75]\n"
    "ds_read_b128 v[140:143], v5 offset:5376\n"
    "v_mfma_f32_16x16x32_bf16 v[68:71], v[208:211], v[128:131], v[68:71]\n"
    "v_mfma_f32_16x16x32_bf16 v[72:75], v[208:211], v[152:155], v[72:75]\n"
    "ds_read_b128 v[172:175], v5 offset:5504\n"
    "v_mfma_f32_16x16x32_bf16 v[68:71], v[164:167], v[152:155], v[68:71]\n"
    "v_mfma_f32_16x16x32_bf16 v[72:75], v[164:167], v[144:147], v[72:75]\n"
    "ds_read_b128 v[156:159], v5 offset:5632\n"
    "v_mfma_f32_16x16x32_bf16 v[68:71], v[228:231], v[144:147], v[68:71]\n"
    "s_waitcnt lgkmcnt(3)\n"
    "v_mfma_f32_16x16x32_bf16 v[72:75], v[228:231], v[148:151], v[72:75]\n"
    "v_mfma_f32_16x16x32_bf16 v[68:71], v[236:239], v[148:151], v[68:71]\n"
    "v_mfma_f32_16x16x32_bf16 v[72:75], v[236:239], v[88:91], v[72:75]\n"
    "v_mfma_f32_16x16x32_bf16 v[68:71], v[252:255], v[88:91], v[68:71]\n"
    "v_mfma_f32_16x16x32_bf16 v[72:75], v[252:255], v[192:195], v[72:75]\n"
    "v_mfma_f32_16x16x32_bf16 v[68:71], v[216:219], v[192:195], v[68:71]\n"
    "s_waitcnt lgkmcnt(0)\n"
    "v_mfma_f32_16x16x32_bf16 v[72:75], v[216:219], v[140:143], v[72:75]\n"
    "v_mfma_f32_16x16x32_bf16 v[68:71], v[168:171], v[140:143], v[68:71]\n"
    "v_mfma_f32_16x16x32_bf16 v[72:75], v[168:171], v[172:175], v[72:75]\n"
    "v_mfma_f32_16x16x32_bf16 v[68:71], v[204:207], v[172:175], v[68:71]\n"
    "v_mfma_f32_16x16x32_bf16 v[72:75], v[204:207], v[156:159], v[72:75]\n"
    "s_nop 6\n"
    "ds_write_b128 v16, v[68:71]\n"
    "ds_write_b128 v16, v[72:75] offset:1088\n"
    "s_waitcnt lgkmcnt(0)\n"
    "s_barrier\n"
    "ds_read_b128 v[96:99], v14\n"
    "ds_read_b128 v[100:103], v14 offset:2176\n"
    "ds_read_b128 v[104:107], v14 offset:4352\n"
    "s_waitcnt lgkmcnt(1)\n"
    "v_fmac_f32 v100, s56, v96\n"
    "v_fmac_f32 v101, s56, v97\n"
    "v_fmac_f32 v102, s56, v98\n"
    "v_fmac_f32 v103, s56, v99\n"
    "v_exp_f32 v100, v100\n"
    "v_exp_f32 v101, v101\n"
    "v_exp_f32 v102, v102\n"
    "v_exp_f32 v103, v103\n"
    "v_add_f32 v100, 1.0, v100\n"
    "v_add_f32 v101, 1.0, v101\n"
    "v_add_f32 v102, 1.0, v102\n"
    "v_add_f32 v103, 1.0, v103\n"
    "v_rcp_f32 v100, v100\n"
    "v_rcp_f32 v101, v101\n"
    "v_rcp_f32 v102, v102\n"
    "v_rcp_f32 v103, v103\n"
    "v_fma_f32 v100, v100, s58, v17\n"
    "v_fma_f32 v101, v101, s58, v17\n"
    "v_fma_f32 v102, v102, s58, v17\n"
    "v_fma_f32 v103, v103, s58, v17\n"
    "s_waitcnt lgkmcnt(0)\n"
    "v_mul_f32 v100, v100, v104\n"
    "v_mul_f32 v101, v101, v105\n"
    "v_mul_f32 v102, v102, v106\n"
    "v_mul_f32 v103, v103, v107\n"
    "v_cvt_pk_bf16_f32 v96, v100, v101\n"
    "v_cvt_pk_bf16_f32 v97, v102, v103\n"
    "buffer_store_dwordx2 v[96:97], v15, s[28:31], 0 offen\n"
    "s_endpgm\n"
    ".Lt7_1%=:\n"
    ".Lpad15%=:\n"
    "s_cbranch_execz .Lpad15r%=\n"
    "s_waitcnt lgkmcnt(1)\n"
    "v_mfma_f32_16x16x32_bf16 v[64:67], v[252:255], v[212:215], v[64:67]\n"
    "ds_read_b128 v[160:163], v5 offset:5376\n"
    "v_mfma_f32_16x16x32_bf16 v[52:55], v[216:219], v[196:199], v[52:55]\n"
    "ds_read_b128 v[84:87], v5 offset:1600\n"
    "v_mfma_f32_16x16x32_bf16 v[56:59], v[216:219], v[124:127], v[56:59]\n"
    "ds_read_b128 v[220:223], v5 offset:1728\n"
    "v_mfma_f32_16x16x32_bf16 v[60:63], v[216:219], v[212:215], v[60:63]\n"
    "v_mfma_f32_16x16x32_bf16 v[64:67], v[216:219], v[244:247], v[64:67]\n"
    "ds_read_b128 v[152:155], v5 offset:1856\n"
    "v_mfma_f32_16x16x32_bf16 v[52:55], v[168:171], v[124:127], v[52:55]\n"
    "v_mfma_f32_16x16x32_bf16 v[56:59], v[168:171], v[212:215], v[56:59]\n"
    "ds_read_b128 v[148:151], v5 offset:1984\n"
    "v_mfma_f32_16x16x32_bf16 v[60:63], v[168:171], v[244:247], v[60:63]\n"
    "s_waitcnt lgkmcnt(4)\n"
    "v_mfma_f32_16x16x32_bf16 v[64:67], v[168:171], v[132:135], v[64:67]\n"
    "ds_read_b128 v[192:195], v5 offset:2144\n"
    "v_mfma_f32_16x16x32_bf16 v[52:55], v[204:207], v[212:215], v[52:55]\n"
    "v_mfma_f32_16x16x32_bf16 v[56:59], v[204:207], v[244:247], v[56:59]\n"
    "ds_read_b128 v[172:175], v5 offset:2272\n"
    "v_mfma_f32_16x16x32_bf16 v[60:63], v[204:207], v[132:135], v[60:63]\n"
    "v_mfma_f32_16x16x32_bf16 v[64:67], v[204:207], v[160:163], v[64:67]\n"
    "ds_read_b128 v[196:199], v5 offset:2400\n"
    "s_waitcnt lgkmcnt(4)\n"
    "v_mfma_f32_16x16x32_bf16 v[68:71], v[96:99], v[84:87], v[68:71]\n"
    "v_mfma_f32_16x16x32_bf16 v[72:75], v[96:99], v[220:223], v[72:75]\n"
    "ds_read_b128 v[124:127], v5 offset:2528\n"
    "v_mfma_f32_16x16x32_bf16 v[68:71], v[104:107], v[220:223], v[68:71]\n"
    "v_cvt_pk_bf16_f32 v52, v52, v53\n"
    "v_cvt_pk_bf16_f32 v53, v54, v55\n"
    "v_mfma_f32_16x16x32_bf16 v[72:75], v[104:107], v[152:155], v[72:75]\n"
    "ds_read_b128 v[212:215], v5 offset:2656\n"
    "v_mfma_f32_16x16x32_bf16 v[68:71], v[108:111], v[152:155], v[68:71]\n"
    "v_cvt_pk_bf16_f32 v54, v56, v57\n"
    "v_cvt_pk_bf16_f32 v55, v58, v59\n"
    "s_waitcnt lgkmcnt(3)\n"
    "v_mfma_f32_16x16x32_bf16 v[72:75], v[108:111], v[148:151], v[72:75]\n"
    "ds_read_b128 v[244:247], v5 offset:2784\n"
    "v_mfma_f32_16x16x32_bf16 v[68:71], v[180:183], v[148:151], v[68:71]\n"
    "v_min_u32 v52, -2, v52\n"
    "v_min_u32 v53, -2, v53\n"
    "v_mfma_f32_16x16x32_bf16 v[72:75], v[180:183], v[192:195], v[72:75]\n"
    "ds_read_b128 v[132:135], v5 offset:2912\n"
    "v_mfma_f32_16x16x32_bf16 v[68:71], v[116:119], v[192:195], v[68:71]\n"
    "v_min_u32 v54, -2, v54\n"
    "v_min_u32 v55, -2, v55\n"
    "v_mfma_f32_16x16x32_bf16 v[72:75], v[116:119], v[172:175], v[72:75]\n"
    "ds_read_b128 v[160:163], v5 offset:3040\n"
    "v_mfma_f32_16x16x32_bf16 v[68:71], v[128:131], v[172:175], v[68:71]\n"
    "s_xor_b32 s43, s41, 147456\n"
    "buffer_store_dwordx4 v[52:55], v8, s[24:27], s43 offen sc1\n"
    "s_waitcnt lgkmcnt(3)\n"
    "v_mfma_f32_16x16x32_bf16 v[72:75], v[128:131], v[196:199], v[72:75]\n"
    "ds_read_b128 v[84:87], v5 offset:3168\n"
    "v_mfma_f32_16x16x32_bf16 v[68:71], v[144:147], v[196:199], v[68:71]\n"
    "v_cvt_pk_bf16_f32 v60, v60, v61\n"
    "v_cvt_pk_bf16_f32 v61, v62, v63\n"
    "v_mfma_f32_16x16x32_bf16 v[72:75], v[144:147], v[124:127], v[72:75]\n"
    "ds_read_b128 v[220:223], v5 offset:3296\n"
    "v_mfma_f32_16x16x32_bf16 v[68:71], v[88:91], v[124:127], v[68:71]\n"
    ".Lpad16%=:\n"
    "s_cbranch_execz .Lpad16r%=\n"
    "v_cvt_pk_bf16_f32 v62, v64, v65\n"
    "v_cvt_pk_bf16_f32 v63, v66, v67\n"
    "v_mfma_f32_16x16x32_bf16 v[72:75], v[88:91], v[212:215], v[72:75]\n"
    "ds_read_b128 v[152:155], v5 offset:3424\n"
    "v_mfma_f32_16x16x32_bf16 v[68:71], v[140:143], v[212:215], v[68:71]\n"
    "v_min_u32 v60, -2, v60\n"
    "v_min_u32 v61, -2, v61\n"
    "s_waitcnt lgkmcnt(3)\n"
    "v_mfma_f32_16x16x32_bf16 v[72:75], v[140:143], v[244:247], v[72:75]\n"
    "ds_read_b128 v[148:151], v5 offset:3552\n"
    "v_mfma_f32_16x16x32_bf16 v[68:71], v[156:159], v[244:247], v[68:71]\n"
    "v_min_u32 v62, -2, v62\n"
    "v_min_u32 v63, -2, v63\n"
    "v_mfma_f32_16x16x32_bf16 v[72:75], v[156:159], v[132:135], v[72:75]\n"
    "ds_read_b128 v[192:195], v5 offset:3680\n"
    "v_mfma_f32_16x16x32_bf16 v[68:71], v[188:191], v[132:135], v[68:71]\n"
    "s_xor_b32 s43, s41, 184320\n"
    "buffer_store_dwordx4 v[60:63], v8, s[24:27], s43 offen sc1\n"
    "v_mfma_f32_16x16x32_bf16 v[72:75], v[188:191], v[160:163], v[72:75]\n"
    "ds_read_b128 v[172:175], v5 offset:3808\n"
    "v_mfma_f32_16x16x32_bf16 v[68:71], v[92:95], v[160:163], v[68:71]\n"
    "s_waitcnt lgkmcnt(3)\n"
    "v_mfma_f32_16x16x32_bf16 v[72:75], v[92:95], v[84:87], v[72:75]\n"
    "ds_read_b128 v[196:199], v5 offset:3936\n"
    "v_mfma_f32_16x16x32_bf16 v[68:71], v[224:227], v[84:87], v[68:71]\n"
    "v_mfma_f32_16x16x32_bf16 v[72:75], v[224:227], v[220:223], v[72:75]\n"
    "ds_read_b128 v[124:127], v5 offset:4064\n"
    "v_mfma_f32_16x16x32_bf16 v[68:71], v[232:235], v[220:223], v[68:71]\n"
    "v_mfma_f32_16x16x32_bf16 v[72:75], v[232:235], v[152:155], v[72:75]\n"
    "ds_read_b128 v[212:215], v5 offset:4224\n"
    "v_mfma_f32_16x16x32_bf16 v[68:71], v[240:243], v[152:155], v[68:71]\n"
    "s_waitcnt lgkmcnt(3)\n"
    "v_mfma_f32_16x16x32_bf16 v[72:75], v[240:243], v[148:151], v[72:75]\n"
    "ds_read_b128 v[244:247], v5 offset:4352\n"
    "v_mfma_f32_16x16x32_bf16 v[68:71], v[248:251], v[148:151], v[68:71]\n"
    "v_mfma_f32_16x16x32_bf16 v[72:75], v[248:251], v[192:195], v[72:75]\n"
    "ds_read_b128 v[132:135], v5 offset:4480\n"
    "v_mfma_f32_16x16x32_bf16 v[68:71], v[184:187], v[192:195], v[68:71]\n"
    "v_mfma_f32_16x16x32_bf16 v[72:75], v[184:187], v[172:175], v[72:75]\n"
    "ds_read_b128 v[160:163], v5 offset:4608\n"
    "v_mfma_f32_16x16x32_bf16 v[68:71], v[100:103], v[172:175], v[68:71]\n"
    "s_waitcnt lgkmcnt(3)\n"
    "v_mfma_f32_16x16x32_bf16 v[72:75], v[100:103], v[196:199], v[72:75]\n"
    "ds_read_b128 v[84:87], v5 offset:4736\n"
    "v_mfma_f32_16x16x32_bf16 v[68:71], v[200:203], v[196:199], v[68:71]\n"
    "v_mfma_f32_16x16x32_bf16 v[72:75], v[200:203], v[124:127], v[72:75]\n"
    "ds_read_b128 v[220:223], v5 offset:4864\n"
    "v_mfma_f32_16x16x32_bf16 v[68:71], v[176:179], v[124:127], v[68:71]\n"
    "v_mfma_f32_16x16x32_bf16 v[72:75], v[176:179], v[212:215], v[72:75]\n"
    "ds_read_b128 v[152:155], v5 offset:4992\n"
    "v_mfma_f32_16x16x32_bf16 v[68:71], v[120:123], v[212:215], v[68:71]\n"
    "s_waitcnt lgkmcnt(3)\n"
    "v_mfma_f32_16x16x32_bf16 v[72:75], v[120:123], v[244:247], v[72:75]\n"
    "ds_read_b128 v[148:151], v5 offset:5120\n"
    "v_mfma_f32_16x16x32_bf16 v[68:71], v[136:139], v[244:247], v[68:71]\n"
    "v_mfma_f32_16x16x32_bf16 v[72:75], v[136:139], v[132:135], v[72:75]\n"
    "ds_read_b128 v[192:195], v5 offset:5248\n"
    "v_mfma_f32_16x16x32_bf16 v[68:71], v[112:115], v[132:135], v[68:71]\n"
    "v_mfma_f32_16x16x32_bf16 v[72:75], v[112:115], v[160:163], v[72:75]\n"
    "ds_read_b128 v[172:175], v5 offset:5376\n"
    "v_mfma_f32_16x16x32_bf16 v[68:71], v[208:211], v[160:163], v[68:71]\n"
    "s_waitcnt lgkmcnt(3)\n"
    "v_mfma_f32_16x16x32_bf16 v[72:75], v[208:211], v[84:87], v[72:75]\n"
    "ds_read_b128 v[196:199], v5 offset:5504\n"
    "v_mfma_f32_16x16x32_bf16 v[68:71], v[164:167], v[84:87], v[68:71]\n"
    "v_mfma_f32_16x16x32_bf16 v[72:75], v[164:167], v[220:223], v[72:75]\n"
    "ds_read_b128 v[124:127], v5 offset:5632\n"
    "v_mfma_f32_16x16x32_bf16 v[68:71], v[228:231], v[220:223], v[68:71]\n"
    "ds_read_b128 v[212:215], v5 offset:1856\n"
    "v_mfma_f32_16x16x32_bf16 v[72:75], v[228:231], v[152:155], v[72:75]\n"
    "ds_read_b128 v[244:247], v5 offset:1984\n"
    "v_mfma_f32_16x16x32_bf16 v[68:71], v[236:239], v[152:155], v[68:71]\n"
    "s_waitcnt lgkmcnt(4)\n"
    "v_mfma_f32_16x16x32_bf16 v[72:75], v[236:239], v[148:151], v[72:75]\n"
    "ds_read_b128 v[132:135], v5 offset:2144\n"
    "v_mfma_f32_16x16x32_bf16 v[68:71], v[252:255], v[148:151], v[68:71]\n"
    "v_mfma_f32_16x16x32_bf16 v[72:75], v[252:255], v[192:195], v[72:75]\n"
    "ds_read_b128 v[160:163], v5 offset:2272\n"
    "v_mfma_f32_16x16x32_bf16 v[68:71], v[216:219], v[192:195], v[68:71]\n"
    "v_mfma_f32_16x16x32_bf16 v[72:75], v[216:219], v[172:175], v[72:75]\n"
    "ds_read_b128 v[84:87], v5 offset:2400\n"
    "v_mfma_f32_16x16x32_bf16 v[68:71], v[168:171], v[172:175], v[68:71]\n"
    "s_waitcnt lgkmcnt(3)\n"
    "v_mfma_f32_16x16x32_bf16 v[72:75], v[168:171], v[196:199], v[72:75]\n"
    "ds_read_b128 v[220:223], v5 offset:2528\n"
    "v_mfma_f32_16x16x32_bf16 v[68:71], v[204:207], v[196:199], v[68:71]\n"
    "v_mfma_f32_16x16x32_bf16 v[72:75], v[204:207], v[124:127], v[72:75]\n"
    "ds_read_b128 v[152:155], v5 offset:2656\n"
    "v_mfma_f32_16x16x32_bf16 v[76:79], v[96:99], v[212:215], v[76:79]\n"
    "v_mfma_f32_16x16x32_bf16 v[80:83], v[96:99], v[244:247], v[80:83]\n"
    "ds_read_b128 v[148:151], v5 offset:2784\n"
    "v_mfma_f32_16x16x32_bf16 v[76:79], v[104:107], v[244:247], v[76:79]\n"
    "s_nop 1\n"
    "v_cvt_pk_bf16_f32 v68, v68, v69\n"
    "v_cvt_pk_bf16_f32 v69, v70, v71\n"
    "v_cvt_pk_bf16_f32 v70, v72, v73\n"
    "v_cvt_pk_bf16_f32 v71, v74, v75\n"
    "s_waitcnt lgkmcnt(3)\n"
    "v_mfma_f32_16x16x32_bf16 v[80:83], v[104:107], v[132:135], v[80:83]\n"
    "ds_read_b128 v[192:195], v5 offset:2912\n"
    "v_min_u32 v68, -2, v68\n"
    "v_min_u32 v69, -2, v69\n"
    "v_mfma_f32_16x16x32_bf16 v[76:79], v[108:111], v[132:135], v[76:79]\n"
    "v_min_u32 v70, -2, v70\n"
    "v_min_u32 v71, -2, v71\n"
    "s_xor_b32 s43, s41, 221184\n"
    "buffer_store_dwordx4 v[68:71], v8, s[24:27], s43 offen sc1\n"
    "v_mfma_f32_16x16x32_bf16 v[80:83], v[108:111], v[160:163], v[80:83]\n"
    "ds_read_b128 v[172:175], v5 offset:3040\n"
    "v_mfma_f32_16x16x32_bf16 v[76:79], v[180:183], v[160:163], v[76:79]\n"
    "v_mfma_f32_16x16x32_bf16 v[80:83], v[180:183], v[84:87], v[80:83]\n"
    "ds_read_b128 v[196:199], v5 offset:3168\n"
    "v_mfma_f32_16x16x32_bf16 v[76:79], v[116:119], v[84:87], v[76:79]\n"
    "s_waitcnt lgkmcnt(3)\n"
    "v_mfma_f32_16x16x32_bf16 v[80:83], v[116:119], v[220:223], v[80:83]\n"
    "ds_read_b128 v[124:127], v5 offset:3296\n"
    "v_mfma_f32_16x16x32_bf16 v[76:79], v[128:131], v[220:223], v[76:79]\n"
    "v_mfma_f32_16x16x32_bf16 v[80:83], v[128:131], v[152:155], v[80:83]\n"
    "ds_read_b128 v[212:215], v5 offset:3424\n"
    "v_mfma_f32_16x16x32_bf16 v[76:79], v[144:147], v[152:155], v[76:79]\n"
    "v_mfma_f32_16x16x32_bf16 v[80:83], v[144:147], v[148:151], v[80:83]\n"
    "ds_read_b128 v[96:99], v5 offset:3552\n"
    "v_mfma_f32_16x16x32_bf16 v[76:79], v[88:91], v[148:151], v[76:79]\n"
    "s_waitcnt lgkmcnt(3)\n"
    "v_mfma_f32_16x16x32_bf16 v[80:83], v[88:91], v[192:195], v[80:83]\n"
    "ds_read_b128 v[244:247], v5 offset:3680\n"
    "v_mfma_f32_16x16x32_bf16 v[76:79], v[140:143], v[192:195], v[76:79]\n"
    "v_mfma_f32_16x16x32_bf16 v[80:83], v[140:143], v[172:175], v[80:83]\n"
    "ds_read_b128 v[104:107], v5 offset:3808\n"
    "v_mfma_f32_16x16x32_bf16 v[76:79], v[156:159], v[172:175], v[76:79]\n"
    "v_mfma_f32_16x16x32_bf16 v[80:83], v[156:159], v[196:199], v[80:83]\n"
    "ds_read_b128 v[132:135], v5 offset:3936\n"
    "v_mfma_f32_16x16x32_bf16 v[76:79], v[188:191], v[196:199], v[76:79]\n"
    "s_waitcnt lgkmcnt(3)\n"
    "v_mfma_f32_16x16x32_bf16 v[80:83], v[188:191], v[124:127], v[80:83]\n"
    "ds_read_b128 v[108:111], v5 offset:4064\n"
    "v_mfma_f32_16x16x32_bf16 v[76:79], v[92:95], v[124:127], v[76:79]\n"
    "v_mfma_f32_16x16x32_bf16 v[80:83], v[92:95], v[212:215], v[80:83]\n"
    "ds_read_b128 v[160:163], v5 offset:4224\n"
    "v_mfma_f32_16x16x32_bf16 v[76:79], v[224:227], v[212:215], v[76:79]\n"
    "v_mfma_f32_16x16x32_bf16 v[80:83], v[224:227], v[96:99], v[80:83]\n"
    "ds_read_b128 v[180:183], v5 offset:4352\n"
    "v_mfma_f32_16x16x32_bf16 v[76:79], v[232:235], v[96:99], v[76:79]\n"
    "s_waitcnt lgkmcnt(3)\n"
    "v_mfma_f32_16x16x32_bf16 v[80:83], v[232:235], v[244:247], v[80:83]\n"
    "ds_read_b128 v[84:87], v5 offset:4480\n"
    "v_mfma_f32_16x16x32_bf16 v[76:79], v[240:243], v[244:247], v[76:79]\n"
    "v_mfma_f32_16x16x32_bf16 v[80:83], v[240:243], v[104:107], v[80:83]\n"
    "ds_read_b128 v[116:119], v5 offset:4608\n"
    "v_mfma_f32_16x16x32_bf16 v[76:79], v[248:251], v[104:107], v[76:79]\n"
    "v_mfma_f32_16x16x32_bf16 v[80:83], v[248:251], v[132:135], v[80:83]\n"
    "ds_read_b128 v[220:223], v5 offset:4736\n"
    "v_mfma_f32_16x16x32_bf16 v[76:79], v[184:187], v[132:135], v[76:79]\n"
    "s_waitcnt lgkmcnt(3)\n"
    "v_mfma_f32_16x16x32_bf16 v[80:83], v[184:187], v[108:111], v[80:83]\n"
    "ds_read_b128 v[128:131], v5 offset:4864\n"
    "v_mfma_f32_16x16x32_bf16 v[76:79], v[100:103], v[108:111], v[76:79]\n"
    "v_mfma_f32_16x16x32_bf16 v[80:83], v[100:103], v[160:163], v[80:83]\n"
    "ds_read_b128 v[152:155], v5 offset:4992\n"
    "v_mfma_f32_16x16x32_bf16 v[76:79], v[200:203], v[160:163], v[76:79]\n"
    "v_mfma_f32_16x16x32_bf16 v[80:83], v[200:203], v[180:183], v[80:83]\n"
    "ds_read_b128 v[144:147], v5 offset:5120\n"
    "v_mfma_f32_16x16x32_bf16 v[76:79], v[176:179], v[180:183], v[76:79]\n"
    "s_waitcnt lgkmcnt(3)\n"
    "v_mfma_f32_16x16x32_bf16 v[80:83], v[176:179], v[84:87], v[80:83]\n"
    "ds_read_b128 v[148:151], v5 offset:5248\n"
    "v_mfma_f32_16x16x32_bf16 v[76:79], v[120:123], v[84:87], v[76:79]\n"
    "v_mfma_f32_16x16x32_bf16 v[80:83], v[120:123], v[116:119], v[80:83]\n"
    "ds_read_b128 v[88:91], v5 offset:5376\n"
    "v_mfma_f32_16x16x32_bf16 v[76:79], v[136:139], v[116:119], v[76:79]\n"
    "v_mfma_f32_16x16x32_bf16 v[80:83], v[136:139], v[220:223], v[80:83]\n"
    "ds_read_b128 v[192:195], v5 offset:5504\n"
    "v_mfma_f32_16x16x32_bf16 v[76:79], v[112:115], v[220:223], v[76:79]\n"
    "s_waitcnt lgkmcnt(3)\n"
    "v_mfma_f32_16x16x32_bf16 v[80:83], v[112:115], v[128:131], v[80:83]\n"
    "ds_read_b128 v[140:143], v5 offset:5632\n"
    "v_mfma_f32_16x16x32_bf16 v[76:79], v[208:211], v[128:131], v[76:79]\n"
    "v_mfma_f32_16x16x32_bf16 v[80:83], v[208:211], v[152:155], v[80:83]\n"
    "ds_read_b128 v[172:175], v5 offset:5760\n"
    "v_mfma_f32_16x16x32_bf16 v[76:79], v[164:167], v[152:155], v[76:79]\n"
    "v_mfma_f32_16x16x32_bf16 v[80:83], v[164:167], v[144:147], v[80:83]\n"
    "ds_read_b128 v[156:159], v5 offset:5888\n"
    "v_mfma_f32_16x16x32_bf16 v[76:79], v[228:231], v[144:147], v[76:79]\n"
    "s_waitcnt lgkmcnt(3)\n"
    "v_mfma_f32_16x16x32_bf16 v[80:83], v[228:231], v[148:151], v[80:83]\n"
    "v_mfma_f32_16x16x32_bf16 v[76:79], v[236:239], v[148:151], v[76:79]\n"
    "v_mfma_f32_16x16x32_bf16 v[80:83], v[236:239], v[88:91], v[80:83]\n"
    "v_mfma_f32_16x16x32_bf16 v[76:79], v[252:255], v[88:91], v[76:79]\n"
    "v_mfma_f32_16x16x32_bf16 v[80:83], v[252:255], v[192:195], v[80:83]\n"
    "v_mfma_f32_16x16x32_bf16 v[76:79], v[216:219], v[192:195], v[76:79]\n"
    "s_waitcnt lgkmcnt(0)\n"
    "v_mfma_f32_16x16x32_bf16 v[80:83], v[216:219], v[140:143], v[80:83]\n"
    "v_mfma_f32_16x16x32_bf16 v[76:79], v[168:171], v[140:143], v[76:79]\n"
    "v_mfma_f32_16x16x32_bf16 v[80:83], v[168:171], v[172:175], v[80:83]\n"
    "v_mfma_f32_16x16x32_bf16 v[76:79], v[204:207], v[172:175], v[76:79]\n"
    "v_mfma_f32_16x16x32_bf16 v[80:83], v[204:207], v[156:159], v[80:83]\n"
    "s_nop 6\n"
    "ds_write_b128 v16, v[76:79]\n"
    "ds_write_b128 v16, v[80:83] offset:1088\n"
    "s_waitcnt lgkmcnt(0)\n"
    "s_barrier\n"
    "ds_read_b128 v[96:99], v14\n"
    "ds_read_b128 v[100:103], v14 offset:2176\n"
    "ds_read_b128 v[104:107], v14 offset:4352\n"
    "s_waitcnt lgkmcnt(1)\n"
    "v_fmac_f32 v100, s56, v96\n"
    "v_fmac_f32 v101, s56, v97\n"
    "v_fmac_f32 v102, s56, v98\n"
    "v_fmac_f32 v103, s56, v99\n"
    "v_exp_f32 v100, v100\n"
    "v_exp_f32 v101, v101\n"
    "v_exp_f32 v102, v102\n"
    "v_exp_f32 v103, v103\n"
    "v_add_f32 v100, 1.0, v100\n"
    "v_add_f32 v101, 1.0, v101\n"
    "v_add_f32 v102, 1.0, v102\n"
    "v_add_f32 v103, 1.0, v103\n"
    "v_rcp_f32 v100, v100\n"
    "v_rcp_f32 v101, v101\n"
    "v_rcp_f32 v102, v102\n"
    "v_rcp_f32 v103, v103\n"
    "v_fma_f32 v100, v100, s58, v17\n"
    "v_fma_f32 v101, v101, s58, v17\n"
    "v_fma_f32 v102, v102, s58, v17\n"
    "v_fma_f32 v103, v103, s58, v17\n"
    "s_waitcnt lgkmcnt(0)\n"
    "v_mul_f32 v100, v100, v104\n"
    "v_mul_f32 v101, v101, v105\n"
    "v_mul_f32 v102, v102, v106\n"
    "v_mul_f32 v103, v103, v107\n"
    "v_cvt_pk_bf16_f32 v96, v100, v101\n"
    "v_cvt_pk_bf16_f32 v97, v102, v103\n"
    "buffer_store_dwordx2 v[96:97], v15, s[28:31], 0 offen\n"
    "s_endpgm\n"
    ".Lend%=:\n"
    :
    : [karg] "s"(__builtin_amdgcn_kernarg_segment_ptr()), [bid] "s"(blockIdx.x), [tid] "v"(threadIdx.x), [lds] "s"((unsigned)(size_t)smem)
    : "v1", "v2", "v3", "v4", "v5", "v6", "v7", "v8", "v9", "v10", "v11", "v12", "v13", "v14", "v15", "v16", "v17", "v18", "v19", "v20", "v21", "v22", "v23", "v24", "v25", "v26", "v27", "v28", "v29", "v30", "v31", "v32", "v33", "v34", "v35", "v36", "v37", "v38", "v39", "v40", "v41", "v42", "v43", "v44", "v45", "v46", "v47", "v48", "v49", "v50", "v51", "v52", "v53", "v54", "v55", "v56", "v57", "v58", "v59", "v60", "v61", "v62", "v63", "v64", "v65", "v66", "v67", "v68", "v69", "v70", "v71", "v72", "v73", "v74", "v75", "v76", "v77", "v78", "v79", "v80", "v81", "v82", "v83", "v84", "v85", "v86", "v87", "v88", "v89", "v90", "v91", "v92", "v93", "v94", "v95", "v96", "v97", "v98", "v99", "v100", "v101", "v102", "v103", "v104", "v105", "v106", "v107", "v108", "v109", "v110", "v111", "v112", "v113", "v114", "v115", "v116", "v117", "v118", "v119", "v120", "v121", "v122", "v123", "v124", "v125", "v126", "v127", "v128", "v129", "v130", "v131", "v132", "v133", "v134", "v135", "v136", "v137", "v138", "v139", "v140", "v141", "v142", "v143", "v144", "v145", "v146", "v147", "v148", "v149", "v150", "v151", "v152", "v153", "v154", "v155", "v156", "v157", "v158", "v159", "v160", "v161", "v162", "v163", "v164", "v165", "v166", "v167", "v168", "v169", "v170", "v171", "v172", "v173", "v174", "v175", "v176", "v177", "v178", "v179", "v180", "v181", "v182", "v183", "v184", "v185", "v186", "v187", "v188", "v189", "v190", "v191", "v192", "v193", "v194", "v195", "v196", "v197", "v198", "v199", "v200", "v201", "v202", "v203", "v204", "v205", "v206", "v207", "v208", "v209", "v210", "v211", "v212", "v213", "v214", "v215", "v216", "v217", "v218", "v219", "v220", "v221", "v222", "v223", "v224", "v225", "v226", "v227", "v228", "v229", "v230", "v231", "v232", "v233", "v234", "v235", "v236", "v237", "v238", "v239", "v240", "v241", "v242", "v243", "v244", "v245", "v246", "v247", "v248", "v249", "v250", "v251", "v252", "v253", "v254", "v255", "s8", "s9", "s10", "s11", "s12", "s13", "s14", "s15", "s16", "s17", "s18", "s19", "s20", "s21", "s22", "s23", "s24", "s25", "s26", "s27", "s28", "s29", "s30", "s31", "s32", "s33", "s34", "s35", "s36", "s37", "s38", "s39", "s40", "s41", "s42", "s43", "s44", "s45", "s46", "s47", "s48", "s49", "s50", "s51", "s52", "s53", "s54", "s55", "s56", "s57", "s58", "s59", "vcc", "m0", "memory", "scc");
}

extern "C" void kernel_launch(void* const* d_in, const int* in_sizes, int n_in,
                              void* d_out, int out_size, void* d_ws, size_t ws_size,
                              hipStream_t stream) {
  conv_kernel<<<256, 512, 0, stream>>>(d_in[0], d_in[1], d_out, d_ws);
}
```
